```python
import torch

def _softmax(x, axis=-1):
    return torch.softmax(x, dim=axis)
def _gelu(x):
    return torch.nn.functional.gelu(x, approximate="tanh")
def _silu(x):
    return torch.nn.functional.silu(x)
def _sigmoid(x):
    return torch.sigmoid(x)
def _layernorm(x, axis=-1, eps=1e-5):
    m = x.mean(dim=axis, keepdim=True)
    v = x.var(dim=axis, keepdim=True, unbiased=False)
    return (x - m) / torch.sqrt(v + eps)
def _scatter_add(tgt, idx, src, axis=0):
    return tgt.clone().index_add_(axis, idx, src)
def _segment_sum(src, seg, n):
    out = torch.zeros((n,) + src.shape[1:], dtype=src.dtype)
    return out.index_add_(0, seg, src)
def _pad(x, pw):
    flat = tuple(v for pair in reversed(pw) for v in pair)
    return torch.nn.functional.pad(x, flat)

kernel_name = 'recurrent_scan__linear_kernel_hedgehog__glu_gated_swiglu__prompt'

def setup_inputs():

    _w = (lambda a: (lambda t: t.to(torch.bfloat16) if t.is_floating_point() else t)(torch.as_tensor(a)))
    def _A_setup():
        B, S, D, H = 1, 2048, 512, 32
        Dh = D // H
        return {
            "x": _w((lambda *s: torch.randn(*s, dtype=torch.bfloat16) * 0.05)(B, S, D)),
            "wqkv": _w((lambda *s: torch.randn(*s, dtype=torch.bfloat16) * 0.8)(D, 3 * D)),
            "w_phi": _w((lambda *s: torch.randn(*s, dtype=torch.bfloat16) * 0.8)(H, Dh, Dh) * (Dh ** -0.5)),
            "wo": _w((lambda *s: torch.randn(*s, dtype=torch.bfloat16) * 0.8)(D, D)),
        }
    _A_in = _A_setup()

    def _B_setup():
        B, S, D, F = 1, 2048, 512, 1376
        return {
            "x": _w((lambda *s: torch.randn(*s, dtype=torch.bfloat16) * 0.05)(B, S, D)),
            "w1": _w((lambda *s: torch.randn(*s, dtype=torch.bfloat16) * 0.2)(D, F) * (2.0 / D) ** 0.5),
            "w3": _w((lambda *s: torch.randn(*s, dtype=torch.bfloat16) * 0.2)(D, F) * (2.0 / D) ** 0.5),
            "w2": _w((lambda *s: torch.randn(*s, dtype=torch.bfloat16) * 0.2)(F, D) * (2.0 / F) ** 0.5),
        }
    _B_in = _B_setup()

    return {
        "A_x": _A_in["x"],
        "A_wqkv": _A_in["wqkv"],
        "A_w_phi": _A_in["w_phi"],
        "A_wo": _A_in["wo"],
        "B_x": _B_in["x"],
        "B_w1": _B_in["w1"],
        "B_w3": _B_in["w3"],
        "B_w2": _B_in["w2"],
    }

def reference(A_x, A_wqkv, A_w_phi, A_wo, B_x, B_w1, B_w3, B_w2):
    def _A_ref(x, wqkv, w_phi, wo):
        B, S, D = x.shape
        H = 32; Dh = D // H
        qkv = (lambda x, *d: x.permute(*d))((x @ wqkv).reshape(B, S, 3, H, Dh), 2, 0, 3, 1, 4)
        q, k, v = qkv[0], qkv[1], qkv[2]
        pq = torch.einsum("bhsd,hdf->bhsf", q, w_phi)
        pk = torch.einsum("bhsd,hdf->bhsf", k, w_phi)
        phi_q = torch.concatenate([_softmax(pq, axis=-1), _softmax(-pq, axis=-1)], axis=-1)
        phi_k = torch.concatenate([_softmax(pk, axis=-1), _softmax(-pk, axis=-1)], axis=-1)
        kv = torch.einsum("bhsf,bhsd->bhfd", phi_k, v)
        z  = torch.einsum("bhsf->bhf", phi_k)
        num = torch.einsum("bhqf,bhfd->bhqd", phi_q, kv)
        den = torch.einsum("bhqf,bhf->bhq", phi_q, z)[..., None] + 1e-6
        o = num / den
        return (lambda x, *d: x.permute(*d))(o, 0, 2, 1, 3).reshape(B, S, D) @ wo

    def _B_ref(x, w1, w3, w2):
        gate = torch.einsum("bsd,df->bsf", x, w1)
        up = torch.einsum("bsd,df->bsf", x, w3)
        h = _silu(gate) * up
        out = torch.einsum("bsf,fd->bsd", h, w2)
        return out

    x = A_x
    xn = _layernorm(x, axis=-1)
    s = _A_ref(xn, A_wqkv, A_w_phi, A_wo)
    g = _silu(_B_ref(xn, B_w1, B_w3, B_w2))
    return x + s * g
```

```cpp
#include <hip/hip_runtime.h>
#include <hip/hip_bf16.h>
#include <stdint.h>

typedef __attribute__((ext_vector_type(8))) short bf16x8;
typedef __attribute__((ext_vector_type(4))) short s16x4;
typedef __attribute__((ext_vector_type(16))) float f32x16;
typedef __attribute__((ext_vector_type(4))) float f32x4;
typedef __attribute__((ext_vector_type(4))) unsigned u32x4;
typedef __attribute__((ext_vector_type(2))) unsigned u32x2;
typedef unsigned short u16;
#define LAS __attribute__((address_space(3)))
#define DI __device__ __forceinline__
#define MFMA(a, b, c) __builtin_amdgcn_mfma_f32_32x32x16_bf16((a), (b), (c), 0, 0, 0)

constexpr int S_ = 2048, D_ = 512, NH = 32, F_ = 1376;
constexpr int KS_F = F_ / 16;
constexpr int KS_W = 32 + KS_F;
constexpr size_t WS_HB = 0;
constexpr size_t WS_PQ = WS_HB + (size_t)64 * KS_F * 1024;
constexpr size_t WS_WT = WS_PQ + (size_t)64 * 64 * 1024;
constexpr size_t WS_KVP = WS_WT + (size_t)16 * KS_W * 1024;
constexpr size_t WS_KVF = WS_KVP + (size_t)32 * 16 * 4096;
constexpr size_t WS_END = WS_KVF + 65536;

typedef __attribute__((ext_vector_type(2))) __bf16 bf16x2_t;
typedef __attribute__((ext_vector_type(2))) float f32x2;
DI unsigned cvtpk(float lo, float hi) { f32x2 v = {lo, hi}; return __builtin_bit_cast(unsigned, __builtin_convertvector(v, bf16x2_t)); }
DI float bflo(unsigned u) { return __uint_as_float(u << 16); }
DI float bfhi(unsigned u) { return __uint_as_float(u & 0xffff0000u); }
DI float bf16r(float x) { return bflo(cvtpk(x, x)); }
DI void glds16(const void* g, unsigned lds_base) {
  unsigned sv;
  asm volatile("s_mov_b32 %0, m0\n\ts_mov_b32 m0, %2\n\ts_nop 0\n\tglobal_load_lds_dwordx4 %1, off\n\ts_mov_b32 m0, %0"
               : "=&s"(sv) : "v"(g), "s"(lds_base) : "memory");
}
typedef short v4i16_t __attribute__((ext_vector_type(4)));
DI s16x4 trrd(unsigned a) { return __builtin_bit_cast(s16x4, __builtin_amdgcn_ds_read_tr16_b64_v4i16((LAS v4i16_t*)(uintptr_t)a)); }
DI bf16x8 trfrag(unsigned lo, unsigned hi) { s16x4 a = trrd(lo), b = trrd(hi); return __builtin_shufflevector(a, b, 0, 1, 2, 3, 4, 5, 6, 7); }
DI bf16x8 pack8(const float* v) {
  u32x4 p; p[0] = cvtpk(v[0], v[1]); p[1] = cvtpk(v[2], v[3]); p[2] = cvtpk(v[4], v[5]); p[3] = cvtpk(v[6], v[7]);
  return __builtin_bit_cast(bf16x8, p);
}
DI float xhalf(float v) {
  auto rr = __builtin_amdgcn_permlane32_swap(__float_as_uint(v), __float_as_uint(v), false, false);
  unsigned a = rr[0], b = rr[1];
  return __uint_as_float((threadIdx.x & 32) ? a : b);
}
template <int C> DI float dppror(float v) { return __uint_as_float(__builtin_amdgcn_update_dpp(0u, __float_as_uint(v), 0x120 + C, 0xf, 0xf, false)); }
DI float row16sum(float v) { v += dppror<8>(v); v += dppror<4>(v); v += dppror<2>(v); v += dppror<1>(v); return v; }
DI float silu_f(float x) { return x * __builtin_amdgcn_rcpf(1.0f + __builtin_amdgcn_exp2f(-1.4426950408889634f * x)); }
#define BAR() asm volatile("s_waitcnt lgkmcnt(0)\n\ts_barrier" ::: "memory")
#define WAITV0() asm volatile("s_waitcnt vmcnt(0)" ::: "memory")

enum { T_NONE = 0, T_Q = 1, T_K = 2, T_V = 3, T_G = 4, T_U = 5 };

DI void tile_of(int ng, int g, int r, int& type, int& idx) {
  type = T_NONE; idx = 0;
  if (ng < 11) {
    if (g == 0) { if (r == 0) { type = T_K; idx = ng; } else if (r == 1) { type = T_V; idx = ng; } else if (r == 2) { type = T_G; idx = 3 * ng; } else if (r == 3) { type = T_U; idx = 3 * ng; } }
    else { if (r < 4) { type = (r & 1) ? T_U : T_G; idx = 3 * ng + 1 + (r >> 1); } else if (ng == 0) { type = T_Q; idx = 15; } }
  } else {
    int j = ng - 11;
    if (g == 0) { if (r == 0) { type = T_K; idx = ng; } else if (r == 1) { type = T_V; idx = ng; } else if (r == 2) { type = T_G; idx = 33 + 2 * j; } else if (r == 3) { type = T_U; idx = 33 + 2 * j; } else { type = T_Q; idx = 3 * j; } }
    else { if (r < 2) { type = r ? T_U : T_G; idx = 34 + 2 * j; } else if (r < 4) { type = T_Q; idx = 3 * j + r - 1; } }
  }
}

constexpr int SLOT = 32768;
constexpr int LDS_SCR = 4 * SLOT;
constexpr int K1_LDS = 5 * SLOT;

__global__ __launch_bounds__(512, 2) void k1_kernel(const u16* __restrict__ x, const u16* __restrict__ wqkv, const u16* __restrict__ wphi,
                                                    const u16* __restrict__ wo, const u16* __restrict__ w1, const u16* __restrict__ w3,
                                                    const u16* __restrict__ w2, char* __restrict__ ws) {
  __shared__ __attribute__((aligned(16))) char smem[K1_LDS];
  const int tid = threadIdx.x, lane = tid & 63;
  const int wave = __builtin_amdgcn_readfirstlane(tid >> 6), grp = wave >> 2, wr = wave & 3;
  const int b = blockIdx.x, xcd = b & 7, idx = b >> 3;
  const int mt = (xcd & 1) * 8 + (idx & 7), ng = (xcd >> 1) * 4 + (idx >> 3);
  const int r32 = lane & 31, h = lane >> 5;
  const unsigned lds0 = (unsigned)(uintptr_t)smem;

  const int rq = lane >> 4, c16 = lane & 15;
  u32x4 xr[4][4];
#pragma unroll
  for (int i = 0; i < 4; ++i) {
    const int row = mt * 128 + (i >> 1) * 64 + wave * 8 + (i & 1) * 4 + rq;
    const u32x4* p = (const u32x4*)(x + (size_t)row * D_) + c16;
#pragma unroll
    for (int u = 0; u < 4; ++u) xr[i][u] = p[16 * u];
  }
  auto load_tile = [&](int r) {
    int type, ti; tile_of(ng, grp, r, type, ti);
    if (type == T_NONE) return;
    const u16* src; int ld;
    if (type <= T_V) { src = wqkv + (type - 1) * 512 + 32 * ti; ld = 1536; }
    else { src = (type == T_G ? w1 : w3) + 32 * ti; ld = F_; }
    const u16* g = src + (size_t)(wr * 128 + (lane >> 2)) * ld + (lane & 3) * 8;
    const unsigned dst = lds0 + ((2 * r + grp) & 3) * SLOT + wr * 8192;
#pragma unroll
    for (int i = 0; i < 8; ++i) glds16(g + (size_t)i * 16 * ld, dst + i * 1024);
  };
  load_tile(0);
  {
    const int gw = b * 8 + wave;
    if (gw < 16 * KS_W) {
      const int nb = gw / KS_W, ks = gw - nb * KS_W;
      const u16* src = (ks < 32 ? wo + (size_t)ks * 16 * D_ : w2 + (size_t)(ks - 32) * 16 * D_) + nb * 32 + r32 + (size_t)(4 * h) * D_;
      u32x4 v;
#pragma unroll
      for (int j = 0; j < 4; ++j) {
        const int k0 = 8 * (j >> 1) + 2 * (j & 1);
        v[j] = (unsigned)src[(size_t)k0 * D_] | ((unsigned)src[(size_t)(k0 + 1) * D_] << 16);
      }
      *(u32x4*)(ws + WS_WT + (size_t)gw * 1024 + lane * 16) = v;
    }
  }
  bf16x8 xf[32];
  const unsigned stage = lds0 + 2 * SLOT;
#pragma unroll
  for (int half = 0; half < 2; ++half) {
    u32x4 o[2][4];
#pragma unroll
    for (int ii = 0; ii < 2; ++ii) {
      const int i = half * 2 + ii;
      float v[32];
#pragma unroll
      for (int u = 0; u < 4; ++u)
#pragma unroll
        for (int e = 0; e < 4; ++e) { v[u * 8 + 2 * e] = bflo(xr[i][u][e]); v[u * 8 + 2 * e + 1] = bfhi(xr[i][u][e]); }
      float s0 = 0.f, s1 = 0.f;
#pragma unroll
      for (int e = 0; e < 32; e += 2) { s0 += v[e]; s1 += v[e + 1]; }
      const float mean = row16sum(s0 + s1) * (1.0f / 512.0f);
      const float mb = bf16r(mean);
      float q0 = 0.f, q1 = 0.f;
#pragma unroll
      for (int e = 0; e < 32; e += 2) { v[e] -= mb; v[e + 1] -= mb; q0 += v[e] * v[e]; q1 += v[e + 1] * v[e + 1]; }
      const float dm = mean - mb;
      const float var = fmaxf(row16sum(q0 + q1) * (1.0f / 512.0f) - dm * dm, 0.f);
      const float sd = bf16r(sqrtf(bf16r(bf16r(var) + 1e-5f)));
      const float rs = 1.0f / sd;
#pragma unroll
      for (int u = 0; u < 4; ++u)
#pragma unroll
        for (int e = 0; e < 4; ++e) {
          const unsigned d = cvtpk(v[u * 8 + 2 * e], v[u * 8 + 2 * e + 1]);
          o[ii][u][e] = cvtpk(bflo(d) * rs, bfhi(d) * rs);
        }
    }
    if (half) BAR();
#pragma unroll
    for (int ii = 0; ii < 2; ++ii) {
      const int rl = wave * 8 + ii * 4 + rq;
#pragma unroll
      for (int u = 0; u < 4; ++u)
        *(LAS u32x4*)(uintptr_t)(stage + rl * 1024 + (((c16 + 16 * u) ^ (rl & 15)) << 4)) = o[ii][u];
    }
    BAR();
    if ((wr >> 1) == half) {
      const int rl = (wr & 1) * 32 + r32;
      const unsigned rb = stage + rl * 1024; const int t = h ^ (rl & 15);
#pragma unroll
      for (int ks = 0; ks < 32; ++ks)
        xf[ks] = *(const LAS bf16x8*)(uintptr_t)(rb + (((2 * (ks & 7)) ^ t) << 4) + (ks >> 3) * 256);
    }
  }
  WAITV0();
  BAR();
  load_tile(1);

  const int i16 = lane & 15, blk = (lane >> 4) & 1;
  const unsigned troff = (8 * h + (i16 >> 2)) * 64 + 8 * (4 * blk + (i16 & 3));
  const int mblk = mt * 4 + wr;
  float sg[16];
#pragma unroll
  for (int i = 0; i < 16; ++i) sg[i] = 0.f;
  const int nr = (ng == 0 || ng >= 11) ? 5 : 4;

  for (int r = 0; r < nr; ++r) {
    int type, ti; tile_of(ng, grp, r, type, ti);
    if (r >= 1 && r + 1 < nr) load_tile(r + 1);
    u32x4 wf = {0u, 0u, 0u, 0u};
    if (type == T_Q || type == T_K) {
      const u16* wp = wphi + (size_t)(2 * ti + (r32 >> 4)) * 256 + (4 * h) * 16 + (r32 & 15);
#pragma unroll
      for (int j = 0; j < 4; ++j) { const int d0 = 8 * (j >> 1) + 2 * (j & 1); wf[j] = (unsigned)wp[d0 * 16] | ((unsigned)wp[(d0 + 1) * 16] << 16); }
    }
    if (r == 2 && grp == 0 && wr < 2) {
      f32x16 a2;
#pragma unroll
      for (int i = 0; i < 16; ++i) a2[i] = 0.f;
      const unsigned pk = lds0 + LDS_SCR + wr * 8192, vi = lds0 + LDS_SCR + 16384;
      const int rowq = 8 * h + (i16 >> 2);
#pragma unroll
      for (int ks = 0; ks < 8; ++ks) {
        const int r0 = 16 * ks + rowq, r1 = r0 + 4;
        const int ca = 4 * blk + (i16 & 3), cb = 4 * (blk ^ wr) + (i16 & 3);
        bf16x8 af = trfrag(pk + r0 * 64 + 8 * (ca ^ ((r0 >> 1) & 7)), pk + r1 * 64 + 8 * (ca ^ ((r1 >> 1) & 7)));
        bf16x8 bfr = trfrag(vi + r0 * 64 + 8 * (cb ^ ((r0 >> 1) & 7)), vi + r1 * 64 + 8 * (cb ^ ((r1 >> 1) & 7)));
        if (blk) {
#pragma unroll
          for (int j = 0; j < 8; ++j) bfr[j] = (short)0x3f80;
        }
        a2 = MFMA(af, bfr, a2);
      }
      f32x4* dst = (f32x4*)(ws + WS_KVP + ((size_t)(2 * ng + wr) * 16 + mt) * 4096 + lane * 64);
#pragma unroll
      for (int q = 0; q < 4; ++q) { f32x4 t = {a2[4 * q], a2[4 * q + 1], a2[4 * q + 2], a2[4 * q + 3]}; dst[q] = t; }
    }
    f32x16 acc;
#pragma unroll
    for (int i = 0; i < 16; ++i) acc[i] = 0.f;
    if (type != T_NONE) {
      const unsigned sb = lds0 + ((2 * r + grp) & 3) * SLOT + troff;
#pragma unroll
      for (int ks = 0; ks < 32; ++ks) {
        bf16x8 wfr = trfrag(sb + ks * 1024, sb + ks * 1024 + 256);
        acc = MFMA(wfr, xf[ks], acc);
      }
    }
    WAITV0();
    if (type == T_Q || type == T_K) {
      float av[16];
#pragma unroll
      for (int i = 0; i < 16; ++i) av[i] = acc[i];
      const bf16x8 qb0 = pack8(av), qb1 = pack8(av + 8);
      const u32x4 z4 = {0u, 0u, 0u, 0u};
      const bf16x8 a0 = __builtin_bit_cast(bf16x8, (r32 < 16) ? wf : z4), a1 = __builtin_bit_cast(bf16x8, (r32 < 16) ? z4 : wf);
      f32x16 pq;
#pragma unroll
      for (int i = 0; i < 16; ++i) pq[i] = 0.f;
      pq = MFMA(a0, qb0, pq);
      pq = MFMA(a1, qb1, pq);
#pragma unroll
      for (int s = 0; s < 2; ++s) {
        float p[8];
#pragma unroll
        for (int j = 0; j < 8; j += 2) { const unsigned u = cvtpk(pq[8 * s + j], pq[8 * s + j + 1]); p[j] = bflo(u); p[j + 1] = bfhi(u); }
        float mx = p[0], mn = p[0];
#pragma unroll
        for (int j = 1; j < 8; ++j) { mx = fmaxf(mx, p[j]); mn = fminf(mn, p[j]); }
        mx = fmaxf(mx, xhalf(mx)); mn = fminf(mn, xhalf(mn));
        float ep[8], em[8], sp = 0.f, sm = 0.f;
#pragma unroll
        for (int j = 0; j < 8; ++j) {
          ep[j] = __builtin_amdgcn_exp2f((p[j] - mx) * 1.4426950408889634f); sp += ep[j];
          em[j] = __builtin_amdgcn_exp2f((mn - p[j]) * 1.4426950408889634f); sm += em[j];
        }
        sp += xhalf(sp); sm += xhalf(sm);
        const float rp = __builtin_amdgcn_rcpf(sp), rm = __builtin_amdgcn_rcpf(sm);
#pragma unroll
        for (int j = 0; j < 8; ++j) { ep[j] *= rp; em[j] *= rm; }
        const bf16x8 fp = pack8(ep), fm = pack8(em);
        if (type == T_Q) {
          char* dst = ws + WS_PQ + ((size_t)(mblk * 32 + 2 * ti + s) * 2) * 1024 + lane * 16;
          *(bf16x8*)dst = fp; *(bf16x8*)(dst + 1024) = fm;
        } else {
          const int row = wr * 32 + r32; const int sw = (row >> 1) & 7;
          const unsigned pk = lds0 + LDS_SCR + s * 8192 + row * 64;
          const u32x4 up = __builtin_bit_cast(u32x4, fp), um = __builtin_bit_cast(u32x4, fm);
          u32x2 t;
          t[0] = up[0]; t[1] = up[1]; *(LAS u32x2*)(uintptr_t)(pk + 8 * ((h) ^ sw)) = t;
          t[0] = up[2]; t[1] = up[3]; *(LAS u32x2*)(uintptr_t)(pk + 8 * ((2 + h) ^ sw)) = t;
          t[0] = um[0]; t[1] = um[1]; *(LAS u32x2*)(uintptr_t)(pk + 8 * ((4 + h) ^ sw)) = t;
          t[0] = um[2]; t[1] = um[3]; *(LAS u32x2*)(uintptr_t)(pk + 8 * ((6 + h) ^ sw)) = t;
        }
      }
    } else if (type == T_V) {
      const int row = wr * 32 + r32; const int sw = (row >> 1) & 7;
      const unsigned vi = lds0 + LDS_SCR + 16384 + row * 64;
#pragma unroll
      for (int a = 0; a < 4; ++a) {
        u32x2 t; t[0] = cvtpk(acc[4 * a], acc[4 * a + 1]); t[1] = cvtpk(acc[4 * a + 2], acc[4 * a + 3]);
        *(LAS u32x2*)(uintptr_t)(vi + 8 * ((2 * a + h) ^ sw)) = t;
      }
    } else if (type == T_G) {
#pragma unroll
      for (int i = 0; i < 16; i += 2) {
        const unsigned u = cvtpk(acc[i], acc[i + 1]);
        const unsigned w = cvtpk(silu_f(bflo(u)), silu_f(bfhi(u)));
        sg[i] = bflo(w); sg[i + 1] = bfhi(w);
      }
    } else if (type == T_U) {
      float hv[16];
#pragma unroll
      for (int i = 0; i < 16; i += 2) { const unsigned u = cvtpk(acc[i], acc[i + 1]); hv[i] = sg[i] * bflo(u); hv[i + 1] = sg[i + 1] * bfhi(u); }
      char* dst = ws + WS_HB + ((size_t)mblk * KS_F + 2 * ti) * 1024 + lane * 16;
      *(bf16x8*)dst = pack8(hv); *(bf16x8*)(dst + 1024) = pack8(hv + 8);
    }
    BAR();
  }
}

__global__ __launch_bounds__(64) void kr_kernel(char* __restrict__ ws) {
  const int head = blockIdx.x, lane = threadIdx.x;
  float a[16];
#pragma unroll
  for (int i = 0; i < 16; ++i) a[i] = 0.f;
#pragma unroll
  for (int m = 0; m < 16; ++m) {
    const f32x4* p = (const f32x4*)(ws + WS_KVP + ((size_t)head * 16 + m) * 4096 + lane * 64);
#pragma unroll
    for (int q = 0; q < 4; ++q) { const f32x4 t = p[q]; a[4 * q] += t[0]; a[4 * q + 1] += t[1]; a[4 * q + 2] += t[2]; a[4 * q + 3] += t[3]; }
  }
  char* dst = ws + WS_KVF + (size_t)head * 2048 + lane * 16;
  *(bf16x8*)dst = pack8(a); *(bf16x8*)(dst + 1024) = pack8(a + 8);
}

constexpr int XROW = 68;
__global__ __launch_bounds__(256, 1) void k2_kernel(const u16* __restrict__ x, const char* __restrict__ ws, u16* __restrict__ out) {
  __shared__ __attribute__((aligned(16))) float xch[4 * 64 * XROW];
  const int tid = threadIdx.x, lane = tid & 63;
  const int wave = __builtin_amdgcn_readfirstlane(tid >> 6);
  const int b = blockIdx.x, xcd = b & 7, idx = b >> 3;
  const int mt = xcd * 4 + (idx & 3), nt = idx >> 2;
  const int r32 = lane & 31, h = lane >> 5;
  const int erow = mt * 64 + (tid >> 2), ecol = nt * 64 + (tid & 3) * 16;
  const u32x4 xa = *(const u32x4*)(x + (size_t)erow * D_ + ecol), xb = *(const u32x4*)(x + (size_t)erow * D_ + ecol + 8);

  f32x16 acc[2][2];
#pragma unroll
  for (int n = 0; n < 2; ++n)
#pragma unroll
    for (int m = 0; m < 2; ++m)
#pragma unroll
      for (int i = 0; i < 16; ++i) acc[n][m][i] = 0.f;
  const char* wt0 = ws + WS_WT + ((size_t)(2 * nt) * KS_W) * 1024 + lane * 16;
  const char* wt1 = wt0 + (size_t)KS_W * 1024;
  if (wave < 2) {
    constexpr int P = 4;
    const int h0 = wave * 16;
    const char* kvp = ws + WS_KVF + (size_t)h0 * 2048 + lane * 16;
    const char* pq0 = ws + WS_PQ + ((size_t)(2 * mt) * 32 + h0) * 2048 + lane * 16;
    const char* pq1 = pq0 + 65536;
    const char* wa = wt0 + h0 * 1024; const char* wb = wt1 + h0 * 1024;
    bf16x8 bufs[P][8];
    auto ld = [&](int i, bf16x8* d) {
      d[0] = *(const bf16x8*)(kvp + i * 2048); d[1] = *(const bf16x8*)(kvp + i * 2048 + 1024);
      d[2] = *(const bf16x8*)(pq0 + i * 2048); d[3] = *(const bf16x8*)(pq0 + i * 2048 + 1024);
      d[4] = *(const bf16x8*)(pq1 + i * 2048); d[5] = *(const bf16x8*)(pq1 + i * 2048 + 1024);
      d[6] = *(const bf16x8*)(wa + i * 1024); d[7] = *(const bf16x8*)(wb + i * 1024);
    };
#pragma unroll
    for (int i = 0; i < P; ++i) ld(i, bufs[i]);
#pragma unroll
    for (int i = 0; i < 16; ++i) {
      bf16x8* d = bufs[i % P];
      bf16x8 of[2];
#pragma unroll
      for (int m = 0; m < 2; ++m) {
        f32x16 o;
#pragma unroll
        for (int q = 0; q < 16; ++q) o[q] = 0.f;
        o = MFMA(d[0], d[2 + 2 * m], o);
        o = MFMA(d[1], d[3 + 2 * m], o);
        const float den = bf16r(bf16r(o[8]) + 1e-6f);
        const float rd = __builtin_amdgcn_rcpf(den);
        float ov[8];
#pragma unroll
        for (int j = 0; j < 8; j += 2) { const unsigned u = cvtpk(o[j], o[j + 1]); ov[j] = bflo(u) * rd; ov[j + 1] = bfhi(u) * rd; }
        of[m] = pack8(ov);
      }
      const bf16x8 w0 = d[6], w1f = d[7];
      acc[0][0] = MFMA(w0, of[0], acc[0][0]); acc[0][1] = MFMA(w0, of[1], acc[0][1]);
      acc[1][0] = MFMA(w1f, of[0], acc[1][0]); acc[1][1] = MFMA(w1f, of[1], acc[1][1]);
      if (i + P < 16) ld(i + P, bufs[i % P]);
    }
  } else {
    constexpr int P = 8, NK = KS_F / 2;
    const int k0 = (wave - 2) * NK;
    const char* ha = ws + WS_HB + ((size_t)(2 * mt) * KS_F + k0) * 1024 + lane * 16;
    const char* hb = ha + (size_t)KS_F * 1024;
    const char* wa = wt0 + (32 + k0) * 1024; const char* wb = wt1 + (32 + k0) * 1024;
    bf16x8 bufs[P][4];
    auto ld = [&](int i, bf16x8* d) {
      d[0] = *(const bf16x8*)(ha + i * 1024); d[1] = *(const bf16x8*)(hb + i * 1024);
      d[2] = *(const bf16x8*)(wa + i * 1024); d[3] = *(const bf16x8*)(wb + i * 1024);
    };
#pragma unroll
    for (int i = 0; i < P; ++i) ld(i, bufs[i]);
#pragma unroll
    for (int i = 0; i < NK; ++i) {
      bf16x8* d = bufs[i % P];
      acc[0][0] = MFMA(d[2], d[0], acc[0][0]); acc[0][1] = MFMA(d[2], d[1], acc[0][1]);
      acc[1][0] = MFMA(d[3], d[0], acc[1][0]); acc[1][1] = MFMA(d[3], d[1], acc[1][1]);
      if (i + P < NK) ld(i + P, bufs[i % P]);
    }
  }
  float* mine = xch + wave * 64 * XROW;
#pragma unroll
  for (int n = 0; n < 2; ++n)
#pragma unroll
    for (int m = 0; m < 2; ++m)
#pragma unroll
      for (int a = 0; a < 4; ++a) {
        f32x4 t = {acc[n][m][4 * a], acc[n][m][4 * a + 1], acc[n][m][4 * a + 2], acc[n][m][4 * a + 3]};
        *(f32x4*)(mine + (32 * m + r32) * XROW + 32 * n + 8 * a + 4 * h) = t;
      }
  __syncthreads();
  const float* rp = xch + (tid >> 2) * XROW + (tid & 3) * 16;
  u32x4 res[2];
#pragma unroll
  for (int q = 0; q < 4; ++q) {
    const f32x4 s0 = *(const f32x4*)(rp + 4 * q), s1 = *(const f32x4*)(rp + 64 * XROW + 4 * q);
    const f32x4 o0 = *(const f32x4*)(rp + 128 * XROW + 4 * q), o1 = *(const f32x4*)(rp + 192 * XROW + 4 * q);
    float y[4];
#pragma unroll
    for (int e = 0; e < 4; ++e) {
      const float s = bf16r(s0[e] + s1[e]), o = bf16r(o0[e] + o1[e]);
      const float g = bf16r(silu_f(o));
      const float t = bf16r(s * g);
      const unsigned xw = (q < 2 ? xa : xb)[(q & 1) * 2 + (e >> 1)];
      y[e] = ((e & 1) ? bfhi(xw) : bflo(xw)) + t;
    }
    res[q >> 1][(q & 1) * 2] = cvtpk(y[0], y[1]); res[q >> 1][(q & 1) * 2 + 1] = cvtpk(y[2], y[3]);
  }
  *(u32x4*)(out + (size_t)erow * D_ + ecol) = res[0];
  *(u32x4*)(out + (size_t)erow * D_ + ecol + 8) = res[1];
}

extern "C" void kernel_launch(void* const* d_in, const int* in_sizes, int n_in, void* d_out, int out_size, void* d_ws, size_t ws_size,
                              hipStream_t stream) {
  const u16* x = (const u16*)d_in[0];
  const u16* wqkv = (const u16*)d_in[1];
  const u16* wphi = (const u16*)d_in[2];
  const u16* wo = (const u16*)d_in[3];
  const u16* w1 = (const u16*)d_in[5];
  const u16* w3 = (const u16*)d_in[6];
  const u16* w2 = (const u16*)d_in[7];
  char* ws = (char*)d_ws;
  k1_kernel<<<256, 512, 0, stream>>>(x, wqkv, wphi, wo, w1, w3, w2, ws);
  kr_kernel<<<32, 64, 0, stream>>>(ws);
  k2_kernel<<<256, 256, 0, stream>>>(x, ws, (u16*)d_out);
}
```

```cpp
#include <hip/hip_runtime.h>
#include <hip/hip_bf16.h>
#include <stdint.h>

typedef __attribute__((ext_vector_type(8))) short bf16x8;
typedef __attribute__((ext_vector_type(4))) short s16x4;
typedef __attribute__((ext_vector_type(16))) float f32x16;
typedef __attribute__((ext_vector_type(4))) float f32x4;
typedef __attribute__((ext_vector_type(4))) unsigned u32x4;
typedef __attribute__((ext_vector_type(2))) unsigned u32x2;
typedef unsigned short u16;
#define LAS __attribute__((address_space(3)))
#define DI __device__ __forceinline__
#define MFMA(a, b, c) __builtin_amdgcn_mfma_f32_32x32x16_bf16((a), (b), (c), 0, 0, 0)

constexpr int S_ = 2048, D_ = 512, NH = 32, F_ = 1376;
constexpr int KS_F = F_ / 16;
constexpr int KS_W = 32 + KS_F;
constexpr size_t WS_HB = 0;
constexpr size_t WS_PQ = WS_HB + (size_t)64 * KS_F * 1024;
constexpr size_t WS_WT = WS_PQ + (size_t)64 * 64 * 1024;
constexpr size_t WS_KVP = WS_WT + (size_t)16 * KS_W * 1024;
constexpr size_t WS_KVF = WS_KVP + (size_t)32 * 16 * 4096;
constexpr size_t WS_END = WS_KVF + 65536;

typedef __attribute__((ext_vector_type(2))) __bf16 bf16x2_t;
typedef __attribute__((ext_vector_type(2))) float f32x2;
DI unsigned cvtpk(float lo, float hi) { f32x2 v = {lo, hi}; return __builtin_bit_cast(unsigned, __builtin_convertvector(v, bf16x2_t)); }
DI float bflo(unsigned u) { return __uint_as_float(u << 16); }
DI float bfhi(unsigned u) { return __uint_as_float(u & 0xffff0000u); }
DI float bf16r(float x) { return bflo(cvtpk(x, x)); }
DI void glds16(const void* g, unsigned lds_base) {
  unsigned sv;
  asm volatile("s_mov_b32 %0, m0\n\ts_mov_b32 m0, %2\n\ts_nop 0\n\tglobal_load_lds_dwordx4 %1, off\n\ts_mov_b32 m0, %0"
               : "=&s"(sv) : "v"(g), "s"(lds_base) : "memory");
}
typedef short v4i16_t __attribute__((ext_vector_type(4)));
DI s16x4 trrd(unsigned a) { return __builtin_bit_cast(s16x4, __builtin_amdgcn_ds_read_tr16_b64_v4i16((LAS v4i16_t*)(uintptr_t)a)); }
DI bf16x8 trfrag(unsigned lo, unsigned hi) { s16x4 a = trrd(lo), b = trrd(hi); return __builtin_shufflevector(a, b, 0, 1, 2, 3, 4, 5, 6, 7); }
DI bf16x8 pack8(const float* v) {
  u32x4 p; p[0] = cvtpk(v[0], v[1]); p[1] = cvtpk(v[2], v[3]); p[2] = cvtpk(v[4], v[5]); p[3] = cvtpk(v[6], v[7]);
  return __builtin_bit_cast(bf16x8, p);
}
DI float xhalf(float v) {
  auto rr = __builtin_amdgcn_permlane32_swap(__float_as_uint(v), __float_as_uint(v), false, false);
  unsigned a = rr[0], b = rr[1];
  return __uint_as_float((threadIdx.x & 32) ? a : b);
}
template <int C> DI float dppror(float v) { return __uint_as_float(__builtin_amdgcn_update_dpp(0u, __float_as_uint(v), 0x120 + C, 0xf, 0xf, false)); }
DI float row16sum(float v) { v += dppror<8>(v); v += dppror<4>(v); v += dppror<2>(v); v += dppror<1>(v); return v; }
DI float silu_f(float x) { return x * __builtin_amdgcn_rcpf(1.0f + __builtin_amdgcn_exp2f(-1.4426950408889634f * x)); }
#define STAMP(i)
#define STAMPR(i)
#define STAMP_FLUSH(base)
#define BAR() asm volatile("s_waitcnt lgkmcnt(0)\n\ts_barrier" ::: "memory")
#define WAITV0() asm volatile("s_waitcnt vmcnt(0)" ::: "memory")

enum { T_NONE = 0, T_Q = 1, T_K = 2, T_V = 3, T_G = 4, T_U = 5 };
#define TD(ty, ix) ((unsigned long long)(((ty) << 6) | (ix)))
#define SB0() __builtin_amdgcn_sched_barrier(0)
DI float dot2(unsigned a, unsigned b, float c) {
  return __builtin_amdgcn_fdot2_f32_bf16(__builtin_bit_cast(bf16x2_t, a), __builtin_bit_cast(bf16x2_t, b), c, false);
}

constexpr int SLOT = 32768;
constexpr int LDS_SCR = 4 * SLOT;
constexpr int LDS_CS = LDS_SCR + 24576;
constexpr int LDS_ST = LDS_CS + 2048;
constexpr int K1_LDS = 5 * SLOT;

__global__ __launch_bounds__(512, 2) void k1_kernel(const u16* __restrict__ x, const u16* __restrict__ wqkv, const u16* __restrict__ wphi,
                                                    const u16* __restrict__ wo, const u16* __restrict__ w1, const u16* __restrict__ w3,
                                                    const u16* __restrict__ w2, char* __restrict__ ws) {
  __shared__ __attribute__((aligned(16))) char smem[K1_LDS];
  const int tid = threadIdx.x, lane = tid & 63;
  const int wave = __builtin_amdgcn_readfirstlane(tid >> 6), grp = wave >> 2, wr = wave & 3;
  const int b = blockIdx.x, xcd = b & 7, idx = b >> 3;
  const int mt = (xcd & 1) * 8 + (idx & 7), ng = (xcd >> 1) * 4 + (idx >> 3);
  const int r32 = lane & 31, h = lane >> 5;
  const unsigned lds0 = (unsigned)(uintptr_t)smem;

  u32x4 xr[16];
  {
    const u32x4* p = (const u32x4*)(x + (size_t)(mt * 128 + wave * 16) * D_) + lane;
#pragma unroll
    for (int i = 0; i < 16; ++i) xr[i] = p[64 * i];
  }
  unsigned long long tl0, tl1; int nt;
  if (ng < 11) {
    tl0 = TD(T_K, ng) | (TD(T_V, ng) << 9) | (TD(T_G, 3 * ng) << 18) | (TD(T_U, 3 * ng) << 27) | (ng == 0 ? (TD(T_Q, 15) << 36) : 0ull);
    tl1 = TD(T_G, 3 * ng + 1) | (TD(T_U, 3 * ng + 1) << 9) | (TD(T_G, 3 * ng + 2) << 18) | (TD(T_U, 3 * ng + 2) << 27);
    nt = ng == 0 ? 9 : 8;
  } else {
    const int j = ng - 11;
    tl0 = TD(T_K, ng) | (TD(T_V, ng) << 9) | (TD(T_G, 33 + 2 * j) << 18) | (TD(T_U, 33 + 2 * j) << 27) | (TD(T_Q, 3 * j) << 36);
    tl1 = TD(T_G, 34 + 2 * j) | (TD(T_U, 34 + 2 * j) << 9) | (TD(T_Q, 3 * j + 1) << 18) | (TD(T_Q, 3 * j + 2) << 27);
    nt = 9;
  }
  auto tdesc = [&](int t) -> int { return (int)((((t & 1) ? tl1 : tl0) >> (9 * (t >> 1))) & 511); };
  auto tsrc = [&](int d, int& ld) -> const u16* {
    const int type = d >> 6, ti = d & 63;
    if (type <= T_V) { ld = 1536; return wqkv + (type - 1) * 512 + 32 * ti; }
    ld = F_; return (type == T_G ? w1 : w3) + 32 * ti;
  };
  auto load_tile = [&](int t) {
    int ld; const u16* src = tsrc(tdesc(t), ld);
    const u16* g = src + (size_t)(wave * 64 + (lane >> 2)) * ld + (lane & 3) * 8;
    const unsigned dst = lds0 + (t & 3) * SLOT + wave * 4096;
#pragma unroll
    for (int i = 0; i < 4; ++i) glds16(g + (size_t)i * 16 * ld, dst + i * 1024);
  };
  load_tile(0);
  {
    unsigned acc_t = 0;
    for (int t = 1; t < nt; ++t) { int ld; const u16* src = tsrc(tdesc(t), ld); acc_t ^= *(const unsigned*)(src + (size_t)tid * ld); }
    asm volatile("" ::"v"(acc_t));
  }
  {
    const int gw = b * 8 + wave;
    if (gw < 16 * KS_W) {
      const int nb = gw / KS_W, ks = gw - nb * KS_W;
      const u16* src = (ks < 32 ? wo + (size_t)ks * 16 * D_ : w2 + (size_t)(ks - 32) * 16 * D_) + nb * 32 + r32 + (size_t)(4 * h) * D_;
      u32x4 v;
#pragma unroll
      for (int j = 0; j < 4; ++j) {
        const int k0 = 8 * (j >> 1) + 2 * (j & 1);
        v[j] = (unsigned)src[(size_t)k0 * D_] | ((unsigned)src[(size_t)(k0 + 1) * D_] << 16);
      }
      *(u32x4*)(ws + WS_WT + (size_t)gw * 1024 + lane * 16) = v;
    }
  }
  bf16x8 xf[32];
  {
    const unsigned stage = lds0 + SLOT;
#pragma unroll
    for (int i = 0; i < 16; ++i) *(LAS u32x4*)(uintptr_t)(stage + (wave * 16 + i) * 1024 + ((lane ^ i) << 4)) = xr[i];
    WAITV0();
    BAR();
    const int rl = wr * 32 + r32;
    const unsigned rb = stage + rl * 1024; const int t = h ^ (rl & 15);
#pragma unroll
    for (int ks = 0; ks < 32; ++ks)
      xf[ks] = *(const LAS bf16x8*)(uintptr_t)(rb + (((2 * (ks & 7)) ^ t) << 4) + (ks >> 3) * 256);
    BAR();
  }
  STAMP(1);
  load_tile(1); load_tile(2);

  const int i16 = lane & 15, blk = (lane >> 4) & 1;
  const unsigned troff = (8 * h + (i16 >> 2)) * 64 + 8 * (4 * blk + (i16 & 3));
  const int mblk = mt * 4 + wr;
  unsigned sg[8];
#pragma unroll
  for (int i = 0; i < 8; ++i) sg[i] = 0u;
  f32x16 acc;
#pragma unroll
  for (int i = 0; i < 16; ++i) acc[i] = 0.f;
  u32x4 wf = {0u, 0u, 0u, 0u};
  float rs = 0.f; bf16x8 bm;
#pragma unroll
  for (int j = 0; j < 8; ++j) bm[j] = 0;

  for (int p = 0; p <= nt; ++p) {
    const bool ldn = p + 3 < nt;
    if ((p & 1) == grp) {
      if (p < nt) {
        const int d = tdesc(p), type = d >> 6, ti = d & 63;
        if (type == T_Q || type == T_K) {
          const u16* wp = wphi + (size_t)(2 * ti + (r32 >> 4)) * 256 + (4 * h) * 16 + (r32 & 15);
#pragma unroll
          for (int j = 0; j < 4; ++j) { const int d0 = 8 * (j >> 1) + 2 * (j & 1); wf[j] = (unsigned)wp[d0 * 16] | ((unsigned)wp[(d0 + 1) * 16] << 16); }
        }
        if (ldn) load_tile(p + 3);
        const unsigned sb = lds0 + (p & 3) * SLOT + troff, sq = sb + wr * 8192;
        bf16x8 wq[4]; u32x4 cq = {0u, 0u, 0u, 0u}; float cs = 0.f;
#pragma unroll
        for (int ks = 0; ks < 4; ++ks) wq[ks] = trfrag(sb + ks * 1024, sb + ks * 1024 + 256);
#pragma unroll
        for (int i = 0; i < 16; ++i) acc[i] = 0.f;
        SB0();
#pragma unroll
        for (int ks = 0; ks < 32; ++ks) {
          acc = MFMA(wq[ks & 3], xf[ks], acc);
          if (ks + 4 < 32) wq[ks & 3] = trfrag(sb + (ks + 4) * 1024, sb + (ks + 4) * 1024 + 256);
          if ((ks & 3) == 0) cq = __builtin_bit_cast(u32x4, trfrag(sq + (ks >> 2) * 1024, sq + (ks >> 2) * 1024 + 256));
          if ((ks & 3) == 3) { cs = dot2(cq[0], 0x3f803f80u, cs); cs = dot2(cq[1], 0x3f803f80u, cs); cs = dot2(cq[2], 0x3f803f80u, cs); cs = dot2(cq[3], 0x3f803f80u, cs); }
          SB0();
        }
        *(LAS float*)(uintptr_t)(lds0 + LDS_CS + grp * 1024 + lane * 16 + wr * 4) = cs;
      }
      STAMP(2 + 2 * p);
      if (ldn) asm volatile("s_waitcnt vmcnt(4)" ::: "memory"); else WAITV0();
    } else if (p == 0) {
      load_tile(3);
      float s0 = 0.f, s1 = 0.f, q0 = 0.f, q1 = 0.f;
#pragma unroll
      for (int ks = 0; ks < 32; ++ks) {
        const u32x4 v = __builtin_bit_cast(u32x4, xf[ks]);
        s0 = dot2(v[0], 0x3f803f80u, s0); q0 = dot2(v[0], v[0], q0); s1 = dot2(v[1], 0x3f803f80u, s1); q1 = dot2(v[1], v[1], q1);
        s0 = dot2(v[2], 0x3f803f80u, s0); q0 = dot2(v[2], v[2], q0); s1 = dot2(v[3], 0x3f803f80u, s1); q1 = dot2(v[3], v[3], q1);
      }
      float sm = s0 + s1, sq2 = q0 + q1;
      sm += xhalf(sm); sq2 += xhalf(sq2);
      const float mean = sm * (1.0f / 512.0f);
      const float var = fmaxf(sq2 * (1.0f / 512.0f) - mean * mean, 0.f);
      const float sd = bf16r(sqrtf(bf16r(bf16r(var) + 1e-5f)));
      f32x2 st = {bf16r(mean), 1.0f / sd};
      if (h == 0) *(LAS f32x2*)(uintptr_t)(lds0 + LDS_ST + (wr * 32 + r32) * 8) = st;
      STAMP(2);
      asm volatile("s_waitcnt vmcnt(4)" ::: "memory");
    } else {
      const int d = tdesc(p - 1), type = d >> 6, ti = d & 63;
      if (ldn) load_tile(p + 3);
      int ln = lane; asm volatile("" : "+v"(ln));
      if (p <= 2) {
        const f32x2 st = *(const LAS f32x2*)(uintptr_t)(lds0 + LDS_ST + (wr * 32 + (ln & 31)) * 8);
        rs = st[1];
        const short nm = (short)(__float_as_uint(-st[0]) >> 16);
        bm[0] = nm; bm[1] = nm; bm[2] = nm;
      }
      {
        const f32x4 c4 = *(const LAS f32x4*)(uintptr_t)(lds0 + LDS_CS + grp * 1024 + ln * 16);
        const float cs = (c4[0] + c4[1]) + (c4[2] + c4[3]);
        const float c0 = bf16r(cs), r1 = cs - c0, c1 = bf16r(r1), c2 = r1 - c1;
        u32x4 am = {cvtpk(c0, c1), cvtpk(c2, 0.f), 0u, 0u};
        acc = MFMA(__builtin_bit_cast(bf16x8, am), bm, acc);
      }
      float av[16];
#pragma unroll
      for (int i = 0; i < 16; ++i) av[i] = acc[i] * rs;
      if (type == T_Q || type == T_K) {
        const bf16x8 qb0 = pack8(av), qb1 = pack8(av + 8);
        const u32x4 z4 = {0u, 0u, 0u, 0u};
        const bf16x8 a0 = __builtin_bit_cast(bf16x8, (r32 < 16) ? wf : z4), a1 = __builtin_bit_cast(bf16x8, (r32 < 16) ? z4 : wf);
        f32x16 pq;
#pragma unroll
        for (int i = 0; i < 16; ++i) pq[i] = 0.f;
        pq = MFMA(a0, qb0, pq);
        pq = MFMA(a1, qb1, pq);
        bf16x8 fp[2], fm[2];
#pragma unroll
        for (int s = 0; s < 2; ++s) {
          float pv[8];
#pragma unroll
          for (int j = 0; j < 8; j += 2) { const unsigned u = cvtpk(pq[8 * s + j], pq[8 * s + j + 1]); pv[j] = bflo(u); pv[j + 1] = bfhi(u); }
          float mx = pv[0], mn = pv[0];
#pragma unroll
          for (int j = 1; j < 8; ++j) { mx = fmaxf(mx, pv[j]); mn = fminf(mn, pv[j]); }
          mx = fmaxf(mx, xhalf(mx)); mn = fminf(mn, xhalf(mn));
          const float L2E = 1.4426950408889634f, mxl = -mx * L2E, mnl = mn * L2E;
          float ep[8], em[8], sp = 0.f, sm = 0.f;
#pragma unroll
          for (int j = 0; j < 8; ++j) {
            ep[j] = __builtin_amdgcn_exp2f(fmaf(pv[j], L2E, mxl)); sp += ep[j];
            em[j] = __builtin_amdgcn_exp2f(fmaf(pv[j], -L2E, mnl)); sm += em[j];
          }
          sp += xhalf(sp); sm += xhalf(sm);
          const float rp = __builtin_amdgcn_rcpf(sp), rm = __builtin_amdgcn_rcpf(sm);
#pragma unroll
          for (int j = 0; j < 8; ++j) { ep[j] *= rp; em[j] *= rm; }
          fp[s] = pack8(ep); fm[s] = pack8(em);
        }
        if (ldn) asm volatile("s_waitcnt vmcnt(4)" ::: "memory"); else WAITV0();
        if (type == T_Q) {
          char* dst = ws + WS_PQ + ((size_t)(mblk * 32 + 2 * ti) * 2) * 1024 + ln * 16;
          *(bf16x8*)dst = fp[0]; *(bf16x8*)(dst + 1024) = fm[0]; *(bf16x8*)(dst + 2048) = fp[1]; *(bf16x8*)(dst + 3072) = fm[1];
        } else {
          const int row = wr * 32 + (ln & 31), sw = (row >> 1) & 7, hh = ln >> 5;
#pragma unroll
          for (int s = 0; s < 2; ++s) {
            const unsigned pk = lds0 + LDS_SCR + s * 8192 + row * 64;
            const u32x4 up = __builtin_bit_cast(u32x4, fp[s]), um = __builtin_bit_cast(u32x4, fm[s]);
            u32x2 t;
            t[0] = up[0]; t[1] = up[1]; *(LAS u32x2*)(uintptr_t)(pk + 8 * ((hh) ^ sw)) = t;
            t[0] = up[2]; t[1] = up[3]; *(LAS u32x2*)(uintptr_t)(pk + 8 * ((2 + hh) ^ sw)) = t;
            t[0] = um[0]; t[1] = um[1]; *(LAS u32x2*)(uintptr_t)(pk + 8 * ((4 + hh) ^ sw)) = t;
            t[0] = um[2]; t[1] = um[3]; *(LAS u32x2*)(uintptr_t)(pk + 8 * ((6 + hh) ^ sw)) = t;
          }
        }
      } else if (type == T_V) {
        const int row = wr * 32 + (ln & 31), sw = (row >> 1) & 7, hh = ln >> 5;
        const unsigned vi = lds0 + LDS_SCR + 16384 + row * 64;
#pragma unroll
        for (int a = 0; a < 4; ++a) {
          u32x2 t; t[0] = cvtpk(av[4 * a], av[4 * a + 1]); t[1] = cvtpk(av[4 * a + 2], av[4 * a + 3]);
          *(LAS u32x2*)(uintptr_t)(vi + 8 * ((2 * a + hh) ^ sw)) = t;
        }
        if (ldn) asm volatile("s_waitcnt vmcnt(4)" ::: "memory"); else WAITV0();
      } else if (type == T_G) {
#pragma unroll
        for (int i = 0; i < 16; i += 2) {
          const unsigned u = cvtpk(av[i], av[i + 1]);
          sg[i >> 1] = cvtpk(silu_f(bflo(u)), silu_f(bfhi(u)));
        }
        if (ldn) asm volatile("s_waitcnt vmcnt(4)" ::: "memory"); else WAITV0();
      } else {
        float hv[16];
#pragma unroll
        for (int i = 0; i < 16; i += 2) { const unsigned u = cvtpk(av[i], av[i + 1]); hv[i] = bflo(sg[i >> 1]) * bflo(u); hv[i + 1] = bfhi(sg[i >> 1]) * bfhi(u); }
        const bf16x8 h0 = pack8(hv), h1 = pack8(hv + 8);
        if (ldn) asm volatile("s_waitcnt vmcnt(4)" ::: "memory"); else WAITV0();
        char* dst = ws + WS_HB + ((size_t)mblk * KS_F + 2 * ti) * 1024 + ln * 16;
        *(bf16x8*)dst = h0; *(bf16x8*)(dst + 1024) = h1;
        if (p == 4 && wr < 2) {
          f32x16 a2;
#pragma unroll
          for (int i = 0; i < 16; ++i) a2[i] = 0.f;
          const int rowq = 8 * (ln >> 5) + ((ln & 15) >> 2), bk = (ln >> 4) & 1;
          const int ca = 4 * bk + (ln & 3), cb = 4 * (bk ^ wr) + (ln & 3);
          const int sw0 = (rowq >> 1) & 7, sw1 = ((rowq + 4) >> 1) & 7;
          const unsigned pk = lds0 + LDS_SCR + wr * 8192 + rowq * 64, vi = lds0 + LDS_SCR + 16384 + rowq * 64;
          const unsigned pa0 = pk + 8 * (ca ^ sw0), pa1 = pk + 256 + 8 * (ca ^ sw1), pb0 = vi + 8 * (cb ^ sw0), pb1 = vi + 256 + 8 * (cb ^ sw1);
#pragma unroll
          for (int ks = 0; ks < 8; ++ks) {
            bf16x8 af = trfrag(pa0 + ks * 1024, pa1 + ks * 1024);
            bf16x8 bfr = trfrag(pb0 + ks * 1024, pb1 + ks * 1024);
            if (blk) {
#pragma unroll
              for (int j = 0; j < 8; ++j) bfr[j] = (short)0x3f80;
            }
            a2 = MFMA(af, bfr, a2);
          }
          f32x4* dstp = (f32x4*)(ws + WS_KVP + ((size_t)(2 * ng + wr) * 16 + mt) * 4096 + ln * 64);
#pragma unroll
          for (int q = 0; q < 4; ++q) { f32x4 t = {a2[4 * q], a2[4 * q + 1], a2[4 * q + 2], a2[4 * q + 3]}; dstp[q] = t; }
        }
      }
      STAMP(2 + 2 * p);
    }
    if (p < nt) BAR();
    STAMP(3 + 2 * p);
  }
  STAMPR(31);
  STAMP_FLUSH(0);
}

__global__ __launch_bounds__(64) void kr_kernel(char* __restrict__ ws) {
  const int head = blockIdx.x, lane = threadIdx.x;
  float a[16];
#pragma unroll
  for (int i = 0; i < 16; ++i) a[i] = 0.f;
#pragma unroll
  for (int m = 0; m < 16; ++m) {
    const f32x4* p = (const f32x4*)(ws + WS_KVP + ((size_t)head * 16 + m) * 4096 + lane * 64);
#pragma unroll
    for (int q = 0; q < 4; ++q) { const f32x4 t = p[q]; a[4 * q] += t[0]; a[4 * q + 1] += t[1]; a[4 * q + 2] += t[2]; a[4 * q + 3] += t[3]; }
  }
  char* dst = ws + WS_KVF + (size_t)head * 2048 + lane * 16;
  *(bf16x8*)dst = pack8(a); *(bf16x8*)(dst + 1024) = pack8(a + 8);
}

constexpr int XROW = 68;
__global__ __launch_bounds__(256, 1) void k2_kernel(const u16* __restrict__ x, const char* __restrict__ ws, u16* __restrict__ out) {
  __shared__ __attribute__((aligned(16))) float xch[4 * 64 * XROW];
  const int tid = threadIdx.x, lane = tid & 63;
  const int wave = __builtin_amdgcn_readfirstlane(tid >> 6);
  const int b = blockIdx.x, xcd = b & 7, idx = b >> 3;
  const int mt = xcd * 4 + (idx & 3), nt = idx >> 2;
  const int r32 = lane & 31, h = lane >> 5;
  STAMP(0); STAMPR(30);
  const int erow = mt * 64 + (tid >> 2), ecol = nt * 64 + (tid & 3) * 16;
  const u32x4 xa = *(const u32x4*)(x + (size_t)erow * D_ + ecol), xb = *(const u32x4*)(x + (size_t)erow * D_ + ecol + 8);

  f32x16 acc[2][2];
#pragma unroll
  for (int n = 0; n < 2; ++n)
#pragma unroll
    for (int m = 0; m < 2; ++m)
#pragma unroll
      for (int i = 0; i < 16; ++i) acc[n][m][i] = 0.f;
  const char* wt0 = ws + WS_WT + ((size_t)(2 * nt) * KS_W) * 1024 + lane * 16;
  const char* wt1 = wt0 + (size_t)KS_W * 1024;
  if (wave < 2) {
    constexpr int P = 6;
    const int h0 = wave * 16;
    const char* kvp = ws + WS_KVF + (size_t)h0 * 2048 + lane * 16;
    const char* pq0 = ws + WS_PQ + ((size_t)(2 * mt) * 32 + h0) * 2048 + lane * 16;
    const char* pq1 = pq0 + 65536;
    const char* wa = wt0 + h0 * 1024; const char* wb = wt1 + h0 * 1024;
    bf16x8 bufs[P][8];
    auto ld = [&](int i, bf16x8* d) {
      d[0] = *(const bf16x8*)(kvp + i * 2048); d[1] = *(const bf16x8*)(kvp + i * 2048 + 1024);
      d[2] = *(const bf16x8*)(pq0 + i * 2048); d[3] = *(const bf16x8*)(pq0 + i * 2048 + 1024);
      d[4] = *(const bf16x8*)(pq1 + i * 2048); d[5] = *(const bf16x8*)(pq1 + i * 2048 + 1024);
      d[6] = *(const bf16x8*)(wa + i * 1024); d[7] = *(const bf16x8*)(wb + i * 1024);
    };
#pragma unroll
    for (int i = 0; i < P; ++i) ld(i, bufs[i]);
    SB0();
#pragma unroll
    for (int i = 0; i < 16; ++i) {
      bf16x8* d = bufs[i % P];
      bf16x8 of[2];
#pragma unroll
      for (int m = 0; m < 2; ++m) {
        f32x16 o;
#pragma unroll
        for (int q = 0; q < 16; ++q) o[q] = 0.f;
        o = MFMA(d[0], d[2 + 2 * m], o);
        o = MFMA(d[1], d[3 + 2 * m], o);
        const float den = bf16r(bf16r(o[8]) + 1e-6f);
        const float rd = __builtin_amdgcn_rcpf(den);
        float ov[8];
#pragma unroll
        for (int j = 0; j < 8; j += 2) { const unsigned u = cvtpk(o[j], o[j + 1]); ov[j] = bflo(u) * rd; ov[j + 1] = bfhi(u) * rd; }
        of[m] = pack8(ov);
      }
      const bf16x8 w0 = d[6], w1f = d[7];
      acc[0][0] = MFMA(w0, of[0], acc[0][0]); acc[0][1] = MFMA(w0, of[1], acc[0][1]);
      acc[1][0] = MFMA(w1f, of[0], acc[1][0]); acc[1][1] = MFMA(w1f, of[1], acc[1][1]);
      SB0();
      if (i + P < 16) ld(i + P, bufs[i % P]);
      SB0();
    }
  } else {
    constexpr int P = 12, NK = KS_F / 2;
    const int k0 = (wave - 2) * NK;
    const char* ha = ws + WS_HB + ((size_t)(2 * mt) * KS_F + k0) * 1024 + lane * 16;
    const char* hb = ha + (size_t)KS_F * 1024;
    const char* wa = wt0 + (32 + k0) * 1024; const char* wb = wt1 + (32 + k0) * 1024;
    bf16x8 bufs[P][4];
    auto ld = [&](int i, bf16x8* d) {
      d[0] = *(const bf16x8*)(ha + i * 1024); d[1] = *(const bf16x8*)(hb + i * 1024);
      d[2] = *(const bf16x8*)(wa + i * 1024); d[3] = *(const bf16x8*)(wb + i * 1024);
    };
#pragma unroll
    for (int i = 0; i < P; ++i) ld(i, bufs[i]);
    SB0();
#pragma unroll
    for (int i = 0; i < NK; ++i) {
      bf16x8* d = bufs[i % P];
      acc[0][0] = MFMA(d[2], d[0], acc[0][0]); acc[0][1] = MFMA(d[2], d[1], acc[0][1]);
      acc[1][0] = MFMA(d[3], d[0], acc[1][0]); acc[1][1] = MFMA(d[3], d[1], acc[1][1]);
      SB0();
      if (i + P < NK) ld(i + P, bufs[i % P]);
      SB0();
    }
  }
  STAMP(1);
  float* mine = xch + wave * 64 * XROW;
#pragma unroll
  for (int n = 0; n < 2; ++n)
#pragma unroll
    for (int m = 0; m < 2; ++m)
#pragma unroll
      for (int a = 0; a < 4; ++a) {
        f32x4 t = {acc[n][m][4 * a], acc[n][m][4 * a + 1], acc[n][m][4 * a + 2], acc[n][m][4 * a + 3]};
        *(f32x4*)(mine + (32 * m + r32) * XROW + 32 * n + 8 * a + 4 * h) = t;
      }
  __syncthreads();
  STAMP(2);
  const float* rp = xch + (tid >> 2) * XROW + (tid & 3) * 16;
  u32x4 res[2];
#pragma unroll
  for (int q = 0; q < 4; ++q) {
    const f32x4 s0 = *(const f32x4*)(rp + 4 * q), s1 = *(const f32x4*)(rp + 64 * XROW + 4 * q);
    const f32x4 o0 = *(const f32x4*)(rp + 128 * XROW + 4 * q), o1 = *(const f32x4*)(rp + 192 * XROW + 4 * q);
    float y[4];
#pragma unroll
    for (int e = 0; e < 4; ++e) {
      const float s = bf16r(s0[e] + s1[e]), o = bf16r(o0[e] + o1[e]);
      const float g = bf16r(silu_f(o));
      const float t = bf16r(s * g);
      const unsigned xw = (q < 2 ? xa : xb)[(q & 1) * 2 + (e >> 1)];
      y[e] = ((e & 1) ? bfhi(xw) : bflo(xw)) + t;
    }
    res[q >> 1][(q & 1) * 2] = cvtpk(y[0], y[1]); res[q >> 1][(q & 1) * 2 + 1] = cvtpk(y[2], y[3]);
  }
  *(u32x4*)(out + (size_t)erow * D_ + ecol) = res[0];
  *(u32x4*)(out + (size_t)erow * D_ + ecol + 8) = res[1];
  STAMP(3); STAMPR(31);
}

extern "C" void kernel_launch(void* const* d_in, const int* in_sizes, int n_in, void* d_out, int out_size, void* d_ws, size_t ws_size,
                              hipStream_t stream) {
  const u16* x = (const u16*)d_in[0];
  const u16* wqkv = (const u16*)d_in[1];
  const u16* wphi = (const u16*)d_in[2];
  const u16* wo = (const u16*)d_in[3];
  const u16* w1 = (const u16*)d_in[5];
  const u16* w3 = (const u16*)d_in[6];
  const u16* w2 = (const u16*)d_in[7];
  char* ws = (char*)d_ws;
  k1_kernel<<<256, 512, 0, stream>>>(x, wqkv, wphi, wo, w1, w3, w2, ws);
  kr_kernel<<<32, 64, 0, stream>>>(ws);
  k2_kernel<<<256, 256, 0, stream>>>(x, ws, (u16*)d_out);
}
```

```cpp
#include <hip/hip_runtime.h>
#include <hip/hip_bf16.h>
#include <stdint.h>

typedef __attribute__((ext_vector_type(8))) short bf16x8;
typedef __attribute__((ext_vector_type(4))) short s16x4;
typedef __attribute__((ext_vector_type(16))) float f32x16;
typedef __attribute__((ext_vector_type(4))) float f32x4;
typedef __attribute__((ext_vector_type(4))) unsigned u32x4;
typedef __attribute__((ext_vector_type(2))) unsigned u32x2;
typedef unsigned short u16;
#define LAS __attribute__((address_space(3)))
#define DI __device__ __forceinline__
#define MFMA(a, b, c) __builtin_amdgcn_mfma_f32_32x32x16_bf16((a), (b), (c), 0, 0, 0)

constexpr int S_ = 2048, D_ = 512, NH = 32, F_ = 1376;
constexpr int KS_F = F_ / 16;
constexpr int KS_W = 32 + KS_F;
constexpr size_t WS_AB = 0;
constexpr size_t WS_WT = WS_AB + (size_t)64 * KS_W * 1024;
constexpr size_t WS_P1 = WS_WT + (size_t)16 * KS_W * 1024;
constexpr size_t WS_P2 = WS_P1 + (size_t)16 * 16 * 16 * 1024;
constexpr size_t WS_EP = WS_P2 + (size_t)16 * 16 * 512;
constexpr size_t WS_END = WS_EP + 16 * 128;

typedef __attribute__((ext_vector_type(2))) __bf16 bf16x2_t;
typedef __attribute__((ext_vector_type(2))) float f32x2;
DI unsigned cvtpk(float lo, float hi) { f32x2 v = {lo, hi}; return __builtin_bit_cast(unsigned, __builtin_convertvector(v, bf16x2_t)); }
DI float bflo(unsigned u) { return __uint_as_float(u << 16); }
DI float bfhi(unsigned u) { return __uint_as_float(u & 0xffff0000u); }
DI float bf16r(float x) { return bflo(cvtpk(x, x)); }
DI void glds16(const void* g, unsigned lds_base) {
  unsigned sv;
  asm volatile("s_mov_b32 %0, m0\n\ts_mov_b32 m0, %2\n\ts_nop 0\n\tglobal_load_lds_dwordx4 %1, off\n\ts_mov_b32 m0, %0"
               : "=&s"(sv) : "v"(g), "s"(lds_base) : "memory");
}
typedef short v4i16_t __attribute__((ext_vector_type(4)));
DI s16x4 trrd(unsigned a) { return __builtin_bit_cast(s16x4, __builtin_amdgcn_ds_read_tr16_b64_v4i16((LAS v4i16_t*)(uintptr_t)a)); }
DI bf16x8 trfrag(unsigned lo, unsigned hi) { s16x4 a = trrd(lo), b = trrd(hi); return __builtin_shufflevector(a, b, 0, 1, 2, 3, 4, 5, 6, 7); }
DI bf16x8 pack8(const float* v) {
  u32x4 p; p[0] = cvtpk(v[0], v[1]); p[1] = cvtpk(v[2], v[3]); p[2] = cvtpk(v[4], v[5]); p[3] = cvtpk(v[6], v[7]);
  return __builtin_bit_cast(bf16x8, p);
}
DI float xhalf(float v) {
  auto rr = __builtin_amdgcn_permlane32_swap(__float_as_uint(v), __float_as_uint(v), false, false);
  unsigned a = rr[0], b = rr[1];
  return __uint_as_float((threadIdx.x & 32) ? a : b);
}
template <int C> DI float dppror(float v) { return __uint_as_float(__builtin_amdgcn_update_dpp(0u, __float_as_uint(v), 0x120 + C, 0xf, 0xf, false)); }
DI float row16sum(float v) { v += dppror<8>(v); v += dppror<4>(v); v += dppror<2>(v); v += dppror<1>(v); return v; }
DI float silu_f(float x) { return x * __builtin_amdgcn_rcpf(1.0f + __builtin_amdgcn_exp2f(-1.4426950408889634f * x)); }
#define STAMP(i)
#define STAMPR(i)
#define STAMP_FLUSH(base)
#define BAR() asm volatile("s_waitcnt lgkmcnt(0)\n\ts_barrier" ::: "memory")
#define WAITV0() asm volatile("s_waitcnt vmcnt(0)" ::: "memory")

enum { T_NONE = 0, T_Q = 1, T_K = 2, T_V = 3, T_G = 4, T_U = 5 };
#define TD(ty, ix) ((unsigned long long)(((ty) << 6) | (ix)))
#define SB0() __builtin_amdgcn_sched_barrier(0)
#define GAS __attribute__((address_space(1)))
typedef unsigned long long u64;
DI float dot2(unsigned a, unsigned b, float c) {
  return __builtin_amdgcn_fdot2_f32_bf16(__builtin_bit_cast(bf16x2_t, a), __builtin_bit_cast(bf16x2_t, b), c, false);
}
DI void st_gran(void* p, unsigned val, unsigned tag) { __hip_atomic_store((GAS u64*)p, ((u64)tag << 32) | val, __ATOMIC_RELAXED, __HIP_MEMORY_SCOPE_AGENT); }
DI u64 ld_gran(const void* p) { return __hip_atomic_load((GAS u64*)p, __ATOMIC_RELAXED, __HIP_MEMORY_SCOPE_AGENT); }
constexpr unsigned SPIN_MAX = 1u << 16;

constexpr int SLOT = 32768;
constexpr int LDS_SCR = 4 * SLOT;
constexpr int LDS_KV = LDS_SCR + 24576;
constexpr int LDS_DBG = LDS_KV + 4096;
constexpr int K1_LDS = 5 * SLOT;
constexpr int NT = 9;

__global__ __launch_bounds__(512, 2) void k1_kernel(const u16* __restrict__ x, const u16* __restrict__ wqkv, const u16* __restrict__ wphi,
                                                    const u16* __restrict__ wo, const u16* __restrict__ w1, const u16* __restrict__ w3,
                                                    const u16* __restrict__ w2, char* __restrict__ ws) {
  __shared__ __attribute__((aligned(16))) char smem[K1_LDS];
  const int tid = threadIdx.x, lane = tid & 63;
  const int wave = __builtin_amdgcn_readfirstlane(tid >> 6), grp = wave >> 2, wr = wave & 3;
  const int b = blockIdx.x, xcd = b & 7, idx = b >> 3;
  const int mt = (xcd & 1) * 8 + (idx & 7), ng = (xcd >> 1) * 4 + (idx >> 3);
  const int r32 = lane & 31, h = lane >> 5;
  const unsigned lds0 = (unsigned)(uintptr_t)smem;

  const int rq = lane >> 4, c16 = lane & 15;
  u32x4 xr[4][4];
#pragma unroll
  for (int i = 0; i < 4; ++i) {
    const u32x4* p = (const u32x4*)(x + (size_t)(mt * 128 + wave * 16 + i * 4 + rq) * D_) + c16;
#pragma unroll
    for (int u = 0; u < 4; ++u) xr[i][u] = p[16 * u];
  }
  const unsigned tag = __builtin_amdgcn_readfirstlane(__hip_atomic_load((GAS unsigned*)(ws + WS_EP + ng * 128), __ATOMIC_RELAXED, __HIP_MEMORY_SCOPE_AGENT)) + 1u;
  unsigned long long tl0, tl1;
  {
    const int g0 = ng < 11 ? 3 * ng : 33 + 2 * (ng - 11);
    tl0 = TD(T_K, ng) | (TD(T_V, ng) << 9) | (TD(T_G, g0) << 18) | (TD(T_U, g0) << 27) | (TD(T_Q, ng) << 36);
    tl1 = TD(T_G, g0 + 1) | (TD(T_U, g0 + 1) << 9);
    if (ng < 11) tl1 |= (TD(T_G, g0 + 2) << 18) | (TD(T_U, g0 + 2) << 27);
  }
  auto tdesc = [&](int t) -> int { return (int)((((t & 1) ? tl1 : tl0) >> (9 * (t >> 1))) & 511); };
  auto tsrc = [&](int d, int& ld) -> const u16* {
    const int type = d >> 6, ti = d & 63;
    if (type <= T_V) { ld = 1536; return wqkv + (type - 1) * 512 + 32 * ti; }
    ld = F_; return (type == T_G ? w1 : w3) + 32 * ti;
  };
  auto load_tile = [&](int t) -> bool {
    const int d = t < NT ? tdesc(t) : 0;
    if (d == 0) return false;
    int ld; const u16* src = tsrc(d, ld);
    const u16* g = src + (size_t)(wave * 64 + (lane >> 2)) * ld + (lane & 3) * 8;
    const unsigned dst = lds0 + (t & 3) * SLOT + wave * 4096;
#pragma unroll
    for (int i = 0; i < 4; ++i) glds16(g + (size_t)i * 16 * ld, dst + i * 1024);
    return true;
  };
  load_tile(0);
  unsigned dummy = 0;
  {
    const int d = tdesc(wave + 1);
    if (d) { int ld; const u16* src = tsrc(d, ld) + (size_t)((idx & 7) * 64 + lane) * ld; asm volatile("global_load_dword %0, %1, off" : "+v"(dummy) : "v"(src) : "memory"); }
  }
  {
    const int gw = b * 8 + wave;
    if (gw < 16 * KS_W) {
      const int nb = gw / KS_W, ks = gw - nb * KS_W;
      const u16* src = (ks < 32 ? wo + (size_t)ks * 16 * D_ : w2 + (size_t)(ks - 32) * 16 * D_) + nb * 32 + r32 + (size_t)(4 * h) * D_;
      u32x4 v;
#pragma unroll
      for (int j = 0; j < 4; ++j) {
        const int k0 = 8 * (j >> 1) + 2 * (j & 1);
        v[j] = (unsigned)src[(size_t)k0 * D_] | ((unsigned)src[(size_t)(k0 + 1) * D_] << 16);
      }
      *(u32x4*)(ws + WS_WT + (size_t)gw * 1024 + lane * 16) = v;
    }
  }
  bf16x8 xf[32];
  {
    const unsigned stage = lds0 + SLOT;
    unsigned sel_lo = 0x00003f80u, sel_hi = 0x3f800000u;
    asm volatile("" : "+s"(sel_lo), "+s"(sel_hi));
#pragma unroll
    for (int i = 0; i < 4; ++i) {
      float s0 = 0.f, s1 = 0.f;
#pragma unroll
      for (int u = 0; u < 4; ++u) { s0 = dot2(xr[i][u][0], 0x3f803f80u, s0); s1 = dot2(xr[i][u][1], 0x3f803f80u, s1); s0 = dot2(xr[i][u][2], 0x3f803f80u, s0); s1 = dot2(xr[i][u][3], 0x3f803f80u, s1); }
      const float mean = row16sum(s0 + s1) * (1.0f / 512.0f);
      const float mb = bf16r(mean), nmb = -mb;
      float q0 = 0.f, q1 = 0.f;
#pragma unroll
      for (int u = 0; u < 4; ++u)
#pragma unroll
        for (int e = 0; e < 4; ++e) {
          const float lo = dot2(xr[i][u][e], sel_lo, nmb), hi = dot2(xr[i][u][e], sel_hi, nmb);
          q0 = fmaf(lo, lo, q0); q1 = fmaf(hi, hi, q1);
          xr[i][u][e] = cvtpk(lo, hi);
        }
      const float dm = mean - mb;
      const float var = fmaxf(row16sum(q0 + q1) * (1.0f / 512.0f) - dm * dm, 0.f);
      const float rs = 1.0f / bf16r(sqrtf(bf16r(bf16r(var) + 1e-5f)));
      const int rl = wave * 16 + i * 4 + rq;
#pragma unroll
      for (int u = 0; u < 4; ++u) {
        u32x4 o;
#pragma unroll
        for (int e = 0; e < 4; ++e) o[e] = cvtpk(bflo(xr[i][u][e]) * rs, bfhi(xr[i][u][e]) * rs);
        *(LAS u32x4*)(uintptr_t)(stage + rl * 1024 + (((c16 + 16 * u) ^ (rl & 15)) << 4)) = o;
      }
    }
    WAITV0();
    asm volatile("" : "+v"(dummy));
    BAR();
    const int rl = wr * 32 + r32;
    const unsigned rb = stage + rl * 1024; const int t = h ^ (rl & 15);
#pragma unroll
    for (int ks = 0; ks < 32; ++ks)
      xf[ks] = *(const LAS bf16x8*)(uintptr_t)(rb + (((2 * (ks & 7)) ^ t) << 4) + (ks >> 3) * 256);
    BAR();
  }
  STAMP(1);
  load_tile(1); load_tile(2);

  const int i16 = lane & 15, blk = (lane >> 4) & 1;
  const unsigned troff = (8 * h + (i16 >> 2)) * 64 + 8 * (4 * blk + (i16 & 3));
  const int mblk = mt * 4 + wr;
  unsigned sg[8];
#pragma unroll
  for (int i = 0; i < 8; ++i) sg[i] = 0u;
  f32x16 acc;
#pragma unroll
  for (int i = 0; i < 16; ++i) acc[i] = 0.f;
  u32x4 wf = {0u, 0u, 0u, 0u};
#define WAITLD() do { if (ldn) asm volatile("s_waitcnt vmcnt(4)" ::: "memory"); else WAITV0(); } while (0)

  for (int p = 0; p <= NT; ++p) {
    int ln = lane; asm volatile("" : "+v"(ln));
    if ((p & 1) == grp) {
      const int d = p < NT ? tdesc(p) : 0, type = d >> 6, ti = d & 63;
      if (type == T_Q || type == T_K) {
        const u16* wp = wphi + (size_t)(2 * ti + ((ln & 31) >> 4)) * 256 + (4 * (ln >> 5)) * 16 + (ln & 15);
#pragma unroll
        for (int j = 0; j < 4; ++j) { const int d0 = 8 * (j >> 1) + 2 * (j & 1); wf[j] = (unsigned)wp[d0 * 16] | ((unsigned)wp[(d0 + 1) * 16] << 16); }
      }
      const bool ldn = load_tile(p + 3);
      if (d) {
        __builtin_amdgcn_s_setprio(2);
        const unsigned sb = lds0 + (p & 3) * SLOT + troff;
        bf16x8 wq[4];
#pragma unroll
        for (int ks = 0; ks < 4; ++ks) wq[ks] = trfrag(sb + ks * 1024, sb + ks * 1024 + 256);
#pragma unroll
        for (int i = 0; i < 16; ++i) acc[i] = 0.f;
        SB0();
#pragma unroll
        for (int ks = 0; ks < 32; ++ks) {
          acc = MFMA(wq[ks & 3], xf[ks], acc);
          if (ks + 4 < 32) wq[ks & 3] = trfrag(sb + (ks + 4) * 1024, sb + (ks + 4) * 1024 + 256);
          SB0();
        }
        __builtin_amdgcn_s_setprio(0);
      }
      STAMP(2 + 2 * p);
      WAITLD();
    } else {
      const int d = p > 0 ? tdesc(p - 1) : 0, type = d >> 6, ti = d & 63;
      const bool ldn = load_tile(p + 3);
      u64 g1[8];
      const char* p1r = ws + WS_P1 + ((size_t)(ng * 16 + mt) * 16 + (ln >> 5) * 8) * 1024 + ((ln >> 4) & 1) * 512 + (wr * 16 + (ln & 15)) * 8;
      if (p == 6) {
#pragma unroll
        for (int j = 0; j < 8; ++j) g1[j] = ld_gran(p1r + j * 1024);
      }
      if (type == T_Q || type == T_K) {
        float av[16];
#pragma unroll
        for (int i = 0; i < 16; ++i) av[i] = acc[i];
        const bf16x8 qb0 = pack8(av), qb1 = pack8(av + 8);
        const u32x4 z4 = {0u, 0u, 0u, 0u};
        const bf16x8 a0 = __builtin_bit_cast(bf16x8, (r32 < 16) ? wf : z4), a1 = __builtin_bit_cast(bf16x8, (r32 < 16) ? z4 : wf);
        f32x16 pq;
#pragma unroll
        for (int i = 0; i < 16; ++i) pq[i] = 0.f;
        pq = MFMA(a0, qb0, pq);
        pq = MFMA(a1, qb1, pq);
        bf16x8 fp[2], fm[2];
#pragma unroll
        for (int s = 0; s < 2; ++s) {
          float pv[8];
#pragma unroll
          for (int j = 0; j < 8; j += 2) { const unsigned u = cvtpk(pq[8 * s + j], pq[8 * s + j + 1]); pv[j] = bflo(u); pv[j + 1] = bfhi(u); }
          float mx = pv[0], mn = pv[0];
#pragma unroll
          for (int j = 1; j < 8; ++j) { mx = fmaxf(mx, pv[j]); mn = fminf(mn, pv[j]); }
          mx = fmaxf(mx, xhalf(mx)); mn = fminf(mn, xhalf(mn));
          const float L2E = 1.4426950408889634f, mxl = -mx * L2E, mnl = mn * L2E;
          float ep[8], em[8], sp = 0.f, sm = 0.f;
#pragma unroll
          for (int j = 0; j < 8; ++j) {
            ep[j] = __builtin_amdgcn_exp2f(fmaf(pv[j], L2E, mxl)); sp += ep[j];
            em[j] = __builtin_amdgcn_exp2f(fmaf(pv[j], -L2E, mnl)); sm += em[j];
          }
          sp += xhalf(sp); sm += xhalf(sm);
          const float rp = __builtin_amdgcn_rcpf(sp), rm = __builtin_amdgcn_rcpf(sm);
#pragma unroll
          for (int j = 0; j < 8; ++j) { ep[j] *= rp; em[j] *= rm; }
          fp[s] = pack8(ep); fm[s] = pack8(em);
        }
        WAITLD();
        if (type == T_Q) {
#pragma unroll
          for (int s = 0; s < 2; ++s) {
            const bf16x8 k0 = *(const LAS bf16x8*)(uintptr_t)(lds0 + LDS_KV + s * 2048 + ln * 16), k1 = *(const LAS bf16x8*)(uintptr_t)(lds0 + LDS_KV + s * 2048 + 1024 + ln * 16);
            f32x16 o;
#pragma unroll
            for (int q = 0; q < 16; ++q) o[q] = 0.f;
            o = MFMA(k0, fp[s], o);
            o = MFMA(k1, fm[s], o);
            const float rd = __builtin_amdgcn_rcpf(bf16r(bf16r(o[8]) + 1e-6f));
            float ov[8];
#pragma unroll
            for (int j = 0; j < 8; j += 2) { const unsigned u = cvtpk(o[j], o[j + 1]); ov[j] = bflo(u) * rd; ov[j + 1] = bfhi(u) * rd; }
            *(bf16x8*)(ws + WS_AB + ((size_t)mblk * KS_W + 2 * ti + s) * 1024 + ln * 16) = pack8(ov);
          }
        } else {
          const int row = wr * 32 + (ln & 31), sw = (row >> 1) & 7, hh = ln >> 5;
#pragma unroll
          for (int s = 0; s < 2; ++s) {
            const unsigned pk = lds0 + LDS_SCR + s * 8192 + row * 64;
            const u32x4 up = __builtin_bit_cast(u32x4, fp[s]), um = __builtin_bit_cast(u32x4, fm[s]);
            u32x2 t;
            t[0] = up[0]; t[1] = up[1]; *(LAS u32x2*)(uintptr_t)(pk + 8 * ((hh) ^ sw)) = t;
            t[0] = up[2]; t[1] = up[3]; *(LAS u32x2*)(uintptr_t)(pk + 8 * ((2 + hh) ^ sw)) = t;
            t[0] = um[0]; t[1] = um[1]; *(LAS u32x2*)(uintptr_t)(pk + 8 * ((4 + hh) ^ sw)) = t;
            t[0] = um[2]; t[1] = um[3]; *(LAS u32x2*)(uintptr_t)(pk + 8 * ((6 + hh) ^ sw)) = t;
          }
        }
      } else if (type == T_V) {
        const int row = wr * 32 + (ln & 31), sw = (row >> 1) & 7, hh = ln >> 5;
        const unsigned vi = lds0 + LDS_SCR + 16384 + row * 64;
#pragma unroll
        for (int a = 0; a < 4; ++a) {
          u32x2 t; t[0] = cvtpk(acc[4 * a], acc[4 * a + 1]); t[1] = cvtpk(acc[4 * a + 2], acc[4 * a + 3]);
          *(LAS u32x2*)(uintptr_t)(vi + 8 * ((2 * a + hh) ^ sw)) = t;
        }
        WAITLD();
      } else if (type == T_G) {
#pragma unroll
        for (int i = 0; i < 16; i += 2) {
          const unsigned u = cvtpk(acc[i], acc[i + 1]);
          sg[i >> 1] = cvtpk(silu_f(bflo(u)), silu_f(bfhi(u)));
        }
        WAITLD();
      } else if (type == T_U) {
        float hv[16];
#pragma unroll
        for (int i = 0; i < 16; i += 2) { const unsigned u = cvtpk(acc[i], acc[i + 1]); hv[i] = bflo(sg[i >> 1]) * bflo(u); hv[i + 1] = bfhi(sg[i >> 1]) * bfhi(u); }
        const bf16x8 h0 = pack8(hv), h1 = pack8(hv + 8);
        WAITLD();
        char* dst = ws + WS_AB + ((size_t)mblk * KS_W + 32 + 2 * ti) * 1024 + ln * 16;
        *(bf16x8*)dst = h0; *(bf16x8*)(dst + 1024) = h1;
      } else {
        WAITLD();
      }
      if (p == 4 && wr < 2) {
        f32x16 a2;
#pragma unroll
        for (int i = 0; i < 16; ++i) a2[i] = 0.f;
        const int rowq = 8 * (ln >> 5) + ((ln & 15) >> 2), bk = (ln >> 4) & 1;
        const int ca = 4 * bk + (ln & 3), cb = 4 * (bk ^ wr) + (ln & 3);
        const int sw0 = (rowq >> 1) & 7, sw1 = ((rowq + 4) >> 1) & 7;
        const unsigned pk = lds0 + LDS_SCR + wr * 8192 + rowq * 64, vi = lds0 + LDS_SCR + 16384 + rowq * 64;
        const unsigned pa0 = pk + 8 * (ca ^ sw0), pa1 = pk + 256 + 8 * (ca ^ sw1), pb0 = vi + 8 * (cb ^ sw0), pb1 = vi + 256 + 8 * (cb ^ sw1);
#pragma unroll
        for (int ks = 0; ks < 8; ++ks) {
          bf16x8 af = trfrag(pa0 + ks * 1024, pa1 + ks * 1024);
          bf16x8 bfr = trfrag(pb0 + ks * 1024, pb1 + ks * 1024);
          if (bk) {
#pragma unroll
            for (int j = 0; j < 8; ++j) bfr[j] = (short)0x3f80;
          }
          a2 = MFMA(af, bfr, a2);
        }
        char* p1w = ws + WS_P1 + (((size_t)(ng * 16 + wr * 8) * 16 + mt) * 1024) + ln * 8;
#pragma unroll
        for (int i = 0; i < 16; ++i) st_gran(p1w + (size_t)(i >> 1) * 16384 + (i & 1) * 512, __float_as_uint(a2[i]), tag);
      }
      if (p == 6) {
        for (unsigned spin = 0; spin < SPIN_MAX; ++spin) {
          bool ok = true;
#pragma unroll
          for (int j = 0; j < 8; ++j) ok &= (unsigned)(g1[j] >> 32) == tag;
          if (__all(ok)) break;
          __builtin_amdgcn_s_sleep(8);
#pragma unroll
          for (int j = 0; j < 8; ++j) g1[j] = ld_gran(p1r + j * 1024);
        }
        float a = 0.f;
#pragma unroll
        for (int j = 0; j < 8; ++j) a += __uint_as_float((unsigned)g1[j]);
        const float ao = xhalf(a);
        a = (ln & 32) ? ao + a : a + ao;
        const float c = __shfl_xor(a, 16);
        if (ln < 16) st_gran(ws + WS_P2 + (size_t)(ng * 16 + mt) * 512 + (wr * 16 + ln) * 8, cvtpk(a, c), tag);
        if (mt == 0 && wr == 0 && ln == 0) __hip_atomic_store((GAS unsigned*)(ws + WS_EP + ng * 128), tag, __ATOMIC_RELAXED, __HIP_MEMORY_SCOPE_AGENT);
      }
      if (p == 8 && wr < 2) {
        const char* p2r = ws + WS_P2 + (size_t)(ng * 16 + wr * 8) * 512 + ln * 8;
        u64 g2[8];
        for (unsigned spin = 0; spin < SPIN_MAX; ++spin) {
          bool ok = true;
#pragma unroll
          for (int j = 0; j < 8; ++j) g2[j] = ld_gran(p2r + j * 512);
#pragma unroll
          for (int j = 0; j < 8; ++j) ok &= (unsigned)(g2[j] >> 32) == tag;
          if (__all(ok)) break;
          __builtin_amdgcn_s_sleep(8);
        }
        u32x4 f0 = {(unsigned)g2[0], (unsigned)g2[1], (unsigned)g2[2], (unsigned)g2[3]}, f1 = {(unsigned)g2[4], (unsigned)g2[5], (unsigned)g2[6], (unsigned)g2[7]};
        *(LAS u32x4*)(uintptr_t)(lds0 + LDS_KV + wr * 2048 + ln * 16) = f0;
        *(LAS u32x4*)(uintptr_t)(lds0 + LDS_KV + wr * 2048 + 1024 + ln * 16) = f1;
      }
      STAMP(2 + 2 * p);
    }
    if (p < NT) BAR();
    STAMP(3 + 2 * p);
  }
  STAMPR(31);
  STAMP_FLUSH(0);
}

constexpr int XROW = 68;
__global__ __launch_bounds__(256, 1) void k2_kernel(const u16* __restrict__ x, const char* __restrict__ ws, u16* __restrict__ out) {
  __shared__ __attribute__((aligned(16))) float xch[4 * 64 * XROW];
  const int tid = threadIdx.x, lane = tid & 63;
  const int wave = __builtin_amdgcn_readfirstlane(tid >> 6);
  const int b = blockIdx.x, xcd = b & 7, idx = b >> 3;
  const int mt = xcd * 4 + (idx & 3), nt = idx >> 2;
  const int r32 = lane & 31, h = lane >> 5;
  STAMP(0); STAMPR(30);
  f32x16 acc[2][2];
#pragma unroll
  for (int n = 0; n < 2; ++n)
#pragma unroll
    for (int m = 0; m < 2; ++m)
#pragma unroll
      for (int i = 0; i < 16; ++i) acc[n][m][i] = 0.f;
  const int k0 = wave == 0 ? 0 : 32 + (wave - 1) * 29;
  const int nk = wave == 0 ? 32 : (wave == 3 ? 28 : 29);
  const char* wa = ws + WS_WT + ((size_t)(2 * nt) * KS_W + k0) * 1024 + lane * 16;
  const char* wb = wa + (size_t)KS_W * 1024;
  const char* ha = ws + WS_AB + ((size_t)(2 * mt) * KS_W + k0) * 1024 + lane * 16;
  const char* hb = ha + (size_t)KS_W * 1024;
  constexpr int P = 14;
  bf16x8 bufs[P][4];
  auto ld = [&](int i, bf16x8* d) {
    d[0] = *(const bf16x8*)(ha + i * 1024); d[1] = *(const bf16x8*)(hb + i * 1024);
    d[2] = *(const bf16x8*)(wa + i * 1024); d[3] = *(const bf16x8*)(wb + i * 1024);
  };
#pragma unroll
  for (int i = 0; i < P; ++i) ld(i, bufs[i]);
  const int erow = mt * 64 + (tid >> 2), ecol = nt * 64 + (tid & 3) * 16;
  const u32x4 xa = *(const u32x4*)(x + (size_t)erow * D_ + ecol), xb = *(const u32x4*)(x + (size_t)erow * D_ + ecol + 8);
  SB0();
#pragma unroll
  for (int i = 0; i < 28; ++i) {
    bf16x8* d = bufs[i % P];
    acc[0][0] = MFMA(d[2], d[0], acc[0][0]); acc[0][1] = MFMA(d[2], d[1], acc[0][1]);
    acc[1][0] = MFMA(d[3], d[0], acc[1][0]); acc[1][1] = MFMA(d[3], d[1], acc[1][1]);
    SB0();
    if (i + P < 28) ld(i + P, bufs[i % P]);
    else if (i + P < 32) { if (i + P < nk) ld(i + P, bufs[i % P]); }
    SB0();
  }
#pragma unroll
  for (int i = 28; i < 32; ++i) {
    if (i < nk) {
      bf16x8* d = bufs[i % P];
      acc[0][0] = MFMA(d[2], d[0], acc[0][0]); acc[0][1] = MFMA(d[2], d[1], acc[0][1]);
      acc[1][0] = MFMA(d[3], d[0], acc[1][0]); acc[1][1] = MFMA(d[3], d[1], acc[1][1]);
    }
  }
  STAMP(1);
  float* mine = xch + wave * 64 * XROW;
#pragma unroll
  for (int n = 0; n < 2; ++n)
#pragma unroll
    for (int m = 0; m < 2; ++m)
#pragma unroll
      for (int a = 0; a < 4; ++a) {
        f32x4 t = {acc[n][m][4 * a], acc[n][m][4 * a + 1], acc[n][m][4 * a + 2], acc[n][m][4 * a + 3]};
        *(f32x4*)(mine + (32 * m + r32) * XROW + 32 * n + 8 * a + 4 * h) = t;
      }
  __syncthreads();
  STAMP(2);
  const float* rp = xch + (tid >> 2) * XROW + (tid & 3) * 16;
  u32x4 res[2];
#pragma unroll
  for (int q = 0; q < 4; ++q) {
    const f32x4 s0 = *(const f32x4*)(rp + 4 * q), o0 = *(const f32x4*)(rp + 64 * XROW + 4 * q);
    const f32x4 o1 = *(const f32x4*)(rp + 128 * XROW + 4 * q), o2 = *(const f32x4*)(rp + 192 * XROW + 4 * q);
#pragma unroll
    for (int e = 0; e < 4; e += 2) {
      const unsigned su = cvtpk(s0[e], s0[e + 1]);
      const unsigned ou = cvtpk((o0[e] + o1[e]) + o2[e], (o0[e + 1] + o1[e + 1]) + o2[e + 1]);
      const unsigned gu = cvtpk(silu_f(bflo(ou)), silu_f(bfhi(ou)));
      const unsigned tu = cvtpk(bflo(su) * bflo(gu), bfhi(su) * bfhi(gu));
      const unsigned xw = (q < 2 ? xa : xb)[(q & 1) * 2 + (e >> 1)];
      res[q >> 1][(q & 1) * 2 + (e >> 1)] = cvtpk(bflo(xw) + bflo(tu), bfhi(xw) + bfhi(tu));
    }
  }
  *(u32x4*)(out + (size_t)erow * D_ + ecol) = res[0];
  *(u32x4*)(out + (size_t)erow * D_ + ecol + 8) = res[1];
  STAMP(3); STAMPR(31);
}

extern "C" void kernel_launch(void* const* d_in, const int* in_sizes, int n_in, void* d_out, int out_size, void* d_ws, size_t ws_size,
                              hipStream_t stream) {
  const u16* x = (const u16*)d_in[0];
  const u16* wqkv = (const u16*)d_in[1];
  const u16* wphi = (const u16*)d_in[2];
  const u16* wo = (const u16*)d_in[3];
  const u16* w1 = (const u16*)d_in[5];
  const u16* w3 = (const u16*)d_in[6];
  const u16* w2 = (const u16*)d_in[7];
  char* ws = (char*)d_ws;
  k1_kernel<<<256, 512, 0, stream>>>(x, wqkv, wphi, wo, w1, w3, w2, ws);
  k2_kernel<<<256, 256, 0, stream>>>(x, ws, (u16*)d_out);
}
```

```cpp
#include <hip/hip_runtime.h>
#include <hip/hip_bf16.h>
#include <stdint.h>

typedef __attribute__((ext_vector_type(8))) short bf16x8;
typedef __attribute__((ext_vector_type(4))) short s16x4;
typedef __attribute__((ext_vector_type(16))) float f32x16;
typedef __attribute__((ext_vector_type(4))) float f32x4;
typedef __attribute__((ext_vector_type(4))) unsigned u32x4;
typedef __attribute__((ext_vector_type(2))) unsigned u32x2;
typedef unsigned short u16;
#define LAS __attribute__((address_space(3)))
#define DI __device__ __forceinline__
#define MFMA(a, b, c) __builtin_amdgcn_mfma_f32_32x32x16_bf16((a), (b), (c), 0, 0, 0)

constexpr int S_ = 2048, D_ = 512, NH = 32, F_ = 1376;
constexpr int KS_F = F_ / 16;
constexpr int KS_W = 32 + KS_F;
constexpr size_t WS_AB = 0;
constexpr size_t WS_WT = WS_AB + (size_t)64 * KS_W * 1024;
constexpr size_t WS_P1 = WS_WT + (size_t)16 * KS_W * 1024;
constexpr size_t WS_P2 = WS_P1 + (size_t)16 * 16 * 16 * 1024;
constexpr size_t WS_EP = WS_P2 + (size_t)16 * 16 * 512;
constexpr size_t WS_END = WS_EP + 16 * 128;

typedef __attribute__((ext_vector_type(2))) __bf16 bf16x2_t;
typedef __attribute__((ext_vector_type(2))) float f32x2;
DI unsigned cvtpk(float lo, float hi) { f32x2 v = {lo, hi}; return __builtin_bit_cast(unsigned, __builtin_convertvector(v, bf16x2_t)); }
DI float bflo(unsigned u) { return __uint_as_float(u << 16); }
DI float bfhi(unsigned u) { return __uint_as_float(u & 0xffff0000u); }
DI float bf16r(float x) { return bflo(cvtpk(x, x)); }
DI void glds16(const void* g, unsigned lds_base) {
  unsigned sv;
  asm volatile("s_mov_b32 %0, m0\n\ts_mov_b32 m0, %2\n\ts_nop 0\n\tglobal_load_lds_dwordx4 %1, off\n\ts_mov_b32 m0, %0"
               : "=&s"(sv) : "v"(g), "s"(lds_base) : "memory");
}
typedef short v4i16_t __attribute__((ext_vector_type(4)));
DI s16x4 trrd(unsigned a) { return __builtin_bit_cast(s16x4, __builtin_amdgcn_ds_read_tr16_b64_v4i16((LAS v4i16_t*)(uintptr_t)a)); }
DI bf16x8 trfrag(unsigned lo, unsigned hi) { s16x4 a = trrd(lo), b = trrd(hi); return __builtin_shufflevector(a, b, 0, 1, 2, 3, 4, 5, 6, 7); }
DI bf16x8 pack8(const float* v) {
  u32x4 p; p[0] = cvtpk(v[0], v[1]); p[1] = cvtpk(v[2], v[3]); p[2] = cvtpk(v[4], v[5]); p[3] = cvtpk(v[6], v[7]);
  return __builtin_bit_cast(bf16x8, p);
}
DI float xhalf(float v) {
  auto rr = __builtin_amdgcn_permlane32_swap(__float_as_uint(v), __float_as_uint(v), false, false);
  unsigned a = rr[0], b = rr[1];
  return __uint_as_float((threadIdx.x & 32) ? a : b);
}
template <int C> DI float dppror(float v) { return __uint_as_float(__builtin_amdgcn_update_dpp(0u, __float_as_uint(v), 0x120 + C, 0xf, 0xf, false)); }
DI float row16sum(float v) { v += dppror<8>(v); v += dppror<4>(v); v += dppror<2>(v); v += dppror<1>(v); return v; }
DI float silu_f(float x) { return x * __builtin_amdgcn_rcpf(1.0f + __builtin_amdgcn_exp2f(-1.4426950408889634f * x)); }
#define STAMP(i)
#define STAMPR(i)
#define STAMP_FLUSH(base)
#define BAR() asm volatile("s_waitcnt lgkmcnt(0)\n\ts_barrier" ::: "memory")
#define WAITV0() asm volatile("s_waitcnt vmcnt(0)" ::: "memory")

enum { T_NONE = 0, T_Q = 1, T_K = 2, T_V = 3, T_G = 4, T_U = 5 };
#define TD(ty, ix) ((unsigned long long)(((ty) << 6) | (ix)))
#define SB0() __builtin_amdgcn_sched_barrier(0)
#define GAS __attribute__((address_space(1)))
typedef unsigned long long u64;
DI float dot2(unsigned a, unsigned b, float c) {
  return __builtin_amdgcn_fdot2_f32_bf16(__builtin_bit_cast(bf16x2_t, a), __builtin_bit_cast(bf16x2_t, b), c, false);
}
DI void st_gran(void* p, unsigned val, unsigned tag) { __hip_atomic_store((GAS u64*)p, ((u64)tag << 32) | val, __ATOMIC_RELAXED, __HIP_MEMORY_SCOPE_AGENT); }
DI u64 ld_gran(const void* p) { return __hip_atomic_load((GAS u64*)p, __ATOMIC_RELAXED, __HIP_MEMORY_SCOPE_AGENT); }
constexpr unsigned SPIN_MAX = 1u << 16;

constexpr int SLOT = 32768;
constexpr int LDS_SCR = 4 * SLOT;
constexpr int LDS_DBG = LDS_SCR + 28672;
constexpr int K1_LDS = 5 * SLOT;

DI void glds16s(unsigned voff, const void* sbase, unsigned lds_base) {
  unsigned sv;
  asm volatile("s_mov_b32 %0, m0\n\ts_mov_b32 m0, %3\n\ts_nop 0\n\tglobal_load_lds_dwordx4 %1, %2\n\ts_mov_b32 m0, %0"
               : "=&s"(sv) : "v"(voff), "s"(sbase), "s"(lds_base) : "memory");
}
DI void center4(u32x4& a, float c) {
  unsigned t0, t1, t2, t3, t4, t5, t6, t7;
  asm("v_dot2_f32_bf16 %0, %8, %12, %14\n\tv_dot2_f32_bf16 %1, %8, %13, %14\n\tv_dot2_f32_bf16 %2, %9, %12, %14\n\tv_dot2_f32_bf16 %3, %9, %13, %14\n\t"
      "v_dot2_f32_bf16 %4, %10, %12, %14\n\tv_dot2_f32_bf16 %5, %10, %13, %14\n\tv_dot2_f32_bf16 %6, %11, %12, %14\n\tv_dot2_f32_bf16 %7, %11, %13, %14\n\t"
      "v_cvt_pk_bf16_f32 %0, %0, %1\n\tv_cvt_pk_bf16_f32 %2, %2, %3\n\tv_cvt_pk_bf16_f32 %4, %4, %5\n\tv_cvt_pk_bf16_f32 %6, %6, %7"
      : "=&v"(t0), "=&v"(t1), "=&v"(t2), "=&v"(t3), "=&v"(t4), "=&v"(t5), "=&v"(t6), "=&v"(t7)
      : "v"(a[0]), "v"(a[1]), "v"(a[2]), "v"(a[3]), "s"(0x00003f80u), "s"(0x3f800000u), "v"(c));
  a[0] = t0; a[1] = t2; a[2] = t4; a[3] = t6;
}
DI float rsum32(float v) { auto rr = __builtin_amdgcn_permlane32_swap(__float_as_uint(v), __float_as_uint(v), false, false); return __uint_as_float(rr[0]) + __uint_as_float(rr[1]); }
DI float rsum16(float v) { auto rr = __builtin_amdgcn_permlane16_swap(__float_as_uint(v), __float_as_uint(v), false, false); return __uint_as_float(rr[0]) + __uint_as_float(rr[1]); }
DI float rmax32(float v) { auto rr = __builtin_amdgcn_permlane32_swap(__float_as_uint(v), __float_as_uint(v), false, false); return fmaxf(__uint_as_float(rr[0]), __uint_as_float(rr[1])); }
DI float rmin32(float v) { auto rr = __builtin_amdgcn_permlane32_swap(__float_as_uint(v), __float_as_uint(v), false, false); return fminf(__uint_as_float(rr[0]), __uint_as_float(rr[1])); }
#define WAITV(n) asm volatile("s_waitcnt vmcnt(" #n ")" ::: "memory")

DI void mfma_tile(f32x16& acc, const bf16x8 (&xf)[32], unsigned sb, bool ld, const char* g, unsigned gstep, unsigned voff, unsigned dst) {
  __builtin_amdgcn_s_setprio(2);
  bf16x8 wq[4];
#pragma unroll
  for (int ks = 0; ks < 4; ++ks) wq[ks] = trfrag(sb + ks * 512, sb + ks * 512 + 256);
#pragma unroll
  for (int i = 0; i < 16; ++i) acc[i] = 0.f;
  SB0();
#pragma unroll
  for (int ks = 0; ks < 32; ++ks) {
    acc = MFMA(wq[ks & 3], xf[ks], acc);
    if (ks + 4 < 32) { const int o = ((ks + 4) >> 2) * 4096 + ((ks + 4) & 3) * 512; wq[ks & 3] = trfrag(sb + o, sb + o + 256); }
    if ((ks & 1) == 0 && ks < 16) { if (ld) glds16s(voff, g + (size_t)(ks >> 1) * gstep, dst + (ks >> 1) * 1024); }
    SB0();
  }
  __builtin_amdgcn_s_setprio(0);
  asm volatile("" : "+v"(acc));
}
DI void feat_map(const f32x16& acc, const u32x4& wf, int r32, bf16x8 (&fp)[2], bf16x8 (&fm)[2]) {
  float av[16];
#pragma unroll
  for (int i = 0; i < 16; ++i) av[i] = acc[i];
  const bf16x8 qb0 = pack8(av), qb1 = pack8(av + 8);
  const u32x4 z4 = {0u, 0u, 0u, 0u};
  const bf16x8 a0 = __builtin_bit_cast(bf16x8, (r32 < 16) ? wf : z4), a1 = __builtin_bit_cast(bf16x8, (r32 < 16) ? z4 : wf);
  f32x16 pq;
#pragma unroll
  for (int i = 0; i < 16; ++i) pq[i] = 0.f;
  pq = MFMA(a0, qb0, pq);
  pq = MFMA(a1, qb1, pq);
#pragma unroll
  for (int s = 0; s < 2; ++s) {
    float pv[8];
#pragma unroll
    for (int j = 0; j < 8; j += 2) { const unsigned u = cvtpk(pq[8 * s + j], pq[8 * s + j + 1]); pv[j] = bflo(u); pv[j + 1] = bfhi(u); }
    float mx = fmaxf(fmaxf(fmaxf(pv[0], pv[1]), pv[2]), fmaxf(fmaxf(pv[3], pv[4]), pv[5])), mn = fminf(fminf(fminf(pv[0], pv[1]), pv[2]), fminf(fminf(pv[3], pv[4]), pv[5]));
    mx = fmaxf(fmaxf(mx, pv[6]), pv[7]); mn = fminf(fminf(mn, pv[6]), pv[7]);
    mx = rmax32(mx); mn = rmin32(mn);
    const float L2E = 1.4426950408889634f, mxl = -mx * L2E, mnl = mn * L2E;
    float ep[8], em[8], sp = 0.f, sm = 0.f;
#pragma unroll
    for (int j = 0; j < 8; ++j) {
      ep[j] = __builtin_amdgcn_exp2f(fmaf(pv[j], L2E, mxl)); sp += ep[j];
      em[j] = __builtin_amdgcn_exp2f(fmaf(pv[j], -L2E, mnl)); sm += em[j];
    }
    sp = rsum32(sp); sm = rsum32(sm);
    const float rp = __builtin_amdgcn_rcpf(sp), rm = __builtin_amdgcn_rcpf(sm);
#pragma unroll
    for (int j = 0; j < 8; ++j) { ep[j] *= rp; em[j] *= rm; }
    fp[s] = pack8(ep); fm[s] = pack8(em);
  }
  asm volatile("" : "+v"(fp[0]), "+v"(fp[1]), "+v"(fm[0]), "+v"(fm[1]));
}
DI void epi_g(const f32x16& acc, unsigned (&sg)[8]) {
#pragma unroll
  for (int i = 0; i < 16; i += 2) {
    const unsigned u = cvtpk(acc[i], acc[i + 1]);
    sg[i >> 1] = cvtpk(silu_f(bflo(u)), silu_f(bfhi(u)));
  }
  asm volatile("" : "+v"(sg[0]), "+v"(sg[1]), "+v"(sg[2]), "+v"(sg[3]), "+v"(sg[4]), "+v"(sg[5]), "+v"(sg[6]), "+v"(sg[7]));
}
DI void epi_u(const f32x16& acc, const unsigned (&sg)[8], char* dst) {
  float hv[16];
#pragma unroll
  for (int i = 0; i < 16; i += 2) { const unsigned u = cvtpk(acc[i], acc[i + 1]); hv[i] = bflo(sg[i >> 1]) * bflo(u); hv[i + 1] = bfhi(sg[i >> 1]) * bfhi(u); }
  *(bf16x8*)dst = pack8(hv); *(bf16x8*)(dst + 1024) = pack8(hv + 8);
}
#define PHASE_END(p) do { STAMP(2 + 2 * (p)); SB0(); BAR(); SB0(); STAMP(3 + 2 * (p)); } while (0)

__global__ __launch_bounds__(512, 2) void k1_kernel(const u16* __restrict__ x, const u16* __restrict__ wqkv, const u16* __restrict__ wphi,
                                                    const u16* __restrict__ wo, const u16* __restrict__ w1, const u16* __restrict__ w3,
                                                    const u16* __restrict__ w2, char* __restrict__ ws) {
  __shared__ __attribute__((aligned(16))) char smem[K1_LDS];
  const int tid = threadIdx.x, lane = tid & 63;
  const int wave = __builtin_amdgcn_readfirstlane(tid >> 6), grp = wave >> 2, wr = wave & 3;
  const int b = blockIdx.x, xcd = b & 7, idx = b >> 3;
  const int mt = (xcd & 1) * 8 + (idx & 7), ng = (xcd >> 1) * 4 + (idx >> 3);
  const int r32 = lane & 31, h = lane >> 5;
  const unsigned lds0 = (unsigned)(uintptr_t)smem;
  const unsigned reg = lds0 + SLOT + (wr * 2 + grp) * 16384;
  {
    const char* xb = (const char*)(x + (size_t)(mt * 128 + wr * 32 + grp * 16) * D_);
    const int rr = lane >> 5, qd = (lane >> 3) & 3, pos = lane & 7;
#pragma unroll
    for (int cc = 0; cc < 2; ++cc)
#pragma unroll
      for (int i = 0; i < 8; ++i)
        glds16s(rr * 1024 + qd * 256 + ((pos ^ (2 * (i & 3) + rr)) << 4), xb + i * 2048 + cc * 128, reg + cc * 8192 + i * 1024);
  }
  const int g0 = ng < 11 ? 3 * ng : 33 + 2 * (ng - 11);
  const bool full = ng < 11;
  const char* wK = (const char*)(wqkv + 512 + 32 * ng);
  const char* wG = (const char*)(w1 + 32 * g0);
  const char* wU = (const char*)(w3 + 32 * g0);
  const unsigned voffA = (lane >> 2) * 3072 + (lane & 3) * 16, voffF = (lane >> 2) * 2752 + (lane & 3) * 16;
  {
    const char* g = wK + (size_t)wave * 64 * 3072;
#pragma unroll
    for (int i = 0; i < 4; ++i) glds16s(voffA, g + i * 16 * 3072, lds0 + wave * 4096 + i * 1024);
  }
  unsigned dummy = 0;
  {
    const int row = (idx & 7) * 64 + lane;
    const int e = (wave >= 6 && !full) ? 0 : (wave - 2) >> 1;
    const char* p = wave < 2 ? wK + (wave ? -1024 : 1024) + (size_t)row * 3072 : ((wave & 1) ? wU : wG) + 64 * e + (size_t)row * 2752;
    asm volatile("global_load_dword %0, %1, off" : "+v"(dummy) : "v"(p) : "memory");
  }
  const unsigned tagv = __hip_atomic_load((GAS unsigned*)(ws + WS_EP + ng * 128), __ATOMIC_RELAXED, __HIP_MEMORY_SCOPE_AGENT);
  u32x4 wf, wfb, rtv, rtb;
  const int gw = b * 8 + wave;
  {
    const u16* wp = wphi + (size_t)(2 * ng + (r32 >> 4)) * 256 + (4 * h) * 16 + (lane & 15);
    const int gc = gw < 16 * KS_W ? gw : 0, nb = gc / KS_W, ks = gc - nb * KS_W;
    const u16* src = (ks < 32 ? wo + (size_t)ks * 16 * D_ : w2 + (size_t)(ks - 32) * 16 * D_) + nb * 32 + r32 + (size_t)(4 * h) * D_;
#pragma unroll
    for (int j = 0; j < 4; ++j) {
      const int k0 = 8 * (j >> 1) + 2 * (j & 1);
      asm volatile("global_load_ushort %0, %2, off offset:%3\n\tglobal_load_ushort %1, %2, off offset:%4" : "=&v"(wf[j]), "=&v"(wfb[j]) : "v"(wp), "n"(k0 * 32), "n"(k0 * 32 + 32) : "memory");
      asm volatile("global_load_ushort %0, %2, off\n\tglobal_load_ushort %1, %2, off offset:1024" : "=&v"(rtv[j]), "=&v"(rtb[j]) : "v"(src + (size_t)k0 * D_) : "memory");
    }
  }
  bf16x8 xf[32];
  unsigned tag;
  {
    const int r16 = lane & 15, qd = lane >> 4;
    const unsigned rb = reg + (r16 >> 1) * 1024 + (r16 & 1) * 512 + qd * 128 + ((r16 & 7) << 4);
    u32x4 xr[16];
    float s0 = 0.f, s1 = 0.f, q0 = 0.f, q1 = 0.f;
    WAITV(30);
#pragma unroll
    for (int q = 0; q < 8; ++q) xr[q] = *(const LAS u32x4*)(uintptr_t)(rb ^ (q << 4));
#pragma unroll
    for (int q = 0; q < 8; ++q)
#pragma unroll
      for (int e = 0; e < 4; e += 2) {
        s0 = dot2(xr[q][e], 0x3f803f80u, s0); s1 = dot2(xr[q][e + 1], 0x3f803f80u, s1);
        q0 = dot2(xr[q][e], xr[q][e], q0); q1 = dot2(xr[q][e + 1], xr[q][e + 1], q1);
      }
    SB0();
    WAITV(22);
#pragma unroll
    for (int q = 0; q < 8; ++q) xr[8 + q] = *(const LAS u32x4*)(uintptr_t)((rb ^ (q << 4)) + 8192);
#pragma unroll
    for (int q = 8; q < 16; ++q)
#pragma unroll
      for (int e = 0; e < 4; e += 2) {
        s0 = dot2(xr[q][e], 0x3f803f80u, s0); s1 = dot2(xr[q][e + 1], 0x3f803f80u, s1);
        q0 = dot2(xr[q][e], xr[q][e], q0); q1 = dot2(xr[q][e + 1], xr[q][e + 1], q1);
      }
    const float mean = rsum32(rsum16(s0 + s1)) * (1.0f / 512.0f);
    const float var = fmaxf(rsum32(rsum16(q0 + q1)) * (1.0f / 512.0f) - mean * mean, 0.f);
    const float nmb = -bf16r(mean);
    const float rs = 1.0f / bf16r(sqrtf(bf16r(bf16r(var) + 1e-5f)));
    const unsigned wb = reg + qd * 4096 + r16 * 16;
#pragma unroll
    for (int q = 0; q < 16; ++q) {
      u32x4 o;
      center4(xr[q], nmb);
#pragma unroll
      for (int e = 0; e < 4; ++e) o[e] = cvtpk(bflo(xr[q][e]) * rs, bfhi(xr[q][e]) * rs);
      *(LAS u32x4*)(uintptr_t)(wb + (q >> 3) * 2048 + (q & 3) * 512 + ((q >> 2) & 1) * 256) = o;
    }
    SB0();
    WAITV(0);
    asm volatile("" : "+v"(dummy), "+v"(wf), "+v"(rtv), "+v"(wfb), "+v"(rtb));
#pragma unroll
    for (int j = 0; j < 4; ++j) { wf[j] |= wfb[j] << 16; rtv[j] |= rtb[j] << 16; }
    tag = __builtin_amdgcn_readfirstlane(tagv) + 1u;
    asm volatile("" : "+s"(tag));
    if (gw < 16 * KS_W) *(u32x4*)(ws + WS_WT + (size_t)gw * 1024 + lane * 16) = rtv;
    BAR();
    const unsigned xb = lds0 + SLOT * (1 + wr) + (r32 >> 4) * 16384 + (h * 16 + (r32 & 15)) * 16;
#pragma unroll
    for (int ks = 0; ks < 32; ++ks) xf[ks] = *(const LAS bf16x8*)(uintptr_t)(xb + ks * 512);
    BAR();
  }
  STAMP(1);
  {
    const char* g = wK + 1024 + (size_t)wave * 64 * 3072;
#pragma unroll
    for (int i = 0; i < 4; ++i) glds16s(voffA, g + i * 16 * 3072, lds0 + SLOT + wave * 4096 + i * 1024);
    const char* g2 = wG + (size_t)wave * 64 * 2752;
#pragma unroll
    for (int i = 0; i < 4; ++i) glds16s(voffF, g2 + i * 16 * 2752, lds0 + 2 * SLOT + wave * 4096 + i * 1024);
  }
  const int i16 = lane & 15, blk = (lane >> 4) & 1;
  const unsigned tr0 = lds0 + (32 * h + (i16 >> 2)) * 64 + 8 * (4 * blk + (i16 & 3));
  const int mblk = mt * 4 + wr;
  const unsigned ldd = lds0 + wr * 8192;
  const size_t rowF = (size_t)wr * 128 * 2752, rowA = (size_t)wr * 128 * 3072;
  char* const hdst = ws + WS_AB + ((size_t)mblk * KS_W + 32 + 2 * g0) * 1024 + lane * 16;
  unsigned sg[8];
  f32x16 acc;
  const int row = wr * 32 + r32, sw = (row >> 1) & 7;
  if (grp == 0) {
    mfma_tile(acc, xf, tr0, true, wG + 64 + rowF, 16 * 2752, voffF, ldd + 3 * SLOT);
    WAITV(12);
    PHASE_END(0);
    {
      bf16x8 fp[2], fm[2];
      feat_map(acc, wf, r32, fp, fm);
#pragma unroll
      for (int s = 0; s < 2; ++s) {
        const unsigned pk = lds0 + LDS_SCR + s * 8192 + row * 64;
        const u32x4 up = __builtin_bit_cast(u32x4, fp[s]), um = __builtin_bit_cast(u32x4, fm[s]);
        u32x2 t;
        t[0] = up[0]; t[1] = up[1]; *(LAS u32x2*)(uintptr_t)(pk + 8 * ((h) ^ sw)) = t;
        t[0] = up[2]; t[1] = up[3]; *(LAS u32x2*)(uintptr_t)(pk + 8 * ((2 + h) ^ sw)) = t;
        t[0] = um[0]; t[1] = um[1]; *(LAS u32x2*)(uintptr_t)(pk + 8 * ((4 + h) ^ sw)) = t;
        t[0] = um[2]; t[1] = um[3]; *(LAS u32x2*)(uintptr_t)(pk + 8 * ((6 + h) ^ sw)) = t;
      }
    }
    SB0(); WAITV(8);
    PHASE_END(1);
    mfma_tile(acc, xf, tr0 + 2 * SLOT, true, wU + 64 + rowF, 16 * 2752, voffF, ldd + 1 * SLOT);
    WAITV(8);
    PHASE_END(2);
    if (wr < 2) {
      f32x16 a2;
#pragma unroll
      for (int i = 0; i < 16; ++i) a2[i] = 0.f;
      const int rowq = 8 * h + (i16 >> 2);
      const int ca = 4 * blk + (lane & 3), cb = 4 * (blk ^ wr) + (lane & 3);
      const int sw0 = (rowq >> 1) & 7, sw1 = ((rowq + 4) >> 1) & 7;
      const unsigned pk = lds0 + LDS_SCR + wr * 8192 + rowq * 64, vi = lds0 + LDS_SCR + 16384 + rowq * 64;
      const unsigned pa0 = pk + 8 * (ca ^ sw0), pa1 = pk + 256 + 8 * (ca ^ sw1), pb0 = vi + 8 * (cb ^ sw0), pb1 = vi + 256 + 8 * (cb ^ sw1);
#pragma unroll
      for (int ks = 0; ks < 8; ++ks) {
        bf16x8 af = trfrag(pa0 + ks * 1024, pa1 + ks * 1024);
        bf16x8 bfr = trfrag(pb0 + ks * 1024, pb1 + ks * 1024);
        if (blk) {
#pragma unroll
          for (int j = 0; j < 8; ++j) bfr[j] = (short)0x3f80;
        }
        a2 = MFMA(af, bfr, a2);
      }
      char* p1w = ws + WS_P1 + (((size_t)(ng * 16 + wr * 8) * 16 + mt) * 1024) + lane * 8;
#pragma unroll
      for (int i = 0; i < 16; ++i) st_gran(p1w + (size_t)(i >> 1) * 16384 + (i & 1) * 512, __float_as_uint(a2[i]), tag);
    }
    epi_g(acc, sg);
    PHASE_END(3);
    mfma_tile(acc, xf, tr0, true, wK - 1024 + rowA, 16 * 3072, voffA, ldd + 3 * SLOT);
    WAITV(8);
    PHASE_END(4);
    epi_u(acc, sg, hdst);
    PHASE_END(5);
    if (full) mfma_tile(acc, xf, tr0 + 2 * SLOT, false, nullptr, 0, 0, 0);
    WAITV(0);
    PHASE_END(6);
    if (full) epi_g(acc, sg);
    PHASE_END(7);
    if (full) mfma_tile(acc, xf, tr0, false, nullptr, 0, 0, 0);
    STAMP(18);
    if (full) epi_u(acc, sg, hdst + 4096);
    STAMP(20);
  } else {
    WAITV(4);
    PHASE_END(0);
    mfma_tile(acc, xf, tr0 + SLOT, true, wU + rowF, 16 * 2752, voffF, ldd);
    WAITV(8);
    PHASE_END(1);
    {
      const unsigned vi = lds0 + LDS_SCR + 16384 + row * 64;
#pragma unroll
      for (int a = 0; a < 4; ++a) {
        u32x2 t; t[0] = cvtpk(acc[4 * a], acc[4 * a + 1]); t[1] = cvtpk(acc[4 * a + 2], acc[4 * a + 3]);
        *(LAS u32x2*)(uintptr_t)(vi + 8 * ((2 * a + h) ^ sw)) = t;
      }
    }
    PHASE_END(2);
    mfma_tile(acc, xf, tr0 + 3 * SLOT, full, wG + 128 + rowF, 16 * 2752, voffF, ldd + 2 * SLOT);
    if (full) WAITV(8); else WAITV(0);
    PHASE_END(3);
    epi_g(acc, sg);
    PHASE_END(4);
    mfma_tile(acc, xf, tr0 + SLOT, full, wU + 128 + rowF, 16 * 2752, voffF, ldd);
    if (full) WAITV(8); else WAITV(0);
    PHASE_END(5);
    {
      u64 g1[8];
      const char* p1r = ws + WS_P1 + ((size_t)(ng * 16 + mt) * 16 + h * 8) * 1024 + blk * 512 + (wr * 16 + i16) * 8;
#pragma unroll
      for (int j = 0; j < 8; ++j) g1[j] = ld_gran(p1r + j * 1024);
      SB0();
      epi_u(acc, sg, hdst + 2048);
      SB0();
      for (unsigned spin = 0; spin < SPIN_MAX; ++spin) {
        bool ok = true;
#pragma unroll
        for (int j = 0; j < 8; ++j) ok &= (unsigned)(g1[j] >> 32) == tag;
        if (__all(ok)) break;
        __builtin_amdgcn_s_sleep(4);
#pragma unroll
        for (int j = 0; j < 8; ++j) g1[j] = ld_gran(p1r + j * 1024);
      }
      float a = 0.f;
#pragma unroll
      for (int j = 0; j < 8; ++j) a += __uint_as_float((unsigned)g1[j]);
      a = rsum32(a);
      auto rr = __builtin_amdgcn_permlane16_swap(__float_as_uint(a), __float_as_uint(a), false, false);
      if (lane < 16) st_gran(ws + WS_P2 + (size_t)(ng * 16 + mt) * 512 + (wr * 16 + lane) * 8, cvtpk(__uint_as_float(rr[0]), __uint_as_float(rr[1])), tag);
      if (mt == 0 && wr == 0 && lane == 0) __hip_atomic_store((GAS unsigned*)(ws + WS_EP + ng * 128), tag, __ATOMIC_RELAXED, __HIP_MEMORY_SCOPE_AGENT);
    }
    PHASE_END(6);
    mfma_tile(acc, xf, tr0 + 3 * SLOT, false, nullptr, 0, 0, 0);
    WAITV(0);
    PHASE_END(7);
    bf16x8 fp[2], fm[2];
    feat_map(acc, wf, r32, fp, fm);
    STAMP(18);
    const char* p2r = ws + WS_P2 + (size_t)(ng * 16) * 512 + lane * 8;
    u64 g2[16];
    for (unsigned spin = 0; spin < SPIN_MAX; ++spin) {
      bool ok = true;
#pragma unroll
      for (int j = 0; j < 16; ++j) g2[j] = ld_gran(p2r + j * 512);
#pragma unroll
      for (int j = 0; j < 16; ++j) ok &= (unsigned)(g2[j] >> 32) == tag;
      if (__all(ok)) break;
      __builtin_amdgcn_s_sleep(4);
    }
    STAMP(19);
#pragma unroll
    for (int s = 0; s < 2; ++s) {
      const u32x4 f0 = {(unsigned)g2[8 * s], (unsigned)g2[8 * s + 1], (unsigned)g2[8 * s + 2], (unsigned)g2[8 * s + 3]};
      const u32x4 f1 = {(unsigned)g2[8 * s + 4], (unsigned)g2[8 * s + 5], (unsigned)g2[8 * s + 6], (unsigned)g2[8 * s + 7]};
      f32x16 o;
#pragma unroll
      for (int q = 0; q < 16; ++q) o[q] = 0.f;
      o = MFMA(__builtin_bit_cast(bf16x8, f0), fp[s], o);
      o = MFMA(__builtin_bit_cast(bf16x8, f1), fm[s], o);
      const float rd = __builtin_amdgcn_rcpf(bf16r(bf16r(o[8]) + 1e-6f));
      float ov[8];
#pragma unroll
      for (int j = 0; j < 8; j += 2) { const unsigned u = cvtpk(o[j], o[j + 1]); ov[j] = bflo(u) * rd; ov[j + 1] = bfhi(u) * rd; }
      *(bf16x8*)(ws + WS_AB + ((size_t)mblk * KS_W + 2 * ng + s) * 1024 + lane * 16) = pack8(ov);
    }
    STAMP(20);
  }
  STAMPR(31);
  STAMP_FLUSH(0);
}

constexpr int XROW = 68;
__global__ __launch_bounds__(256, 1) void k2_kernel(const u16* __restrict__ x, const char* __restrict__ ws, u16* __restrict__ out) {
  __shared__ __attribute__((aligned(16))) float xch[4 * 64 * XROW];
  const int tid = threadIdx.x, lane = tid & 63;
  const int wave = __builtin_amdgcn_readfirstlane(tid >> 6);
  const int b = blockIdx.x, xcd = b & 7, idx = b >> 3;
  const int mt = xcd * 4 + (idx & 3), nt = idx >> 2;
  const int r32 = lane & 31, h = lane >> 5;
  STAMP(0); STAMPR(30);
  f32x16 acc[2][2];
#pragma unroll
  for (int n = 0; n < 2; ++n)
#pragma unroll
    for (int m = 0; m < 2; ++m)
#pragma unroll
      for (int i = 0; i < 16; ++i) acc[n][m][i] = 0.f;
  const int k0 = wave == 0 ? 0 : 32 + (wave - 1) * 29;
  const int nk = wave == 0 ? 32 : (wave == 3 ? 28 : 29);
  const char* wa = ws + WS_WT + ((size_t)(2 * nt) * KS_W + k0) * 1024 + lane * 16;
  const char* wb = wa + (size_t)KS_W * 1024;
  const char* ha = ws + WS_AB + ((size_t)(2 * mt) * KS_W + k0) * 1024 + lane * 16;
  const char* hb = ha + (size_t)KS_W * 1024;
  constexpr int P = 14;
  bf16x8 bufs[P][4];
  auto ld = [&](int i, bf16x8* d) {
    d[0] = *(const bf16x8*)(ha + i * 1024); d[1] = *(const bf16x8*)(hb + i * 1024);
    d[2] = *(const bf16x8*)(wa + i * 1024); d[3] = *(const bf16x8*)(wb + i * 1024);
  };
#pragma unroll
  for (int i = 0; i < P; ++i) ld(i, bufs[i]);
  const int erow = mt * 64 + (tid >> 2), ecol = nt * 64 + (tid & 3) * 16;
  const u32x4 xa = *(const u32x4*)(x + (size_t)erow * D_ + ecol), xb = *(const u32x4*)(x + (size_t)erow * D_ + ecol + 8);
  SB0();
#pragma unroll
  for (int i = 0; i < 28; ++i) {
    bf16x8* d = bufs[i % P];
    acc[0][0] = MFMA(d[2], d[0], acc[0][0]); acc[0][1] = MFMA(d[2], d[1], acc[0][1]);
    acc[1][0] = MFMA(d[3], d[0], acc[1][0]); acc[1][1] = MFMA(d[3], d[1], acc[1][1]);
    SB0();
    if (i + P < 28) ld(i + P, bufs[i % P]);
    else if (i + P < 32) { if (i + P < nk) ld(i + P, bufs[i % P]); }
    SB0();
  }
#pragma unroll
  for (int i = 28; i < 32; ++i) {
    if (i < nk) {
      bf16x8* d = bufs[i % P];
      acc[0][0] = MFMA(d[2], d[0], acc[0][0]); acc[0][1] = MFMA(d[2], d[1], acc[0][1]);
      acc[1][0] = MFMA(d[3], d[0], acc[1][0]); acc[1][1] = MFMA(d[3], d[1], acc[1][1]);
    }
  }
  STAMP(1);
  float* mine = xch + wave * 64 * XROW;
#pragma unroll
  for (int n = 0; n < 2; ++n)
#pragma unroll
    for (int m = 0; m < 2; ++m)
#pragma unroll
      for (int a = 0; a < 4; ++a) {
        f32x4 t = {acc[n][m][4 * a], acc[n][m][4 * a + 1], acc[n][m][4 * a + 2], acc[n][m][4 * a + 3]};
        *(f32x4*)(mine + (32 * m + r32) * XROW + 32 * n + 8 * a + 4 * h) = t;
      }
  __syncthreads();
  STAMP(2);
  const float* rp = xch + (tid >> 2) * XROW + (tid & 3) * 16;
  u32x4 res[2];
#pragma unroll
  for (int q = 0; q < 4; ++q) {
    const f32x4 s0 = *(const f32x4*)(rp + 4 * q), o0 = *(const f32x4*)(rp + 64 * XROW + 4 * q);
    const f32x4 o1 = *(const f32x4*)(rp + 128 * XROW + 4 * q), o2 = *(const f32x4*)(rp + 192 * XROW + 4 * q);
#pragma unroll
    for (int e = 0; e < 4; e += 2) {
      const unsigned su = cvtpk(s0[e], s0[e + 1]);
      const unsigned ou = cvtpk((o0[e] + o1[e]) + o2[e], (o0[e + 1] + o1[e + 1]) + o2[e + 1]);
      const unsigned gu = cvtpk(silu_f(bflo(ou)), silu_f(bfhi(ou)));
      const unsigned tu = cvtpk(bflo(su) * bflo(gu), bfhi(su) * bfhi(gu));
      const unsigned xw = (q < 2 ? xa : xb)[(q & 1) * 2 + (e >> 1)];
      res[q >> 1][(q & 1) * 2 + (e >> 1)] = cvtpk(bflo(xw) + bflo(tu), bfhi(xw) + bfhi(tu));
    }
  }
  *(u32x4*)(out + (size_t)erow * D_ + ecol) = res[0];
  *(u32x4*)(out + (size_t)erow * D_ + ecol + 8) = res[1];
  STAMP(3); STAMPR(31);
}

extern "C" void kernel_launch(void* const* d_in, const int* in_sizes, int n_in, void* d_out, int out_size, void* d_ws, size_t ws_size,
                              hipStream_t stream) {
  const u16* x = (const u16*)d_in[0];
  const u16* wqkv = (const u16*)d_in[1];
  const u16* wphi = (const u16*)d_in[2];
  const u16* wo = (const u16*)d_in[3];
  const u16* w1 = (const u16*)d_in[5];
  const u16* w3 = (const u16*)d_in[6];
  const u16* w2 = (const u16*)d_in[7];
  char* ws = (char*)d_ws;
  k1_kernel<<<256, 512, 0, stream>>>(x, wqkv, wphi, wo, w1, w3, w2, ws);
  k2_kernel<<<256, 256, 0, stream>>>(x, ws, (u16*)d_out);
}
```

```cpp
#include <hip/hip_runtime.h>
#include <hip/hip_bf16.h>
#include <stdint.h>

typedef __attribute__((ext_vector_type(8))) short bf16x8;
typedef __attribute__((ext_vector_type(4))) short s16x4;
typedef __attribute__((ext_vector_type(16))) float f32x16;
typedef __attribute__((ext_vector_type(4))) float f32x4;
typedef __attribute__((ext_vector_type(4))) unsigned u32x4;
typedef __attribute__((ext_vector_type(2))) unsigned u32x2;
typedef unsigned short u16;
#define LAS __attribute__((address_space(3)))
#define DI __device__ __forceinline__
#define MFMA(a, b, c) __builtin_amdgcn_mfma_f32_32x32x16_bf16((a), (b), (c), 0, 0, 0)

constexpr int S_ = 2048, D_ = 512, NH = 32, F_ = 1376;
constexpr int KS_F = F_ / 16;
constexpr int KS_W = 32 + KS_F;
constexpr size_t WS_AB = 0;
constexpr size_t WS_WT = WS_AB + (size_t)64 * KS_W * 1024;
constexpr size_t WS_P1 = WS_WT + (size_t)16 * KS_W * 1024;
constexpr size_t WS_P2 = WS_P1 + (size_t)16 * 16 * 16 * 1024;
constexpr size_t WS_EP = WS_P2 + (size_t)16 * 16 * 512;
constexpr size_t WS_DUMP = WS_EP + 16 * 128;
constexpr size_t WS_END = WS_DUMP + 2048;

typedef __attribute__((ext_vector_type(2))) __bf16 bf16x2_t;
typedef __attribute__((ext_vector_type(2))) float f32x2;
DI unsigned cvtpk(float lo, float hi) { f32x2 v = {lo, hi}; return __builtin_bit_cast(unsigned, __builtin_convertvector(v, bf16x2_t)); }
DI float bflo(unsigned u) { return __uint_as_float(u << 16); }
DI float bfhi(unsigned u) { return __uint_as_float(u & 0xffff0000u); }
DI float bf16r(float x) { return bflo(cvtpk(x, x)); }
DI void glds16(const void* g, unsigned lds_base) {
  unsigned sv;
  asm volatile("s_mov_b32 %0, m0\n\ts_mov_b32 m0, %2\n\ts_nop 0\n\tglobal_load_lds_dwordx4 %1, off\n\ts_mov_b32 m0, %0"
               : "=&s"(sv) : "v"(g), "s"(lds_base) : "memory");
}
typedef short v4i16_t __attribute__((ext_vector_type(4)));
DI s16x4 trrd(unsigned a) { return __builtin_bit_cast(s16x4, __builtin_amdgcn_ds_read_tr16_b64_v4i16((LAS v4i16_t*)(uintptr_t)a)); }
DI bf16x8 trfrag(unsigned lo, unsigned hi) { s16x4 a = trrd(lo), b = trrd(hi); return __builtin_shufflevector(a, b, 0, 1, 2, 3, 4, 5, 6, 7); }
DI bf16x8 pack8(const float* v) {
  u32x4 p; p[0] = cvtpk(v[0], v[1]); p[1] = cvtpk(v[2], v[3]); p[2] = cvtpk(v[4], v[5]); p[3] = cvtpk(v[6], v[7]);
  return __builtin_bit_cast(bf16x8, p);
}
DI float xhalf(float v) {
  auto rr = __builtin_amdgcn_permlane32_swap(__float_as_uint(v), __float_as_uint(v), false, false);
  unsigned a = rr[0], b = rr[1];
  return __uint_as_float((threadIdx.x & 32) ? a : b);
}
template <int C> DI float dppror(float v) { return __uint_as_float(__builtin_amdgcn_update_dpp(0u, __float_as_uint(v), 0x120 + C, 0xf, 0xf, false)); }
DI float row16sum(float v) { v += dppror<8>(v); v += dppror<4>(v); v += dppror<2>(v); v += dppror<1>(v); return v; }
DI float silu_f(float x) { return x * __builtin_amdgcn_rcpf(1.0f + __builtin_amdgcn_exp2f(-1.4426950408889634f * x)); }
#define STAMP(i)
#define STAMPR(i)
#define STAMP_FLUSH(base)
#define BAR() asm volatile("s_waitcnt lgkmcnt(0)\n\ts_barrier" ::: "memory")
#define WAITV0() asm volatile("s_waitcnt vmcnt(0)" ::: "memory")

enum { T_NONE = 0, T_Q = 1, T_K = 2, T_V = 3, T_G = 4, T_U = 5 };
#define TD(ty, ix) ((unsigned long long)(((ty) << 6) | (ix)))
#define SB0() __builtin_amdgcn_sched_barrier(0)
#define GAS __attribute__((address_space(1)))
typedef unsigned long long u64;
DI float dot2(unsigned a, unsigned b, float c) {
  return __builtin_amdgcn_fdot2_f32_bf16(__builtin_bit_cast(bf16x2_t, a), __builtin_bit_cast(bf16x2_t, b), c, false);
}
DI void st_gran(void* p, unsigned val, unsigned tag) { __hip_atomic_store((GAS u64*)p, ((u64)tag << 32) | val, __ATOMIC_RELAXED, __HIP_MEMORY_SCOPE_AGENT); }
DI u64 ld_gran(const void* p) { return __hip_atomic_load((GAS u64*)p, __ATOMIC_RELAXED, __HIP_MEMORY_SCOPE_AGENT); }
constexpr unsigned SPIN_MAX = 1u << 16;

constexpr int SLOT = 32768;
constexpr int LDS_SCR = 4 * SLOT;
constexpr int LDS_DBG = LDS_SCR + 28672;
constexpr int K1_LDS = 5 * SLOT;

DI void glds16s(unsigned voff, const void* sbase, unsigned lds_base) {
  unsigned sv;
  asm volatile("s_mov_b32 %0, m0\n\ts_mov_b32 m0, %3\n\ts_nop 0\n\tglobal_load_lds_dwordx4 %1, %2\n\ts_mov_b32 m0, %0"
               : "=&s"(sv) : "v"(voff), "s"(sbase), "s"(lds_base) : "memory");
}
DI void center4(u32x4& a, float c) {
  unsigned t0, t1, t2, t3, t4, t5, t6, t7;
  asm("v_dot2_f32_bf16 %0, %8, %12, %14\n\tv_dot2_f32_bf16 %1, %8, %13, %14\n\tv_dot2_f32_bf16 %2, %9, %12, %14\n\tv_dot2_f32_bf16 %3, %9, %13, %14\n\t"
      "v_dot2_f32_bf16 %4, %10, %12, %14\n\tv_dot2_f32_bf16 %5, %10, %13, %14\n\tv_dot2_f32_bf16 %6, %11, %12, %14\n\tv_dot2_f32_bf16 %7, %11, %13, %14\n\t"
      "v_cvt_pk_bf16_f32 %0, %0, %1\n\tv_cvt_pk_bf16_f32 %2, %2, %3\n\tv_cvt_pk_bf16_f32 %4, %4, %5\n\tv_cvt_pk_bf16_f32 %6, %6, %7"
      : "=&v"(t0), "=&v"(t1), "=&v"(t2), "=&v"(t3), "=&v"(t4), "=&v"(t5), "=&v"(t6), "=&v"(t7)
      : "v"(a[0]), "v"(a[1]), "v"(a[2]), "v"(a[3]), "s"(0x00003f80u), "s"(0x3f800000u), "v"(c));
  a[0] = t0; a[1] = t2; a[2] = t4; a[3] = t6;
}
DI float rsum32(float v) { auto rr = __builtin_amdgcn_permlane32_swap(__float_as_uint(v), __float_as_uint(v), false, false); return __uint_as_float(rr[0]) + __uint_as_float(rr[1]); }
DI float rsum16(float v) { auto rr = __builtin_amdgcn_permlane16_swap(__float_as_uint(v), __float_as_uint(v), false, false); return __uint_as_float(rr[0]) + __uint_as_float(rr[1]); }
DI float rmax32(float v) { auto rr = __builtin_amdgcn_permlane32_swap(__float_as_uint(v), __float_as_uint(v), false, false); return fmaxf(__uint_as_float(rr[0]), __uint_as_float(rr[1])); }
DI float rmin32(float v) { auto rr = __builtin_amdgcn_permlane32_swap(__float_as_uint(v), __float_as_uint(v), false, false); return fminf(__uint_as_float(rr[0]), __uint_as_float(rr[1])); }
DI float nv(float x) { asm("" : "+v"(x)); return x; }
#define WAITV(n) asm volatile("s_waitcnt vmcnt(" #n ")" ::: "memory")

#define PRIO_M 2
#define LDSP 4
#define PRIO_E 0
template <int RB, class F>
DI void mfma_tile(f32x16& acc, const bf16x8 (&xf)[32], unsigned sb, bool ld, const char* g, unsigned gstep, unsigned voff, unsigned dst, F filler) {
  __builtin_amdgcn_s_setprio(PRIO_M);
  bf16x8 wq[4];
#pragma unroll
  for (int ks = 0; ks < 4; ++ks) wq[ks] = trfrag(sb + ks * 8 * RB, sb + ks * 8 * RB + 4 * RB);
#pragma unroll
  for (int i = 0; i < 16; ++i) acc[i] = 0.f;
  SB0();
#pragma unroll
  for (int ks = 0; ks < 32; ++ks) {
    acc = MFMA(wq[ks & 3], xf[ks], acc);
    if (ks + 4 < 32) { const int o = (((ks + 4) >> 2) * 64 + ((ks + 4) & 3) * 8) * RB; wq[ks & 3] = trfrag(sb + o, sb + o + 4 * RB); }
    if ((ks % LDSP) == 0 && ks < 8 * LDSP) { if (ld) glds16s(voff, g + (size_t)(ks / LDSP) * gstep, dst + (ks / LDSP) * 1024); }
    filler(ks);
    SB0();
  }
  __builtin_amdgcn_s_setprio(PRIO_E);
  asm volatile("" : "+v"(acc));
}
DI void feat_proj(const f32x16& acc, const u32x4& wf, int r32, unsigned (&pp)[8]) {
  float av[16];
#pragma unroll
  for (int i = 0; i < 16; ++i) av[i] = acc[i];
  const bf16x8 qb0 = pack8(av), qb1 = pack8(av + 8);
  const u32x4 z4 = {0u, 0u, 0u, 0u};
  const bf16x8 a0 = __builtin_bit_cast(bf16x8, (r32 < 16) ? wf : z4), a1 = __builtin_bit_cast(bf16x8, (r32 < 16) ? z4 : wf);
  f32x16 pq;
#pragma unroll
  for (int i = 0; i < 16; ++i) pq[i] = 0.f;
  SB0();
  __builtin_amdgcn_s_setprio(3);
  pq = MFMA(a0, qb0, pq);
  pq = MFMA(a1, qb1, pq);
  SB0();
  __builtin_amdgcn_s_setprio(PRIO_E);
#pragma unroll
  for (int j = 0; j < 8; ++j) pp[j] = cvtpk(pq[2 * j], pq[2 * j + 1]);
  asm volatile("" : "+v"(pp[0]), "+v"(pp[1]), "+v"(pp[2]), "+v"(pp[3]), "+v"(pp[4]), "+v"(pp[5]), "+v"(pp[6]), "+v"(pp[7]));
}
DI void feat_soft(const unsigned* pp, bf16x8& fp, bf16x8& fm) {
  float pv[8];
#pragma unroll
  for (int j = 0; j < 4; ++j) { pv[2 * j] = bflo(pp[j]); pv[2 * j + 1] = bfhi(pp[j]); }
  float mx = fmaxf(fmaxf(fmaxf(pv[0], pv[1]), pv[2]), fmaxf(fmaxf(pv[3], pv[4]), pv[5])), mn = fminf(fminf(fminf(pv[0], pv[1]), pv[2]), fminf(fminf(pv[3], pv[4]), pv[5]));
  mx = fmaxf(fmaxf(mx, pv[6]), pv[7]); mn = fminf(fminf(mn, pv[6]), pv[7]);
  mx = rmax32(mx); mn = rmin32(mn);
  const float L2E = 1.4426950408889634f, mxl = nv(-mx * L2E), mnl = nv(mn * L2E);
  float ep[8], em[8], sp = 0.f, sm = 0.f;
#pragma unroll
  for (int j = 0; j < 8; ++j) {
    ep[j] = __builtin_amdgcn_exp2f(nv(fmaf(pv[j], L2E, mxl))); sp = nv(sp + ep[j]);
    em[j] = __builtin_amdgcn_exp2f(nv(fmaf(pv[j], -L2E, mnl))); sm = nv(sm + em[j]);
  }
  sp = nv(rsum32(sp)); sm = nv(rsum32(sm));
  const float rp = __builtin_amdgcn_rcpf(sp), rm = __builtin_amdgcn_rcpf(sm);
#pragma unroll
  for (int j = 0; j < 8; ++j) { ep[j] = nv(ep[j] * rp); em[j] = nv(em[j] * rm); }
  fp = pack8(ep); fm = pack8(em);
  asm volatile("" : "+v"(fp), "+v"(fm));
}
DI void epi_g(const f32x16& acc, unsigned (&sg)[8]) {
#pragma unroll
  for (int i = 0; i < 16; i += 2) {
    const unsigned u = cvtpk(acc[i], acc[i + 1]);
    sg[i >> 1] = cvtpk(nv(silu_f(bflo(u))), nv(silu_f(bfhi(u))));
  }
  asm volatile("" : "+v"(sg[0]), "+v"(sg[1]), "+v"(sg[2]), "+v"(sg[3]), "+v"(sg[4]), "+v"(sg[5]), "+v"(sg[6]), "+v"(sg[7]));
}
DI void epi_u(const f32x16& acc, const unsigned (&sg)[8], char* dst) {
  float hv[16];
#pragma unroll
  for (int i = 0; i < 16; i += 2) { const unsigned u = cvtpk(acc[i], acc[i + 1]); hv[i] = nv(bflo(sg[i >> 1]) * bflo(u)); hv[i + 1] = nv(bfhi(sg[i >> 1]) * bfhi(u)); }
  *(bf16x8*)dst = pack8(hv); *(bf16x8*)(dst + 1024) = pack8(hv + 8);
}
#define PHASE_END(p) do { STAMP(2 + 2 * (p)); SB0(); BAR(); SB0(); STAMP(3 + 2 * (p)); } while (0)

__global__ __launch_bounds__(512, 2) void k1_kernel(const u16* __restrict__ x, const u16* __restrict__ wqkv, const u16* __restrict__ wphi,
                                                    const u16* __restrict__ wo, const u16* __restrict__ w1, const u16* __restrict__ w3,
                                                    const u16* __restrict__ w2, char* __restrict__ ws) {
  __shared__ __attribute__((aligned(16))) char smem[K1_LDS];
  const int tid = threadIdx.x, lane = tid & 63;
  const int wave = __builtin_amdgcn_readfirstlane(tid >> 6), grp = wave >> 2, wr = wave & 3;
  const int b = blockIdx.x, xcd = b & 7, idx = b >> 3;
  const int mt = (xcd & 1) * 8 + (idx & 7), ng = (xcd >> 1) * 4 + (idx >> 3);
  const int r32 = lane & 31, h = lane >> 5;
  const unsigned lds0 = (unsigned)(uintptr_t)smem;
  unsigned dummy0 = 0;
  if (wave == 0) {
    unsigned long long pc; unsigned len;
    asm volatile("s_getpc_b64 %0\n.Lk1_pc:\n\ts_mov_b32 %1, .Lk1_end-.Lk1_pc" : "=s"(pc), "=s"(len));
    const unsigned off = (lane * 8 + (idx & 7)) * 128;
    if (off < len) asm volatile("global_load_dword %0, %1, %2" : "+v"(dummy0) : "v"(off), "s"(pc) : "memory");
  }
  unsigned dummy1 = 0;
  if (grp == 0) asm volatile("global_load_dword %0, %1, %2" : "+v"(dummy1) : "v"(lane * 128), "s"((const char*)(x + (size_t)(mt * 128 + (idx >> 3) * 32 + wr * 8) * D_)) : "memory");
  const unsigned reg = lds0 + SLOT + (wr * 2 + grp) * 16384;
  {
    const char* xb = (const char*)(x + (size_t)(mt * 128 + wr * 32 + grp * 16) * D_);
    const int rr = lane >> 5, qd = (lane >> 3) & 3, pos = lane & 7;
#pragma unroll
    for (int cc = 0; cc < 2; ++cc)
#pragma unroll
      for (int i = 0; i < 8; ++i)
        glds16s(rr * 1024 + qd * 256 + ((pos ^ (2 * (i & 3) + rr)) << 4), xb + i * 2048 + cc * 128, reg + cc * 8192 + i * 1024);
  }
  const int g0 = ng < 11 ? 3 * ng : 33 + 2 * (ng - 11);
  const bool full = ng < 11;
  const char* wK = (const char*)(wqkv + 512 + 32 * ng);
  const char* wG = (const char*)(w1 + 32 * g0);
  const char* wU = (const char*)(w3 + 32 * g0);
  const unsigned voffA = (lane >> 2) * 3072 + (lane & 3) * 16, voffF = (lane >> 2) * 2752 + (lane & 3) * 16;
  unsigned dummy = 0, dummy2 = 0, tagv;
  u32x4 wf, wfb;
  {
    tagv = __hip_atomic_load((GAS unsigned*)(ws + WS_EP + ng * 128), __ATOMIC_RELAXED, __HIP_MEMORY_SCOPE_AGENT);
    const u16* wp = wphi + (size_t)(2 * ng + (r32 >> 4)) * 256 + (4 * h) * 16 + (lane & 15);
#pragma unroll
    for (int j = 0; j < 4; ++j) {
      const int k0 = 8 * (j >> 1) + 2 * (j & 1);
      asm volatile("global_load_ushort %0, %2, off offset:%3\n\tglobal_load_ushort %1, %2, off offset:%4" : "=&v"(wf[j]), "=&v"(wfb[j]) : "v"(wp), "n"(k0 * 32), "n"(k0 * 32 + 32) : "memory");
    }
    if (grp) {
      const char* g = wK + (size_t)wr * 128 * 3072;
#pragma unroll
      for (int i = 0; i < 8; ++i) glds16s(voffA, g + i * 16 * 3072, lds0 + wr * 8192 + i * 1024);
      const int trow = (idx & 7) * 64 + lane;
      const int e = (wr == 3 && !full) ? 0 : wr - 1;
      const char* p = wr == 0 ? wK + 1024 + (size_t)trow * 3072 : wG + 64 * e + (size_t)trow * 2752;
      const char* p2 = wr == 0 ? wK - 1024 + (size_t)trow * 3072 : wU + 64 * e + (size_t)trow * 2752;
      asm volatile("global_load_dword %0, %1, off" : "+v"(dummy) : "v"(p) : "memory");
      asm volatile("global_load_dword %0, %1, off" : "+v"(dummy2) : "v"(p2) : "memory");
    }
  }
  bf16x8 xf[32];
  unsigned tag;
  {
    const int r16 = lane & 15, qd = lane >> 4;
    const unsigned rb = reg + (r16 >> 1) * 1024 + (r16 & 1) * 512 + qd * 128 + ((r16 & 7) << 4);
    u32x4 xr[16];
    float s0 = 0.f, s1 = 0.f, q0 = 0.f, q1 = 0.f;
    if (grp) WAITV(27); else WAITV(17);
#pragma unroll
    for (int q = 0; q < 8; ++q) xr[q] = *(const LAS u32x4*)(uintptr_t)(rb ^ (q << 4));
#pragma unroll
    for (int q = 0; q < 8; ++q)
#pragma unroll
      for (int e = 0; e < 4; e += 2) {
        s0 = dot2(xr[q][e], 0x3f803f80u, s0); s1 = dot2(xr[q][e + 1], 0x3f803f80u, s1);
        q0 = dot2(xr[q][e], xr[q][e], q0); q1 = dot2(xr[q][e + 1], xr[q][e + 1], q1);
      }
    asm volatile("" : "+v"(s0), "+v"(s1), "+v"(q0), "+v"(q1));
    if (grp) WAITV(19); else WAITV(9);
#pragma unroll
    for (int q = 0; q < 8; ++q) xr[8 + q] = *(const LAS u32x4*)(uintptr_t)((rb ^ (q << 4)) + 8192);
#pragma unroll
    for (int q = 8; q < 16; ++q)
#pragma unroll
      for (int e = 0; e < 4; e += 2) {
        s0 = dot2(xr[q][e], 0x3f803f80u, s0); s1 = dot2(xr[q][e + 1], 0x3f803f80u, s1);
        q0 = dot2(xr[q][e], xr[q][e], q0); q1 = dot2(xr[q][e + 1], xr[q][e + 1], q1);
      }
    const float mean = rsum32(rsum16(s0 + s1)) * (1.0f / 512.0f);
    const float var = fmaxf(rsum32(rsum16(q0 + q1)) * (1.0f / 512.0f) - mean * mean, 0.f);
    const float nmb = -bf16r(mean);
    const float rs = 1.0f / bf16r(sqrtf(bf16r(bf16r(var) + 1e-5f)));
    const unsigned wb = reg + qd * 4096 + r16 * 16;
#pragma unroll
    for (int q = 0; q < 16; ++q) {
      u32x4 o;
      center4(xr[q], nmb);
#pragma unroll
      for (int e = 0; e < 4; ++e) o[e] = cvtpk(bflo(xr[q][e]) * rs, bfhi(xr[q][e]) * rs);
      *(LAS u32x4*)(uintptr_t)(wb + (q >> 3) * 2048 + (q & 3) * 512 + ((q >> 2) & 1) * 256) = o;
    }
    SB0();
    WAITV(0);
    asm volatile("" : "+v"(dummy), "+v"(dummy0), "+v"(dummy1), "+v"(dummy2), "+v"(wf), "+v"(wfb));
#pragma unroll
    for (int j = 0; j < 4; ++j) wf[j] |= wfb[j] << 16;
    tag = __builtin_amdgcn_readfirstlane(tagv) + 1u;
    asm volatile("" : "+s"(tag));
    BAR();
    const unsigned xb = lds0 + SLOT * (1 + wr) + (r32 >> 4) * 16384 + (h * 16 + (r32 & 15)) * 16;
#pragma unroll
    for (int ks = 0; ks < 32; ++ks) xf[ks] = *(const LAS bf16x8*)(uintptr_t)(xb + ks * 512);
    BAR();
  }
  STAMP(1);
  const unsigned voffP = (lane >> 3) * 2752 + (((lane & 7) ^ ((lane >> 2) & 4)) << 4);
  const size_t rowP = (size_t)wave * 64 * 2752;
  {
    const char* g = wK + 1024 + (size_t)wave * 64 * 3072;
#pragma unroll
    for (int i = 0; i < 4; ++i) glds16s(voffA, g + i * 16 * 3072, lds0 + SLOT + wave * 4096 + i * 1024);
#pragma unroll
    for (int i = 0; i < 8; ++i) glds16s(voffP, wG + rowP + i * 8 * 2752, lds0 + 2 * SLOT + wave * 8192 + i * 1024);
  }
  const int i16 = lane & 15, blk = (lane >> 4) & 1;
  const unsigned tr0 = lds0 + (32 * h + (i16 >> 2)) * 64 + 8 * (4 * blk + (i16 & 3));
  const unsigned trP = lds0 + (32 * h + (i16 >> 2)) * 128 + ((((grp ^ 1) ^ (i16 >> 3)) & 1) << 6) + 8 * (4 * blk + (i16 & 3));
  const int mblk = mt * 4 + wr;
  const unsigned ldd = lds0 + wr * 8192;
  const size_t rowF = (size_t)wr * 128 * 2752, rowA = (size_t)wr * 128 * 3072;
  char* const hdst = ws + WS_AB + ((size_t)mblk * KS_W + 32 + 2 * g0) * 1024 + lane * 16;
  unsigned sg[8], pp[8];
  f32x16 acc, acc2;
  const int row = wr * 32 + r32, sw = (row >> 1) & 7;
  auto nofill = [](int) {};
  if (grp == 0) {
    mfma_tile<64>(acc, xf, tr0, false, nullptr, 0, 0, 0, nofill);
    WAITV(8);
    PHASE_END(0);
    feat_proj(acc, wf, r32, pp);
#pragma unroll
    for (int s = 0; s < 2; ++s) {
      if (s) {
#pragma unroll
        for (int i = 0; i < 4; ++i) glds16s(voffP, wU + rowP + i * 8 * 2752, lds0 + wave * 8192 + i * 1024);
      }
      bf16x8 fp, fm;
      feat_soft(pp + 4 * s, fp, fm);
      if (s) {
#pragma unroll
        for (int i = 4; i < 8; ++i) glds16s(voffP, wU + rowP + i * 8 * 2752, lds0 + wave * 8192 + i * 1024);
      }
      const unsigned pk = lds0 + LDS_SCR + s * 8192 + row * 64;
      const u32x4 up = __builtin_bit_cast(u32x4, fp), um = __builtin_bit_cast(u32x4, fm);
      u32x2 t;
      t[0] = up[0]; t[1] = up[1]; *(LAS u32x2*)(uintptr_t)(pk + 8 * ((h) ^ sw)) = t;
      t[0] = up[2]; t[1] = up[3]; *(LAS u32x2*)(uintptr_t)(pk + 8 * ((2 + h) ^ sw)) = t;
      t[0] = um[0]; t[1] = um[1]; *(LAS u32x2*)(uintptr_t)(pk + 8 * ((4 + h) ^ sw)) = t;
      t[0] = um[2]; t[1] = um[3]; *(LAS u32x2*)(uintptr_t)(pk + 8 * ((6 + h) ^ sw)) = t;
      if (s == 0) { SB0(); WAITV(0); }
      PHASE_END(1 + s);
    }
    mfma_tile<128>(acc, xf, trP + 2 * SLOT, false, nullptr, 0, 0, 0, nofill);
    WAITV(0);
    PHASE_END(3);
    {
      if (full) {
#pragma unroll
        for (int i = 0; i < 8; ++i) glds16s(voffF, wG + 128 + rowF + i * 16 * 2752, ldd + 3 * SLOT + i * 1024);
      }
      epi_g(acc, sg);
    }
    PHASE_END(4);
    mfma_tile<128>(acc, xf, trP, false, nullptr, 0, 0, 0, nofill);
    PHASE_END(5);
    {
      if (full) {
#pragma unroll
        for (int i = 0; i < 8; ++i) glds16s(voffF, wU + 128 + rowF + i * 16 * 2752, ldd + i * 1024);
      }
      u64 g1[8];
      const char* p1r = ws + WS_P1 + ((size_t)(ng * 16 + mt) * 16 + h * 8) * 1024 + blk * 512 + (wr * 16 + i16) * 8;
#pragma unroll
      for (int j = 0; j < 8; ++j) g1[j] = ld_gran(p1r + j * 1024);
      SB0();
      epi_u(acc, sg, hdst + 2048);
      SB0();
      for (unsigned spin = 0; spin < SPIN_MAX; ++spin) {
        bool ok = true;
#pragma unroll
        for (int j = 0; j < 8; ++j) ok &= (unsigned)(g1[j] >> 32) == tag;
        if (__all(ok)) break;
        __builtin_amdgcn_s_sleep(4);
#pragma unroll
        for (int j = 0; j < 8; ++j) g1[j] = ld_gran(p1r + j * 1024);
      }
      float a = 0.f;
#pragma unroll
      for (int j = 0; j < 8; ++j) a = nv(a + __uint_as_float((unsigned)g1[j]));
      a = rsum32(a);
      auto rr = __builtin_amdgcn_permlane16_swap(__float_as_uint(a), __float_as_uint(a), false, false);
      WAITV(0);
      if (lane < 16) st_gran(ws + WS_P2 + (size_t)(ng * 16 + mt) * 512 + (wr * 16 + lane) * 8, cvtpk(__uint_as_float(rr[0]), __uint_as_float(rr[1])), tag);
      if (mt == 0 && wr == 0 && lane == 0) __hip_atomic_store((GAS unsigned*)(ws + WS_EP + ng * 128), tag, __ATOMIC_RELAXED, __HIP_MEMORY_SCOPE_AGENT);
    }
    PHASE_END(6);
    if (full) {
      mfma_tile<64>(acc, xf, tr0 + 3 * SLOT, false, nullptr, 0, 0, 0, nofill);
      STAMP(16);
      mfma_tile<64>(acc2, xf, tr0, false, nullptr, 0, 0, 0, nofill);
      STAMP(18);
      epi_g(acc, sg);
      epi_u(acc2, sg, hdst + 4096);
    }
    STAMP(20);
  } else {
    u32x4 rta[2], rtb[2];
    const int gw = b * 8 + wr * 2;
#pragma unroll
    for (int f = 0; f < 2; ++f) {
      const int gc = gw + f < 16 * KS_W ? gw + f : 0, nb = gc / KS_W, ks = gc - nb * KS_W;
      const u16* src = (ks < 32 ? wo + (size_t)ks * 16 * D_ : w2 + (size_t)(ks - 32) * 16 * D_) + nb * 32 + r32 + (size_t)(4 * h) * D_;
#pragma unroll
      for (int j = 0; j < 4; ++j) {
        const int k0 = 8 * (j >> 1) + 2 * (j & 1);
        asm volatile("global_load_ushort %0, %2, off\n\tglobal_load_ushort %1, %2, off offset:1024" : "=&v"(rta[f][j]), "=&v"(rtb[f][j]) : "v"(src + (size_t)k0 * D_) : "memory");
      }
    }
    WAITV(24);
    PHASE_END(0);
    mfma_tile<64>(acc2, xf, tr0 + SLOT, false, nullptr, 0, 0, 0, nofill);
    WAITV(16);
    PHASE_END(1);
    {
      const unsigned vi = lds0 + LDS_SCR + 16384 + row * 64;
      mfma_tile<128>(acc, xf, trP + 2 * SLOT, true, wU + rowP, 8 * 2752, voffP, lds0 + wave * 8192, [&](int ks) {
        if (ks >= 1 && ks <= 4) {
          const int a = ks - 1;
          u32x2 t; t[0] = cvtpk(acc2[4 * a], acc2[4 * a + 1]); t[1] = cvtpk(acc2[4 * a + 2], acc2[4 * a + 3]);
          *(LAS u32x2*)(uintptr_t)(vi + 8 * ((2 * a + h) ^ sw)) = t;
        }
      });
    }
    PHASE_END(2);
    if (wr < 2) {
      f32x16 a2;
#pragma unroll
      for (int i = 0; i < 16; ++i) a2[i] = 0.f;
      const int rowq = 8 * h + (i16 >> 2);
      const int ca = 4 * blk + (lane & 3), cb = 4 * (blk ^ wr) + (lane & 3);
      const int sw0 = (rowq >> 1) & 7, sw1 = ((rowq + 4) >> 1) & 7;
      const unsigned pk = lds0 + LDS_SCR + wr * 8192 + rowq * 64, vi = lds0 + LDS_SCR + 16384 + rowq * 64;
      const unsigned pa0 = pk + 8 * (ca ^ sw0), pa1 = pk + 256 + 8 * (ca ^ sw1), pb0 = vi + 8 * (cb ^ sw0), pb1 = vi + 256 + 8 * (cb ^ sw1);
      __builtin_amdgcn_s_setprio(3);
#pragma unroll
      for (int ks = 0; ks < 8; ++ks) {
        bf16x8 af = trfrag(pa0 + ks * 1024, pa1 + ks * 1024);
        bf16x8 bfr = trfrag(pb0 + ks * 1024, pb1 + ks * 1024);
        if (blk) {
#pragma unroll
          for (int j = 0; j < 8; ++j) bfr[j] = (short)0x3f80;
        }
        a2 = MFMA(af, bfr, a2);
      }
      asm volatile("" : "+v"(a2));
      __builtin_amdgcn_s_setprio(PRIO_E);
      char* p1w = ws + WS_P1 + (((size_t)(ng * 16 + wr * 8) * 16 + mt) * 1024) + lane * 8;
#pragma unroll
      for (int i = 0; i < 16; ++i) st_gran(p1w + (size_t)(i >> 1) * 16384 + (i & 1) * 512, __float_as_uint(a2[i]), tag);
      WAITV(24);
    } else {
      WAITV(8);
    }
    {
      asm volatile("" : "+v"(rta[0]), "+v"(rta[1]), "+v"(rtb[0]), "+v"(rtb[1]));
#pragma unroll
      for (int f = 0; f < 2; ++f) {
#pragma unroll
        for (int j = 0; j < 4; ++j) rta[f][j] |= rtb[f][j] << 16;
        *(u32x4*)(ws + (gw + f < 16 * KS_W ? WS_WT + (size_t)(gw + f) * 1024 : WS_DUMP + f * 1024) + lane * 16) = rta[f];
      }
    }
    epi_g(acc, sg);
    if (wr < 2) WAITV(18); else WAITV(2);
    PHASE_END(3);
    mfma_tile<128>(acc, xf, trP, true, wK - 1024 + rowA, 16 * 3072, voffA, ldd + 2 * SLOT, nofill);
    PHASE_END(4);
    epi_u(acc, sg, hdst);
    WAITV(2);
    PHASE_END(5);
    mfma_tile<64>(acc, xf, tr0 + 2 * SLOT, false, nullptr, 0, 0, 0, nofill);
    PHASE_END(6);
    bf16x8 fp[2], fm[2];
    feat_proj(acc, wf, r32, pp);
    feat_soft(pp, fp[0], fm[0]);
    STAMP(16);
    feat_soft(pp + 4, fp[1], fm[1]);
    STAMP(18);
    const char* p2r = ws + WS_P2 + (size_t)(ng * 16) * 512 + lane * 8;
    u64 g2[16];
    for (unsigned spin = 0; spin < SPIN_MAX; ++spin) {
      bool ok = true;
#pragma unroll
      for (int j = 0; j < 16; ++j) g2[j] = ld_gran(p2r + j * 512);
#pragma unroll
      for (int j = 0; j < 16; ++j) ok &= (unsigned)(g2[j] >> 32) == tag;
      if (__all(ok)) break;
      __builtin_amdgcn_s_sleep(4);
    }
    STAMP(19);
    __builtin_amdgcn_s_setprio(3);
#pragma unroll
    for (int s = 0; s < 2; ++s) {
      const u32x4 f0 = {(unsigned)g2[8 * s], (unsigned)g2[8 * s + 1], (unsigned)g2[8 * s + 2], (unsigned)g2[8 * s + 3]};
      const u32x4 f1 = {(unsigned)g2[8 * s + 4], (unsigned)g2[8 * s + 5], (unsigned)g2[8 * s + 6], (unsigned)g2[8 * s + 7]};
      f32x16 o;
#pragma unroll
      for (int q = 0; q < 16; ++q) o[q] = 0.f;
      o = MFMA(__builtin_bit_cast(bf16x8, f0), fp[s], o);
      o = MFMA(__builtin_bit_cast(bf16x8, f1), fm[s], o);
      const float rd = __builtin_amdgcn_rcpf(bf16r(bf16r(o[8]) + 1e-6f));
      float ov[8];
#pragma unroll
      for (int j = 0; j < 8; j += 2) { const unsigned u = cvtpk(o[j], o[j + 1]); ov[j] = nv(bflo(u) * rd); ov[j + 1] = nv(bfhi(u) * rd); }
      *(bf16x8*)(ws + WS_AB + ((size_t)mblk * KS_W + 2 * ng + s) * 1024 + lane * 16) = pack8(ov);
    }
    STAMP(20);
  }
  STAMPR(31);
  STAMP_FLUSH(0);
  asm volatile(".Lk1_end:");
}

constexpr int XROW = 68;
__global__ __launch_bounds__(256, 1) void k2_kernel(const u16* __restrict__ x, const char* __restrict__ ws, u16* __restrict__ out) {
  __shared__ __attribute__((aligned(16))) float xch[4 * 64 * XROW];
  const int tid = threadIdx.x, lane = tid & 63;
  const int wave = __builtin_amdgcn_readfirstlane(tid >> 6);
  const int b = blockIdx.x, xcd = b & 7, idx = b >> 3;
  const int mt = xcd * 4 + (idx & 3), nt = idx >> 2;
  const int r32 = lane & 31, h = lane >> 5;
  STAMP(0); STAMPR(30);
  f32x16 acc[2][2];
#pragma unroll
  for (int n = 0; n < 2; ++n)
#pragma unroll
    for (int m = 0; m < 2; ++m)
#pragma unroll
      for (int i = 0; i < 16; ++i) acc[n][m][i] = 0.f;
  const int k0 = wave == 0 ? 0 : 32 + (wave - 1) * 29;
  const int nk = wave == 0 ? 32 : (wave == 3 ? 28 : 29);
  const char* wa = ws + WS_WT + ((size_t)(2 * nt) * KS_W + k0) * 1024 + lane * 16;
  const char* wb = wa + (size_t)KS_W * 1024;
  const char* ha = ws + WS_AB + ((size_t)(2 * mt) * KS_W + k0) * 1024 + lane * 16;
  const char* hb = ha + (size_t)KS_W * 1024;
  constexpr int P = 14;
  bf16x8 bufs[P][4];
  auto ld = [&](int i, bf16x8* d) {
    d[0] = *(const bf16x8*)(ha + i * 1024); d[1] = *(const bf16x8*)(hb + i * 1024);
    d[2] = *(const bf16x8*)(wa + i * 1024); d[3] = *(const bf16x8*)(wb + i * 1024);
  };
#pragma unroll
  for (int i = 0; i < P; ++i) ld(i, bufs[i]);
  const int erow = mt * 64 + (tid >> 2), ecol = nt * 64 + (tid & 3) * 16;
  const u32x4 xa = *(const u32x4*)(x + (size_t)erow * D_ + ecol), xb = *(const u32x4*)(x + (size_t)erow * D_ + ecol + 8);
  SB0();
#pragma unroll
  for (int i = 0; i < 28; ++i) {
    bf16x8* d = bufs[i % P];
    acc[0][0] = MFMA(d[2], d[0], acc[0][0]); acc[0][1] = MFMA(d[2], d[1], acc[0][1]);
    acc[1][0] = MFMA(d[3], d[0], acc[1][0]); acc[1][1] = MFMA(d[3], d[1], acc[1][1]);
    SB0();
    if (i + P < 28) ld(i + P, bufs[i % P]);
    else if (i + P < 32) { if (i + P < nk) ld(i + P, bufs[i % P]); }
    SB0();
  }
#pragma unroll
  for (int i = 28; i < 32; ++i) {
    if (i < nk) {
      bf16x8* d = bufs[i % P];
      acc[0][0] = MFMA(d[2], d[0], acc[0][0]); acc[0][1] = MFMA(d[2], d[1], acc[0][1]);
      acc[1][0] = MFMA(d[3], d[0], acc[1][0]); acc[1][1] = MFMA(d[3], d[1], acc[1][1]);
    }
  }
  STAMP(1);
  float* mine = xch + wave * 64 * XROW;
#pragma unroll
  for (int n = 0; n < 2; ++n)
#pragma unroll
    for (int m = 0; m < 2; ++m)
#pragma unroll
      for (int a = 0; a < 4; ++a) {
        f32x4 t = {acc[n][m][4 * a], acc[n][m][4 * a + 1], acc[n][m][4 * a + 2], acc[n][m][4 * a + 3]};
        *(f32x4*)(mine + (32 * m + r32) * XROW + 32 * n + 8 * a + 4 * h) = t;
      }
  __syncthreads();
  STAMP(2);
  const float* rp = xch + (tid >> 2) * XROW + (tid & 3) * 16;
  u32x4 res[2];
#pragma unroll
  for (int q = 0; q < 4; ++q) {
    const f32x4 s0 = *(const f32x4*)(rp + 4 * q), o0 = *(const f32x4*)(rp + 64 * XROW + 4 * q);
    const f32x4 o1 = *(const f32x4*)(rp + 128 * XROW + 4 * q), o2 = *(const f32x4*)(rp + 192 * XROW + 4 * q);
#pragma unroll
    for (int e = 0; e < 4; e += 2) {
      const unsigned su = cvtpk(s0[e], s0[e + 1]);
      const unsigned ou = cvtpk((o0[e] + o1[e]) + o2[e], (o0[e + 1] + o1[e + 1]) + o2[e + 1]);
      const unsigned gu = cvtpk(silu_f(bflo(ou)), silu_f(bfhi(ou)));
      const unsigned tu = cvtpk(bflo(su) * bflo(gu), bfhi(su) * bfhi(gu));
      const unsigned xw = (q < 2 ? xa : xb)[(q & 1) * 2 + (e >> 1)];
      res[q >> 1][(q & 1) * 2 + (e >> 1)] = cvtpk(bflo(xw) + bflo(tu), bfhi(xw) + bfhi(tu));
    }
  }
  *(u32x4*)(out + (size_t)erow * D_ + ecol) = res[0];
  *(u32x4*)(out + (size_t)erow * D_ + ecol + 8) = res[1];
  STAMP(3); STAMPR(31);
}

extern "C" void kernel_launch(void* const* d_in, const int* in_sizes, int n_in, void* d_out, int out_size, void* d_ws, size_t ws_size,
                              hipStream_t stream) {
  const u16* x = (const u16*)d_in[0];
  const u16* wqkv = (const u16*)d_in[1];
  const u16* wphi = (const u16*)d_in[2];
  const u16* wo = (const u16*)d_in[3];
  const u16* w1 = (const u16*)d_in[5];
  const u16* w3 = (const u16*)d_in[6];
  const u16* w2 = (const u16*)d_in[7];
  char* ws = (char*)d_ws;
  k1_kernel<<<256, 512, 0, stream>>>(x, wqkv, wphi, wo, w1, w3, w2, ws);
  k2_kernel<<<256, 256, 0, stream>>>(x, ws, (u16*)d_out);
}
```

```cpp
#include <hip/hip_runtime.h>
#include <hip/hip_bf16.h>
#include <stdint.h>

typedef __attribute__((ext_vector_type(8))) short bf16x8;
typedef __attribute__((ext_vector_type(4))) short s16x4;
typedef __attribute__((ext_vector_type(16))) float f32x16;
typedef __attribute__((ext_vector_type(4))) float f32x4;
typedef __attribute__((ext_vector_type(4))) unsigned u32x4;
typedef __attribute__((ext_vector_type(2))) unsigned u32x2;
typedef unsigned short u16;
#define LAS __attribute__((address_space(3)))
#define DI __device__ __forceinline__
#define MFMA(a, b, c) __builtin_amdgcn_mfma_f32_32x32x16_bf16((a), (b), (c), 0, 0, 0)
#define MFMA16(a, b, c) __builtin_amdgcn_mfma_f32_16x16x32_bf16(__builtin_bit_cast(bf16x8, (a)), __builtin_bit_cast(bf16x8, (b)), (c), 0, 0, 0)

constexpr int S_ = 2048, D_ = 512, NH = 32, F_ = 1376;
constexpr int KS_F = F_ / 16;
constexpr int KS_W = 32 + KS_F;
constexpr size_t WS_AB = 0;
constexpr size_t WS_WT = WS_AB + (size_t)64 * KS_W * 1024;
constexpr size_t WS_P1 = WS_WT + (size_t)16 * KS_W * 1024;
constexpr size_t WS_P2 = WS_P1 + (size_t)16 * 16 * 16 * 1024;
constexpr size_t WS_EP = WS_P2 + (size_t)16 * 16 * 512;
constexpr size_t WS_DUMP = WS_EP + 16 * 128;
constexpr size_t WS_END = WS_DUMP + 2048;

typedef __attribute__((ext_vector_type(2))) __bf16 bf16x2_t;
typedef __attribute__((ext_vector_type(2))) float f32x2;
DI unsigned cvtpk(float lo, float hi) { f32x2 v = {lo, hi}; return __builtin_bit_cast(unsigned, __builtin_convertvector(v, bf16x2_t)); }
DI float bflo(unsigned u) { return __uint_as_float(u << 16); }
DI float bfhi(unsigned u) { return __uint_as_float(u & 0xffff0000u); }
DI float bf16r(float x) { return bflo(cvtpk(x, x)); }
DI void glds16(const void* g, unsigned lds_base) {
  unsigned sv;
  asm volatile("s_mov_b32 %0, m0\n\ts_mov_b32 m0, %2\n\ts_nop 0\n\tglobal_load_lds_dwordx4 %1, off\n\ts_mov_b32 m0, %0"
               : "=&s"(sv) : "v"(g), "s"(lds_base) : "memory");
}
typedef short v4i16_t __attribute__((ext_vector_type(4)));
DI s16x4 trrd(unsigned a) { return __builtin_bit_cast(s16x4, __builtin_amdgcn_ds_read_tr16_b64_v4i16((LAS v4i16_t*)(uintptr_t)a)); }
DI bf16x8 trfrag(unsigned lo, unsigned hi) { s16x4 a = trrd(lo), b = trrd(hi); return __builtin_shufflevector(a, b, 0, 1, 2, 3, 4, 5, 6, 7); }
DI bf16x8 pack8(const float* v) {
  u32x4 p; p[0] = cvtpk(v[0], v[1]); p[1] = cvtpk(v[2], v[3]); p[2] = cvtpk(v[4], v[5]); p[3] = cvtpk(v[6], v[7]);
  return __builtin_bit_cast(bf16x8, p);
}
DI float xhalf(float v) {
  auto rr = __builtin_amdgcn_permlane32_swap(__float_as_uint(v), __float_as_uint(v), false, false);
  unsigned a = rr[0], b = rr[1];
  return __uint_as_float((threadIdx.x & 32) ? a : b);
}
template <int C> DI float dppror(float v) { return __uint_as_float(__builtin_amdgcn_update_dpp(0u, __float_as_uint(v), 0x120 + C, 0xf, 0xf, false)); }
DI float row16sum(float v) { v += dppror<8>(v); v += dppror<4>(v); v += dppror<2>(v); v += dppror<1>(v); return v; }
DI float silu_f(float x) { return x * __builtin_amdgcn_rcpf(1.0f + __builtin_amdgcn_exp2f(-1.4426950408889634f * x)); }
#define STAMP(i)
#define STAMPR(i)
#define STAMP_FLUSH(base)
#define BAR() asm volatile("s_waitcnt lgkmcnt(0)\n\ts_barrier" ::: "memory")
#define WAITV0() asm volatile("s_waitcnt vmcnt(0)" ::: "memory")

enum { T_NONE = 0, T_Q = 1, T_K = 2, T_V = 3, T_G = 4, T_U = 5 };
#define TD(ty, ix) ((unsigned long long)(((ty) << 6) | (ix)))
#define SB0() __builtin_amdgcn_sched_barrier(0)
#define GAS __attribute__((address_space(1)))
typedef unsigned long long u64;
DI float dot2(unsigned a, unsigned b, float c) {
  return __builtin_amdgcn_fdot2_f32_bf16(__builtin_bit_cast(bf16x2_t, a), __builtin_bit_cast(bf16x2_t, b), c, false);
}
DI void st_gran(void* p, unsigned val, unsigned tag) { __hip_atomic_store((GAS u64*)p, ((u64)tag << 32) | val, __ATOMIC_RELAXED, __HIP_MEMORY_SCOPE_AGENT); }
DI u64 ld_gran(const void* p) { return __hip_atomic_load((GAS u64*)p, __ATOMIC_RELAXED, __HIP_MEMORY_SCOPE_AGENT); }
constexpr unsigned SPIN_MAX = 1u << 16;

constexpr int SLOT = 32768;
constexpr int LDS_SCR = 4 * SLOT;
constexpr int LDS_DBG = LDS_SCR + 28672;
constexpr int K1_LDS = 5 * SLOT;

DI void glds16s(unsigned voff, const void* sbase, unsigned lds_base) {
  unsigned sv;
  asm volatile("s_mov_b32 %0, m0\n\ts_mov_b32 m0, %3\n\ts_nop 0\n\tglobal_load_lds_dwordx4 %1, %2\n\ts_mov_b32 m0, %0"
               : "=&s"(sv) : "v"(voff), "s"(sbase), "s"(lds_base) : "memory");
}
DI void center4(u32x4& a, float c) {
  unsigned t0, t1, t2, t3, t4, t5, t6, t7;
  asm("v_dot2_f32_bf16 %0, %8, %12, %14\n\tv_dot2_f32_bf16 %1, %8, %13, %14\n\tv_dot2_f32_bf16 %2, %9, %12, %14\n\tv_dot2_f32_bf16 %3, %9, %13, %14\n\t"
      "v_dot2_f32_bf16 %4, %10, %12, %14\n\tv_dot2_f32_bf16 %5, %10, %13, %14\n\tv_dot2_f32_bf16 %6, %11, %12, %14\n\tv_dot2_f32_bf16 %7, %11, %13, %14\n\t"
      "v_cvt_pk_bf16_f32 %0, %0, %1\n\tv_cvt_pk_bf16_f32 %2, %2, %3\n\tv_cvt_pk_bf16_f32 %4, %4, %5\n\tv_cvt_pk_bf16_f32 %6, %6, %7"
      : "=&v"(t0), "=&v"(t1), "=&v"(t2), "=&v"(t3), "=&v"(t4), "=&v"(t5), "=&v"(t6), "=&v"(t7)
      : "v"(a[0]), "v"(a[1]), "v"(a[2]), "v"(a[3]), "s"(0x00003f80u), "s"(0x3f800000u), "v"(c));
  a[0] = t0; a[1] = t2; a[2] = t4; a[3] = t6;
}
DI float rsum32(float v) { auto rr = __builtin_amdgcn_permlane32_swap(__float_as_uint(v), __float_as_uint(v), false, false); return __uint_as_float(rr[0]) + __uint_as_float(rr[1]); }
DI float rsum16(float v) { auto rr = __builtin_amdgcn_permlane16_swap(__float_as_uint(v), __float_as_uint(v), false, false); return __uint_as_float(rr[0]) + __uint_as_float(rr[1]); }
DI float rmax32(float v) { auto rr = __builtin_amdgcn_permlane32_swap(__float_as_uint(v), __float_as_uint(v), false, false); return fmaxf(__uint_as_float(rr[0]), __uint_as_float(rr[1])); }
DI float rmin32(float v) { auto rr = __builtin_amdgcn_permlane32_swap(__float_as_uint(v), __float_as_uint(v), false, false); return fminf(__uint_as_float(rr[0]), __uint_as_float(rr[1])); }
DI float nv(float x) { asm("" : "+v"(x)); return x; }
#define WAITV(n) asm volatile("s_waitcnt vmcnt(" #n ")" ::: "memory")

#define PRIO_M 2
#define PFD 4
#define LDSP 4
#define PRIO_E 0
template <int RB, class F>
DI void mfma_tile(f32x16& acc, const bf16x8 (&xf)[32], unsigned sb, bool ld, const char* g, unsigned gstep, unsigned voff, unsigned dst, F filler) {
  __builtin_amdgcn_s_setprio(PRIO_M);
  bf16x8 wq[PFD];
#pragma unroll
  for (int ks = 0; ks < PFD; ++ks) { const int o = ((ks >> 2) * 64 + (ks & 3) * 8) * RB; wq[ks] = trfrag(sb + o, sb + o + 4 * RB); }
#pragma unroll
  for (int i = 0; i < 16; ++i) acc[i] = 0.f;
  SB0();
#pragma unroll
  for (int ks = 0; ks < 32; ++ks) {
    acc = MFMA(wq[ks % PFD], xf[ks], acc);
    if (ks + PFD < 32) { const int o = (((ks + PFD) >> 2) * 64 + ((ks + PFD) & 3) * 8) * RB; wq[ks % PFD] = trfrag(sb + o, sb + o + 4 * RB); }
    if ((ks % LDSP) == 0 && ks < 8 * LDSP) { if (ld) glds16s(voff, g + (size_t)(ks / LDSP) * gstep, dst + (ks / LDSP) * 1024); }
    filler(ks);
    SB0();
  }
  __builtin_amdgcn_s_setprio(PRIO_E);
  asm volatile("" : "+v"(acc));
}
DI void feat_proj(const f32x16& acc, const u32x4& wf, int r32, unsigned (&pp)[8]) {
  float av[16];
#pragma unroll
  for (int i = 0; i < 16; ++i) av[i] = acc[i];
  const bf16x8 qb0 = pack8(av), qb1 = pack8(av + 8);
  const u32x4 z4 = {0u, 0u, 0u, 0u};
  const bf16x8 a0 = __builtin_bit_cast(bf16x8, (r32 < 16) ? wf : z4), a1 = __builtin_bit_cast(bf16x8, (r32 < 16) ? z4 : wf);
  f32x16 pq;
#pragma unroll
  for (int i = 0; i < 16; ++i) pq[i] = 0.f;
  SB0();
  __builtin_amdgcn_s_setprio(3);
  pq = MFMA(a0, qb0, pq);
  pq = MFMA(a1, qb1, pq);
  SB0();
  __builtin_amdgcn_s_setprio(PRIO_E);
#pragma unroll
  for (int j = 0; j < 8; ++j) pp[j] = cvtpk(pq[2 * j], pq[2 * j + 1]);
  asm volatile("" : "+v"(pp[0]), "+v"(pp[1]), "+v"(pp[2]), "+v"(pp[3]), "+v"(pp[4]), "+v"(pp[5]), "+v"(pp[6]), "+v"(pp[7]));
}
DI void feat_soft(const unsigned* pp, bf16x8& fp, bf16x8& fm) {
  float pv[8];
#pragma unroll
  for (int j = 0; j < 4; ++j) { pv[2 * j] = bflo(pp[j]); pv[2 * j + 1] = bfhi(pp[j]); }
  float mx = fmaxf(fmaxf(fmaxf(pv[0], pv[1]), pv[2]), fmaxf(fmaxf(pv[3], pv[4]), pv[5])), mn = fminf(fminf(fminf(pv[0], pv[1]), pv[2]), fminf(fminf(pv[3], pv[4]), pv[5]));
  mx = fmaxf(fmaxf(mx, pv[6]), pv[7]); mn = fminf(fminf(mn, pv[6]), pv[7]);
  mx = rmax32(mx); mn = rmin32(mn);
  const float L2E = 1.4426950408889634f, mxl = nv(-mx * L2E), mnl = nv(mn * L2E);
  float ep[8], em[8], sp = 0.f, sm = 0.f;
#pragma unroll
  for (int j = 0; j < 8; ++j) {
    ep[j] = __builtin_amdgcn_exp2f(nv(fmaf(pv[j], L2E, mxl))); sp = nv(sp + ep[j]);
    em[j] = __builtin_amdgcn_exp2f(nv(fmaf(pv[j], -L2E, mnl))); sm = nv(sm + em[j]);
  }
  sp = nv(rsum32(sp)); sm = nv(rsum32(sm));
  const float rp = __builtin_amdgcn_rcpf(sp), rm = __builtin_amdgcn_rcpf(sm);
#pragma unroll
  for (int j = 0; j < 8; ++j) { ep[j] = nv(ep[j] * rp); em[j] = nv(em[j] * rm); }
  fp = pack8(ep); fm = pack8(em);
  asm volatile("" : "+v"(fp), "+v"(fm));
}
DI void epi_g(const f32x16& acc, unsigned (&sg)[8]) {
#pragma unroll
  for (int i = 0; i < 16; i += 2) {
    const unsigned u = cvtpk(acc[i], acc[i + 1]);
    sg[i >> 1] = cvtpk(nv(silu_f(bflo(u))), nv(silu_f(bfhi(u))));
  }
  asm volatile("" : "+v"(sg[0]), "+v"(sg[1]), "+v"(sg[2]), "+v"(sg[3]), "+v"(sg[4]), "+v"(sg[5]), "+v"(sg[6]), "+v"(sg[7]));
}
DI void epi_u(const f32x16& acc, const unsigned (&sg)[8], char* dst) {
  float hv[16];
#pragma unroll
  for (int i = 0; i < 16; i += 2) { const unsigned u = cvtpk(acc[i], acc[i + 1]); hv[i] = nv(bflo(sg[i >> 1]) * bflo(u)); hv[i + 1] = nv(bfhi(sg[i >> 1]) * bfhi(u)); }
  *(bf16x8*)dst = pack8(hv); *(bf16x8*)(dst + 1024) = pack8(hv + 8);
}
#define PHASE_END(p) do { STAMP(2 + 2 * (p)); SB0(); BAR(); SB0(); STAMP(3 + 2 * (p)); } while (0)

__global__ __launch_bounds__(512, 2) void k1_kernel(const u16* __restrict__ x, const u16* __restrict__ wqkv, const u16* __restrict__ wphi,
                                                    const u16* __restrict__ wo, const u16* __restrict__ w1, const u16* __restrict__ w3,
                                                    const u16* __restrict__ w2, char* __restrict__ ws) {
  __shared__ __attribute__((aligned(16))) char smem[K1_LDS];
  const int tid = threadIdx.x, lane = tid & 63;
  const int wave = __builtin_amdgcn_readfirstlane(tid >> 6), grp = wave >> 2, wr = wave & 3;
  const int b = blockIdx.x, xcd = b & 7, idx = b >> 3;
  const int mt = (xcd & 1) * 8 + (idx & 7), ng = (xcd >> 1) * 4 + (idx >> 3);
  const int r32 = lane & 31, h = lane >> 5;
  const unsigned lds0 = (unsigned)(uintptr_t)smem;
  unsigned dummy0 = 0;
  if (wave == 0) {
    unsigned long long pc; unsigned len;
    asm volatile("s_getpc_b64 %0\n.Lk1_pc:\n\ts_mov_b32 %1, .Lk1_end-.Lk1_pc" : "=s"(pc), "=s"(len));
    const unsigned off = (lane * 8 + (idx & 7)) * 128;
    if (off < len) asm volatile("global_load_dword %0, %1, %2" : "+v"(dummy0) : "v"(off), "s"(pc) : "memory");
  }
  unsigned dummy1 = 0;
  if (grp == 0) asm volatile("global_load_dword %0, %1, %2" : "+v"(dummy1) : "v"(lane * 128), "s"((const char*)(x + (size_t)(mt * 128 + (idx >> 3) * 32 + wr * 8) * D_)) : "memory");
  const unsigned reg = lds0 + SLOT + (wr * 2 + grp) * 16384;
  {
    const char* xb = (const char*)(x + (size_t)(mt * 128 + wr * 32 + grp * 16) * D_);
    const int rr = lane >> 5, qd = (lane >> 3) & 3, pos = lane & 7;
#pragma unroll
    for (int cc = 0; cc < 2; ++cc)
#pragma unroll
      for (int i = 0; i < 8; ++i)
        glds16s(rr * 1024 + qd * 256 + ((pos ^ (2 * (i & 3) + rr)) << 4), xb + i * 2048 + cc * 128, reg + cc * 8192 + i * 1024);
  }
  const int g0 = ng < 11 ? 3 * ng : 33 + 2 * (ng - 11);
  const bool full = ng < 11;
  const char* wK = (const char*)(wqkv + 512 + 32 * ng);
  const char* wG = (const char*)(w1 + 32 * g0);
  const char* wU = (const char*)(w3 + 32 * g0);
  const unsigned voffA = (lane >> 2) * 3072 + (lane & 3) * 16, voffF = (lane >> 2) * 2752 + (lane & 3) * 16;
  unsigned dummy = 0, dummy2 = 0, tagv;
  u32x4 wf, wfb;
  {
    tagv = __hip_atomic_load((GAS unsigned*)(ws + WS_EP + ng * 128), __ATOMIC_RELAXED, __HIP_MEMORY_SCOPE_AGENT);
    const u16* wp = wphi + (size_t)(2 * ng + (r32 >> 4)) * 256 + (4 * h) * 16 + (lane & 15);
#pragma unroll
    for (int j = 0; j < 4; ++j) {
      const int k0 = 8 * (j >> 1) + 2 * (j & 1);
      asm volatile("global_load_ushort %0, %2, off offset:%3\n\tglobal_load_ushort %1, %2, off offset:%4" : "=&v"(wf[j]), "=&v"(wfb[j]) : "v"(wp), "n"(k0 * 32), "n"(k0 * 32 + 32) : "memory");
    }
    if (grp) {
      const char* g = wK + (size_t)wr * 128 * 3072;
#pragma unroll
      for (int i = 0; i < 8; ++i) glds16s(voffA, g + i * 16 * 3072, lds0 + wr * 8192 + i * 1024);
      const int trow = (idx & 7) * 64 + lane;
      const int e = (wr == 3 && !full) ? 0 : wr - 1;
      const char* p = wr == 0 ? wK + 1024 + (size_t)trow * 3072 : wG + 64 * e + (size_t)trow * 2752;
      const char* p2 = wr == 0 ? wK - 1024 + (size_t)trow * 3072 : wU + 64 * e + (size_t)trow * 2752;
      asm volatile("global_load_dword %0, %1, off" : "+v"(dummy) : "v"(p) : "memory");
      asm volatile("global_load_dword %0, %1, off" : "+v"(dummy2) : "v"(p2) : "memory");
    }
  }
  bf16x8 xf[32];
  unsigned tag;
  {
    const int r16 = lane & 15, qd = lane >> 4;
    const unsigned rb = reg + (r16 >> 1) * 1024 + (r16 & 1) * 512 + qd * 128 + ((r16 & 7) << 4);
    u32x4 xr[16];
    float q0 = 0.f, q1 = 0.f;
    f32x4 sum4 = {0.f, 0.f, 0.f, 0.f};
    const u32x4 ones = {0x3f803f80u, 0x3f803f80u, 0x3f803f80u, 0x3f803f80u};
    const unsigned one = (qd == (r16 >> 2)) ? (0x3f80u << (16 * (r16 & 1))) : 0u;
    const unsigned e0 = (r16 & 2) ? 0u : one, e1 = (r16 & 2) ? one : 0u;
    const u32x4 selL = {e0, e1, 0u, 0u}, selH = {0u, 0u, e0, e1};
    if (grp) WAITV(27); else WAITV(17);
#pragma unroll
    for (int q = 0; q < 8; ++q) xr[q] = *(const LAS u32x4*)(uintptr_t)(rb ^ (q << 4));
#pragma unroll
    for (int q = 0; q < 8; ++q)
#pragma unroll
      for (int e = 0; e < 4; e += 2) {
        if (e == 0) sum4 = MFMA16(ones, xr[q], sum4);
        q0 = dot2(xr[q][e], xr[q][e], q0); q1 = dot2(xr[q][e + 1], xr[q][e + 1], q1);
      }
    asm volatile("" : "+v"(sum4), "+v"(q0), "+v"(q1));
    if (grp) WAITV(19); else WAITV(9);
#pragma unroll
    for (int q = 0; q < 8; ++q) xr[8 + q] = *(const LAS u32x4*)(uintptr_t)((rb ^ (q << 4)) + 8192);
#pragma unroll
    for (int q = 8; q < 16; ++q)
#pragma unroll
      for (int e = 0; e < 4; e += 2) {
        if (e == 0) sum4 = MFMA16(ones, xr[q], sum4);
        q0 = dot2(xr[q][e], xr[q][e], q0); q1 = dot2(xr[q][e + 1], xr[q][e + 1], q1);
      }
    const float mean = sum4[0] * (1.0f / 512.0f);
    const float var = fmaxf(rsum32(rsum16(q0 + q1)) * (1.0f / 512.0f) - mean * mean, 0.f);
    const float nmb = -bf16r(mean);
    const float rs = 1.0f / bf16r(sqrtf(bf16r(bf16r(var) + 1e-5f)));
    const unsigned wb = reg + qd * 4096 + r16 * 16;
#pragma unroll
    for (int q = 0; q < 16; ++q) {
      const f32x4 c4 = {nmb, nmb, nmb, nmb}, z4 = {0.f, 0.f, 0.f, 0.f};
      const f32x4 d0 = MFMA16(selL, xr[q], c4), d1 = MFMA16(selH, xr[q], c4);
      const u32x4 u = {cvtpk(d0[0], d0[1]), cvtpk(d0[2], d0[3]), cvtpk(d1[0], d1[1]), cvtpk(d1[2], d1[3])};
      const f32x4 f0 = MFMA16(selL, u, z4), f1 = MFMA16(selH, u, z4);
      const u32x4 o = {cvtpk(nv(f0[0] * rs), nv(f0[1] * rs)), cvtpk(nv(f0[2] * rs), nv(f0[3] * rs)), cvtpk(nv(f1[0] * rs), nv(f1[1] * rs)), cvtpk(nv(f1[2] * rs), nv(f1[3] * rs))};
      *(LAS u32x4*)(uintptr_t)(wb + (q >> 3) * 2048 + (q & 3) * 512 + ((q >> 2) & 1) * 256) = o;
    }
    SB0();
    WAITV(0);
    asm volatile("" : "+v"(dummy), "+v"(dummy0), "+v"(dummy1), "+v"(dummy2), "+v"(wf), "+v"(wfb));
#pragma unroll
    for (int j = 0; j < 4; ++j) wf[j] |= wfb[j] << 16;
    tag = __builtin_amdgcn_readfirstlane(tagv) + 1u;
    asm volatile("" : "+s"(tag));
    BAR();
    const unsigned xb = lds0 + SLOT * (1 + wr) + (r32 >> 4) * 16384 + (h * 16 + (r32 & 15)) * 16;
#pragma unroll
    for (int ks = 0; ks < 32; ++ks) xf[ks] = *(const LAS bf16x8*)(uintptr_t)(xb + ks * 512);
    BAR();
  }
  STAMP(1);
  const unsigned voffP = (lane >> 3) * 2752 + (((lane & 7) ^ ((lane >> 2) & 4)) << 4);
  const size_t rowP = (size_t)wave * 64 * 2752;
  const int i16 = lane & 15, blk = (lane >> 4) & 1;
  const unsigned tr0 = lds0 + (32 * h + (i16 >> 2)) * 64 + 8 * (4 * blk + (i16 & 3));
  const unsigned trP = lds0 + (32 * h + (i16 >> 2)) * 128 + ((((grp ^ 1) ^ (i16 >> 3)) & 1) << 6) + 8 * (4 * blk + (i16 & 3));
  const int mblk = mt * 4 + wr;
  const unsigned ldd = lds0 + wr * 8192;
  const size_t rowF = (size_t)wr * 128 * 2752, rowA = (size_t)wr * 128 * 3072;
  char* const hdst = ws + WS_AB + ((size_t)mblk * KS_W + 32 + 2 * g0) * 1024 + lane * 16;
  unsigned sg[8], pp[8];
  f32x16 acc, acc2;
  const int row = wr * 32 + r32, sw = (row >> 1) & 7;
  auto nofill = [](int) {};
  if (grp == 0) {
    mfma_tile<64>(acc, xf, tr0, false, nullptr, 0, 0, 0, nofill);
    PHASE_END(0);
    feat_proj(acc, wf, r32, pp);
#pragma unroll
    for (int s = 0; s < 2; ++s) {
      if (s) {
#pragma unroll
        for (int i = 0; i < 4; ++i) glds16s(voffP, wU + rowP + i * 8 * 2752, lds0 + wave * 8192 + i * 1024);
      }
      bf16x8 fp, fm;
      feat_soft(pp + 4 * s, fp, fm);
      if (s) {
#pragma unroll
        for (int i = 4; i < 8; ++i) glds16s(voffP, wU + rowP + i * 8 * 2752, lds0 + wave * 8192 + i * 1024);
      }
      const unsigned pk = lds0 + LDS_SCR + s * 8192 + row * 64;
      const u32x4 up = __builtin_bit_cast(u32x4, fp), um = __builtin_bit_cast(u32x4, fm);
      u32x2 t;
      t[0] = up[0]; t[1] = up[1]; *(LAS u32x2*)(uintptr_t)(pk + 8 * ((h) ^ sw)) = t;
      t[0] = up[2]; t[1] = up[3]; *(LAS u32x2*)(uintptr_t)(pk + 8 * ((2 + h) ^ sw)) = t;
      t[0] = um[0]; t[1] = um[1]; *(LAS u32x2*)(uintptr_t)(pk + 8 * ((4 + h) ^ sw)) = t;
      t[0] = um[2]; t[1] = um[3]; *(LAS u32x2*)(uintptr_t)(pk + 8 * ((6 + h) ^ sw)) = t;
      PHASE_END(1 + s);
    }
    mfma_tile<128>(acc, xf, trP + 2 * SLOT, false, nullptr, 0, 0, 0, nofill);
    WAITV(0);
    PHASE_END(3);
    {
      if (full) {
#pragma unroll
        for (int i = 0; i < 8; ++i) glds16s(voffF, wG + 128 + rowF + i * 16 * 2752, ldd + 3 * SLOT + i * 1024);
      }
      epi_g(acc, sg);
    }
    PHASE_END(4);
    u64 g1[8];
    const char* p1r = ws + WS_P1 + ((size_t)(ng * 16 + mt) * 16 + h * 8) * 1024 + blk * 512 + (wr * 16 + i16) * 8;
    mfma_tile<128>(acc, xf, trP, false, nullptr, 0, 0, 0, [&](int ks) { if (ks >= 8 && ks < 16) g1[ks - 8] = ld_gran(p1r + (ks - 8) * 1024); });
    PHASE_END(5);
    {
      for (unsigned spin = 0; spin < SPIN_MAX; ++spin) {
        bool ok = true;
#pragma unroll
        for (int j = 0; j < 8; ++j) ok &= (unsigned)(g1[j] >> 32) == tag;
        if (__all(ok)) break;
        __builtin_amdgcn_s_sleep(4);
#pragma unroll
        for (int j = 0; j < 8; ++j) g1[j] = ld_gran(p1r + j * 1024);
      }
      float a = 0.f;
#pragma unroll
      for (int j = 0; j < 8; ++j) a = nv(a + __uint_as_float((unsigned)g1[j]));
      a = rsum32(a);
      auto rr = __builtin_amdgcn_permlane16_swap(__float_as_uint(a), __float_as_uint(a), false, false);
      if (lane < 16) st_gran(ws + WS_P2 + (size_t)(ng * 16 + mt) * 512 + (wr * 16 + lane) * 8, cvtpk(__uint_as_float(rr[0]), __uint_as_float(rr[1])), tag);
      if (mt == 0 && wr == 0 && lane == 0) __hip_atomic_store((GAS unsigned*)(ws + WS_EP + ng * 128), tag, __ATOMIC_RELAXED, __HIP_MEMORY_SCOPE_AGENT);
      SB0();
      if (full) {
#pragma unroll
        for (int i = 0; i < 8; ++i) glds16s(voffF, wU + 128 + rowF + i * 16 * 2752, ldd + i * 1024);
      }
      epi_u(acc, sg, hdst + 2048);
    }
    PHASE_END(6);
    if (full) mfma_tile<64>(acc, xf, tr0 + 3 * SLOT, false, nullptr, 0, 0, 0, nofill);
    WAITV(0);
    PHASE_END(7);
    if (full) {
      mfma_tile<64>(acc2, xf, tr0, false, nullptr, 0, 0, 0, nofill);
      STAMP(18);
      epi_g(acc, sg);
      epi_u(acc2, sg, hdst + 4096);
    }
    STAMP(20);
  } else {
#pragma unroll
    for (int i = 0; i < 8; ++i) glds16s(voffA, wK + 1024 + rowA + i * 16 * 3072, ldd + SLOT + i * 1024);
#pragma unroll
    for (int i = 0; i < 16; ++i) glds16s(voffP, wG + rowF + i * 8 * 2752, lds0 + 2 * SLOT + wr * 16384 + i * 1024);
    u32x4 rta[2], rtb[2];
    const int gw = b * 8 + wr * 2;
#pragma unroll
    for (int f = 0; f < 2; ++f) {
      const int gc = gw + f < 16 * KS_W ? gw + f : 0, nb = gc / KS_W, ks = gc - nb * KS_W;
      const u16* src = (ks < 32 ? wo + (size_t)ks * 16 * D_ : w2 + (size_t)(ks - 32) * 16 * D_) + nb * 32 + r32 + (size_t)(4 * h) * D_;
#pragma unroll
      for (int j = 0; j < 4; ++j) {
        const int k0 = 8 * (j >> 1) + 2 * (j & 1);
        asm volatile("global_load_ushort %0, %2, off\n\tglobal_load_ushort %1, %2, off offset:1024" : "=&v"(rta[f][j]), "=&v"(rtb[f][j]) : "v"(src + (size_t)k0 * D_) : "memory");
      }
    }
    WAITV(32);
    PHASE_END(0);
    mfma_tile<64>(acc2, xf, tr0 + SLOT, false, nullptr, 0, 0, 0, nofill);
    WAITV(16);
    PHASE_END(1);
    {
      const unsigned vi = lds0 + LDS_SCR + 16384 + row * 64;
      mfma_tile<128>(acc, xf, trP + 2 * SLOT, true, wU + rowP, 8 * 2752, voffP, lds0 + wave * 8192, [&](int ks) {
        if (ks >= 1 && ks <= 4) {
          const int a = ks - 1;
          u32x2 t; t[0] = cvtpk(acc2[4 * a], acc2[4 * a + 1]); t[1] = cvtpk(acc2[4 * a + 2], acc2[4 * a + 3]);
          *(LAS u32x2*)(uintptr_t)(vi + 8 * ((2 * a + h) ^ sw)) = t;
        }
      });
    }
    PHASE_END(2);
    if (wr < 2) {
      f32x16 a2;
#pragma unroll
      for (int i = 0; i < 16; ++i) a2[i] = 0.f;
      const int rowq = 8 * h + (i16 >> 2);
      const int ca = 4 * blk + (lane & 3), cb = 4 * (blk ^ wr) + (lane & 3);
      const int sw0 = (rowq >> 1) & 7, sw1 = ((rowq + 4) >> 1) & 7;
      const unsigned pk = lds0 + LDS_SCR + wr * 8192 + rowq * 64, vi = lds0 + LDS_SCR + 16384 + rowq * 64;
      const unsigned pa0 = pk + 8 * (ca ^ sw0), pa1 = pk + 256 + 8 * (ca ^ sw1), pb0 = vi + 8 * (cb ^ sw0), pb1 = vi + 256 + 8 * (cb ^ sw1);
      __builtin_amdgcn_s_setprio(3);
#pragma unroll
      for (int ks = 0; ks < 8; ++ks) {
        bf16x8 af = trfrag(pa0 + ks * 1024, pa1 + ks * 1024);
        bf16x8 bfr = trfrag(pb0 + ks * 1024, pb1 + ks * 1024);
        if (blk) {
#pragma unroll
          for (int j = 0; j < 8; ++j) bfr[j] = (short)0x3f80;
        }
        a2 = MFMA(af, bfr, a2);
      }
      asm volatile("" : "+v"(a2));
      __builtin_amdgcn_s_setprio(PRIO_E);
      char* p1w = ws + WS_P1 + (((size_t)(ng * 16 + wr * 8) * 16 + mt) * 1024) + lane * 8;
#pragma unroll
      for (int i = 0; i < 16; ++i) st_gran(p1w + (size_t)(i >> 1) * 16384 + (i & 1) * 512, __float_as_uint(a2[i]), tag);
      WAITV(24);
    } else {
      WAITV(8);
    }
    {
      asm volatile("" : "+v"(rta[0]), "+v"(rta[1]), "+v"(rtb[0]), "+v"(rtb[1]));
#pragma unroll
      for (int f = 0; f < 2; ++f) {
#pragma unroll
        for (int j = 0; j < 4; ++j) rta[f][j] |= rtb[f][j] << 16;
        *(u32x4*)(ws + (gw + f < 16 * KS_W ? WS_WT + (size_t)(gw + f) * 1024 : WS_DUMP + f * 1024) + lane * 16) = rta[f];
      }
    }
    if (wr < 2) WAITV(18); else WAITV(2);
    PHASE_END(3);
    mfma_tile<128>(acc2, xf, trP, true, wK - 1024 + rowA, 16 * 3072, voffA, ldd + 2 * SLOT, nofill);
    PHASE_END(4);
    epi_g(acc, sg);
    epi_u(acc2, sg, hdst);
    WAITV(2);
    PHASE_END(5);
    mfma_tile<64>(acc, xf, tr0 + 2 * SLOT, false, nullptr, 0, 0, 0, nofill);
    PHASE_END(6);
    bf16x8 fp[2], fm[2];
    feat_proj(acc, wf, r32, pp);
    feat_soft(pp, fp[0], fm[0]);
    PHASE_END(7);
    const char* p2r = ws + WS_P2 + (size_t)(ng * 16) * 512 + lane * 8;
    u64 g2[16];
#pragma unroll
    for (int j = 0; j < 16; ++j) g2[j] = ld_gran(p2r + j * 512);
    SB0();
    feat_soft(pp + 4, fp[1], fm[1]);
    STAMP(18);
    for (unsigned spin = 0; spin < SPIN_MAX; ++spin) {
      bool ok = true;
#pragma unroll
      for (int j = 0; j < 16; ++j) ok &= (unsigned)(g2[j] >> 32) == tag;
      if (__all(ok)) break;
      __builtin_amdgcn_s_sleep(4);
#pragma unroll
      for (int j = 0; j < 16; ++j) g2[j] = ld_gran(p2r + j * 512);
    }
    STAMP(19);
    __builtin_amdgcn_s_setprio(3);
#pragma unroll
    for (int s = 0; s < 2; ++s) {
      const u32x4 f0 = {(unsigned)g2[8 * s], (unsigned)g2[8 * s + 1], (unsigned)g2[8 * s + 2], (unsigned)g2[8 * s + 3]};
      const u32x4 f1 = {(unsigned)g2[8 * s + 4], (unsigned)g2[8 * s + 5], (unsigned)g2[8 * s + 6], (unsigned)g2[8 * s + 7]};
      f32x16 o;
#pragma unroll
      for (int q = 0; q < 16; ++q) o[q] = 0.f;
      o = MFMA(__builtin_bit_cast(bf16x8, f0), fp[s], o);
      o = MFMA(__builtin_bit_cast(bf16x8, f1), fm[s], o);
      const float rd = __builtin_amdgcn_rcpf(bf16r(bf16r(o[8]) + 1e-6f));
      float ov[8];
#pragma unroll
      for (int j = 0; j < 8; j += 2) { const unsigned u = cvtpk(o[j], o[j + 1]); ov[j] = nv(bflo(u) * rd); ov[j + 1] = nv(bfhi(u) * rd); }
      *(bf16x8*)(ws + WS_AB + ((size_t)mblk * KS_W + 2 * ng + s) * 1024 + lane * 16) = pack8(ov);
    }
    STAMP(20);
  }
  STAMPR(31);
  STAMP_FLUSH(0);
  asm volatile(".Lk1_end:");
}

constexpr int XROW = 68;
__global__ __launch_bounds__(256, 1) void k2_kernel(const u16* __restrict__ x, const char* __restrict__ ws, u16* __restrict__ out) {
  __shared__ __attribute__((aligned(16))) float xch[4 * 64 * XROW];
  const int tid = threadIdx.x, lane = tid & 63;
  const int wave = __builtin_amdgcn_readfirstlane(tid >> 6);
  const int b = blockIdx.x, xcd = b & 7, idx = b >> 3;
  const int mt = xcd * 4 + (idx & 3), nt = idx >> 2;
  const int r32 = lane & 31, h = lane >> 5;
  STAMP(0); STAMPR(30);
  f32x16 acc[2][2];
#pragma unroll
  for (int n = 0; n < 2; ++n)
#pragma unroll
    for (int m = 0; m < 2; ++m)
#pragma unroll
      for (int i = 0; i < 16; ++i) acc[n][m][i] = 0.f;
  const int k0 = wave == 0 ? 0 : 32 + (wave - 1) * 29;
  const int nk = wave == 0 ? 32 : (wave == 3 ? 28 : 29);
  const char* wa = ws + WS_WT + ((size_t)(2 * nt) * KS_W + k0) * 1024 + lane * 16;
  const char* wb = wa + (size_t)KS_W * 1024;
  const char* ha = ws + WS_AB + ((size_t)(2 * mt) * KS_W + k0) * 1024 + lane * 16;
  const char* hb = ha + (size_t)KS_W * 1024;
  constexpr int P = 14;
  bf16x8 bufs[P][4];
  auto ld = [&](int i, bf16x8* d) {
    d[0] = *(const bf16x8*)(ha + i * 1024); d[1] = *(const bf16x8*)(hb + i * 1024);
    d[2] = *(const bf16x8*)(wa + i * 1024); d[3] = *(const bf16x8*)(wb + i * 1024);
  };
#pragma unroll
  for (int i = 0; i < P; ++i) ld(i, bufs[i]);
  const int erow = mt * 64 + (tid >> 2), ecol = nt * 64 + (tid & 3) * 16;
  const u32x4 xa = *(const u32x4*)(x + (size_t)erow * D_ + ecol), xb = *(const u32x4*)(x + (size_t)erow * D_ + ecol + 8);
  SB0();
#pragma unroll
  for (int i = 0; i < 28; ++i) {
    bf16x8* d = bufs[i % P];
    acc[0][0] = MFMA(d[2], d[0], acc[0][0]); acc[0][1] = MFMA(d[2], d[1], acc[0][1]);
    acc[1][0] = MFMA(d[3], d[0], acc[1][0]); acc[1][1] = MFMA(d[3], d[1], acc[1][1]);
    SB0();
    if (i + P < 28) ld(i + P, bufs[i % P]);
    else if (i + P < 32) { if (i + P < nk) ld(i + P, bufs[i % P]); }
    SB0();
  }
#pragma unroll
  for (int i = 28; i < 32; ++i) {
    if (i < nk) {
      bf16x8* d = bufs[i % P];
      acc[0][0] = MFMA(d[2], d[0], acc[0][0]); acc[0][1] = MFMA(d[2], d[1], acc[0][1]);
      acc[1][0] = MFMA(d[3], d[0], acc[1][0]); acc[1][1] = MFMA(d[3], d[1], acc[1][1]);
    }
  }
  STAMP(1);
  float* mine = xch + wave * 64 * XROW;
#pragma unroll
  for (int n = 0; n < 2; ++n)
#pragma unroll
    for (int m = 0; m < 2; ++m)
#pragma unroll
      for (int a = 0; a < 4; ++a) {
        f32x4 t = {acc[n][m][4 * a], acc[n][m][4 * a + 1], acc[n][m][4 * a + 2], acc[n][m][4 * a + 3]};
        *(f32x4*)(mine + (32 * m + r32) * XROW + 32 * n + 8 * a + 4 * h) = t;
      }
  __syncthreads();
  STAMP(2);
  const float* rp = xch + (tid >> 2) * XROW + (tid & 3) * 16;
  u32x4 res[2];
#pragma unroll
  for (int q = 0; q < 4; ++q) {
    const f32x4 s0 = *(const f32x4*)(rp + 4 * q), o0 = *(const f32x4*)(rp + 64 * XROW + 4 * q);
    const f32x4 o1 = *(const f32x4*)(rp + 128 * XROW + 4 * q), o2 = *(const f32x4*)(rp + 192 * XROW + 4 * q);
#pragma unroll
    for (int e = 0; e < 4; e += 2) {
      const unsigned su = cvtpk(s0[e], s0[e + 1]);
      const unsigned ou = cvtpk((o0[e] + o1[e]) + o2[e], (o0[e + 1] + o1[e + 1]) + o2[e + 1]);
      const unsigned gu = cvtpk(silu_f(bflo(ou)), silu_f(bfhi(ou)));
      const unsigned tu = cvtpk(bflo(su) * bflo(gu), bfhi(su) * bfhi(gu));
      const unsigned xw = (q < 2 ? xa : xb)[(q & 1) * 2 + (e >> 1)];
      res[q >> 1][(q & 1) * 2 + (e >> 1)] = cvtpk(bflo(xw) + bflo(tu), bfhi(xw) + bfhi(tu));
    }
  }
  *(u32x4*)(out + (size_t)erow * D_ + ecol) = res[0];
  *(u32x4*)(out + (size_t)erow * D_ + ecol + 8) = res[1];
  STAMP(3); STAMPR(31);
}

extern "C" void kernel_launch(void* const* d_in, const int* in_sizes, int n_in, void* d_out, int out_size, void* d_ws, size_t ws_size,
                              hipStream_t stream) {
  const u16* x = (const u16*)d_in[0];
  const u16* wqkv = (const u16*)d_in[1];
  const u16* wphi = (const u16*)d_in[2];
  const u16* wo = (const u16*)d_in[3];
  const u16* w1 = (const u16*)d_in[5];
  const u16* w3 = (const u16*)d_in[6];
  const u16* w2 = (const u16*)d_in[7];
  char* ws = (char*)d_ws;
  k1_kernel<<<256, 512, 0, stream>>>(x, wqkv, wphi, wo, w1, w3, w2, ws);
  k2_kernel<<<256, 256, 0, stream>>>(x, ws, (u16*)d_out);
}
```

```cpp
#include <hip/hip_runtime.h>
#include <hip/hip_bf16.h>
#include <stdint.h>

typedef __attribute__((ext_vector_type(8))) short bf16x8;
typedef __attribute__((ext_vector_type(4))) short s16x4;
typedef __attribute__((ext_vector_type(16))) float f32x16;
typedef __attribute__((ext_vector_type(4))) float f32x4;
typedef __attribute__((ext_vector_type(4))) unsigned u32x4;
typedef __attribute__((ext_vector_type(2))) unsigned u32x2;
typedef unsigned short u16;
#define LAS __attribute__((address_space(3)))
#define DI __device__ __forceinline__
#define MFMA(a, b, c) __builtin_amdgcn_mfma_f32_32x32x16_bf16((a), (b), (c), 0, 0, 0)
#define MFMA16(a, b, c) __builtin_amdgcn_mfma_f32_16x16x32_bf16(__builtin_bit_cast(bf16x8, (a)), __builtin_bit_cast(bf16x8, (b)), (c), 0, 0, 0)

constexpr int S_ = 2048, D_ = 512, NH = 32, F_ = 1376;
constexpr int KS_F = F_ / 16;
constexpr int KS_W = 32 + KS_F;
constexpr size_t WS_AB = 0;
constexpr size_t WS_WT = WS_AB + (size_t)64 * KS_W * 1024;
constexpr size_t WS_P1 = WS_WT + (size_t)16 * KS_W * 1024;
constexpr size_t WS_P2 = WS_P1 + (size_t)16 * 16 * 16 * 1024;
constexpr size_t WS_EP = WS_P2 + (size_t)16 * 16 * 512;
constexpr size_t WS_DUMP = WS_EP + 16 * 128;
constexpr size_t WS_END = WS_DUMP + 2048;

typedef __attribute__((ext_vector_type(2))) __bf16 bf16x2_t;
typedef __attribute__((ext_vector_type(2))) float f32x2;
DI unsigned cvtpk(float lo, float hi) { f32x2 v = {lo, hi}; return __builtin_bit_cast(unsigned, __builtin_convertvector(v, bf16x2_t)); }
DI float bflo(unsigned u) { return __uint_as_float(u << 16); }
DI float bfhi(unsigned u) { return __uint_as_float(u & 0xffff0000u); }
DI float bf16r(float x) { return bflo(cvtpk(x, x)); }
DI void glds16(const void* g, unsigned lds_base) {
  unsigned sv;
  asm volatile("s_mov_b32 %0, m0\n\ts_mov_b32 m0, %2\n\ts_nop 0\n\tglobal_load_lds_dwordx4 %1, off\n\ts_mov_b32 m0, %0"
               : "=&s"(sv) : "v"(g), "s"(lds_base) : "memory");
}
typedef short v4i16_t __attribute__((ext_vector_type(4)));
DI s16x4 trrd(unsigned a) { return __builtin_bit_cast(s16x4, __builtin_amdgcn_ds_read_tr16_b64_v4i16((LAS v4i16_t*)(uintptr_t)a)); }
DI bf16x8 trfrag(unsigned lo, unsigned hi) { s16x4 a = trrd(lo), b = trrd(hi); return __builtin_shufflevector(a, b, 0, 1, 2, 3, 4, 5, 6, 7); }
DI bf16x8 pack8(const float* v) {
  u32x4 p; p[0] = cvtpk(v[0], v[1]); p[1] = cvtpk(v[2], v[3]); p[2] = cvtpk(v[4], v[5]); p[3] = cvtpk(v[6], v[7]);
  return __builtin_bit_cast(bf16x8, p);
}
DI float xhalf(float v) {
  auto rr = __builtin_amdgcn_permlane32_swap(__float_as_uint(v), __float_as_uint(v), false, false);
  unsigned a = rr[0], b = rr[1];
  return __uint_as_float((threadIdx.x & 32) ? a : b);
}
template <int C> DI float dppror(float v) { return __uint_as_float(__builtin_amdgcn_update_dpp(0u, __float_as_uint(v), 0x120 + C, 0xf, 0xf, false)); }
DI float row16sum(float v) { v += dppror<8>(v); v += dppror<4>(v); v += dppror<2>(v); v += dppror<1>(v); return v; }
DI float silu_f(float x) { return x * __builtin_amdgcn_rcpf(1.0f + __builtin_amdgcn_exp2f(-1.4426950408889634f * x)); }
#define STAMP(i)
#define STAMPR(i)
#define STAMP_FLUSH(base)
#define BAR() asm volatile("s_waitcnt lgkmcnt(0)\n\ts_barrier" ::: "memory")
#define WAITV0() asm volatile("s_waitcnt vmcnt(0)" ::: "memory")

enum { T_NONE = 0, T_Q = 1, T_K = 2, T_V = 3, T_G = 4, T_U = 5 };
#define TD(ty, ix) ((unsigned long long)(((ty) << 6) | (ix)))
#define SB0() __builtin_amdgcn_sched_barrier(0)
#define GAS __attribute__((address_space(1)))
typedef unsigned long long u64;
DI float dot2(unsigned a, unsigned b, float c) {
  return __builtin_amdgcn_fdot2_f32_bf16(__builtin_bit_cast(bf16x2_t, a), __builtin_bit_cast(bf16x2_t, b), c, false);
}
DI void st_gran(void* p, unsigned val, unsigned tag) { __hip_atomic_store((GAS u64*)p, ((u64)tag << 32) | val, __ATOMIC_RELAXED, __HIP_MEMORY_SCOPE_AGENT); }
DI u64 ld_gran(const void* p) { return __hip_atomic_load((GAS u64*)p, __ATOMIC_RELAXED, __HIP_MEMORY_SCOPE_AGENT); }
constexpr unsigned SPIN_MAX = 1u << 16;

constexpr int SLOT = 32768;
constexpr int LDS_SCR = 4 * SLOT;
constexpr int LDS_DBG = LDS_SCR + 28672;
constexpr int K1_LDS = 5 * SLOT;

DI void glds16s(unsigned voff, const void* sbase, unsigned lds_base) {
  unsigned sv;
  asm volatile("s_mov_b32 %0, m0\n\ts_mov_b32 m0, %3\n\ts_nop 0\n\tglobal_load_lds_dwordx4 %1, %2\n\ts_mov_b32 m0, %0"
               : "=&s"(sv) : "v"(voff), "s"(sbase), "s"(lds_base) : "memory");
}
DI void center4(u32x4& a, float c) {
  unsigned t0, t1, t2, t3, t4, t5, t6, t7;
  asm("v_dot2_f32_bf16 %0, %8, %12, %14\n\tv_dot2_f32_bf16 %1, %8, %13, %14\n\tv_dot2_f32_bf16 %2, %9, %12, %14\n\tv_dot2_f32_bf16 %3, %9, %13, %14\n\t"
      "v_dot2_f32_bf16 %4, %10, %12, %14\n\tv_dot2_f32_bf16 %5, %10, %13, %14\n\tv_dot2_f32_bf16 %6, %11, %12, %14\n\tv_dot2_f32_bf16 %7, %11, %13, %14\n\t"
      "v_cvt_pk_bf16_f32 %0, %0, %1\n\tv_cvt_pk_bf16_f32 %2, %2, %3\n\tv_cvt_pk_bf16_f32 %4, %4, %5\n\tv_cvt_pk_bf16_f32 %6, %6, %7"
      : "=&v"(t0), "=&v"(t1), "=&v"(t2), "=&v"(t3), "=&v"(t4), "=&v"(t5), "=&v"(t6), "=&v"(t7)
      : "v"(a[0]), "v"(a[1]), "v"(a[2]), "v"(a[3]), "s"(0x00003f80u), "s"(0x3f800000u), "v"(c));
  a[0] = t0; a[1] = t2; a[2] = t4; a[3] = t6;
}
DI float rsum32(float v) { auto rr = __builtin_amdgcn_permlane32_swap(__float_as_uint(v), __float_as_uint(v), false, false); return __uint_as_float(rr[0]) + __uint_as_float(rr[1]); }
DI float rsum16(float v) { auto rr = __builtin_amdgcn_permlane16_swap(__float_as_uint(v), __float_as_uint(v), false, false); return __uint_as_float(rr[0]) + __uint_as_float(rr[1]); }
DI float rmax32(float v) { auto rr = __builtin_amdgcn_permlane32_swap(__float_as_uint(v), __float_as_uint(v), false, false); return fmaxf(__uint_as_float(rr[0]), __uint_as_float(rr[1])); }
DI float rmin32(float v) { auto rr = __builtin_amdgcn_permlane32_swap(__float_as_uint(v), __float_as_uint(v), false, false); return fminf(__uint_as_float(rr[0]), __uint_as_float(rr[1])); }
DI float nv(float x) { asm("" : "+v"(x)); return x; }
#define WAITV(n) asm volatile("s_waitcnt vmcnt(" #n ")" ::: "memory")

#define PRIO_M 2
#define PFD 4
#define LDSP 4
#define PRIO_E 0
template <int RB, class F>
DI void mfma_tile(f32x16& acc, const bf16x8 (&xf)[32], unsigned sb, bool ld, const char* g, unsigned gstep, unsigned voff, unsigned dst, F filler) {
  __builtin_amdgcn_s_setprio(PRIO_M);
  bf16x8 wq[PFD];
#pragma unroll
  for (int ks = 0; ks < PFD; ++ks) { const int o = ((ks >> 2) * 64 + (ks & 3) * 8) * RB; wq[ks] = trfrag(sb + o, sb + o + 4 * RB); }
#pragma unroll
  for (int i = 0; i < 16; ++i) acc[i] = 0.f;
  SB0();
#pragma unroll
  for (int ks = 0; ks < 32; ++ks) {
    acc = MFMA(wq[ks % PFD], xf[ks], acc);
    if (ks + PFD < 32) { const int o = (((ks + PFD) >> 2) * 64 + ((ks + PFD) & 3) * 8) * RB; wq[ks % PFD] = trfrag(sb + o, sb + o + 4 * RB); }
    if ((ks % LDSP) == 0 && ks < 8 * LDSP) { if (ld) glds16s(voff, g + (size_t)(ks / LDSP) * gstep, dst + (ks / LDSP) * 1024); }
    filler(ks);
    SB0();
  }
  __builtin_amdgcn_s_setprio(PRIO_E);
  asm volatile("" : "+v"(acc));
}
DI void feat_proj(const f32x16& acc, const u32x4& wf, int r32, unsigned (&pp)[8]) {
  float av[16];
#pragma unroll
  for (int i = 0; i < 16; ++i) av[i] = acc[i];
  const bf16x8 qb0 = pack8(av), qb1 = pack8(av + 8);
  const u32x4 z4 = {0u, 0u, 0u, 0u};
  const bf16x8 a0 = __builtin_bit_cast(bf16x8, (r32 < 16) ? wf : z4), a1 = __builtin_bit_cast(bf16x8, (r32 < 16) ? z4 : wf);
  f32x16 pq;
#pragma unroll
  for (int i = 0; i < 16; ++i) pq[i] = 0.f;
  SB0();
  __builtin_amdgcn_s_setprio(3);
  pq = MFMA(a0, qb0, pq);
  pq = MFMA(a1, qb1, pq);
  SB0();
  __builtin_amdgcn_s_setprio(PRIO_E);
#pragma unroll
  for (int j = 0; j < 8; ++j) pp[j] = cvtpk(pq[2 * j], pq[2 * j + 1]);
  asm volatile("" : "+v"(pp[0]), "+v"(pp[1]), "+v"(pp[2]), "+v"(pp[3]), "+v"(pp[4]), "+v"(pp[5]), "+v"(pp[6]), "+v"(pp[7]));
}
DI void feat_soft(const unsigned* pp, bf16x8& fp, bf16x8& fm) {
  float pv[8];
#pragma unroll
  for (int j = 0; j < 4; ++j) { pv[2 * j] = bflo(pp[j]); pv[2 * j + 1] = bfhi(pp[j]); }
  float mx = fmaxf(fmaxf(fmaxf(pv[0], pv[1]), pv[2]), fmaxf(fmaxf(pv[3], pv[4]), pv[5])), mn = fminf(fminf(fminf(pv[0], pv[1]), pv[2]), fminf(fminf(pv[3], pv[4]), pv[5]));
  mx = fmaxf(fmaxf(mx, pv[6]), pv[7]); mn = fminf(fminf(mn, pv[6]), pv[7]);
  mx = rmax32(mx); mn = rmin32(mn);
  const float L2E = 1.4426950408889634f, mxl = nv(-mx * L2E), mnl = nv(mn * L2E);
  float ep[8], em[8], sp = 0.f, sm = 0.f;
#pragma unroll
  for (int j = 0; j < 8; ++j) {
    ep[j] = __builtin_amdgcn_exp2f(nv(fmaf(pv[j], L2E, mxl))); sp = nv(sp + ep[j]);
    em[j] = __builtin_amdgcn_exp2f(nv(fmaf(pv[j], -L2E, mnl))); sm = nv(sm + em[j]);
  }
  sp = nv(rsum32(sp)); sm = nv(rsum32(sm));
  const float rp = __builtin_amdgcn_rcpf(sp), rm = __builtin_amdgcn_rcpf(sm);
#pragma unroll
  for (int j = 0; j < 8; ++j) { ep[j] = nv(ep[j] * rp); em[j] = nv(em[j] * rm); }
  fp = pack8(ep); fm = pack8(em);
  asm volatile("" : "+v"(fp), "+v"(fm));
}
#define WT_STORE 1
DI void st16(void* p, u32x4 v) {
  asm volatile("global_store_dwordx4 %0, %1, off sc1\n\ts_nop 1" :: "v"(p), "v"(v) : "memory");
}
DI void epi_g(const f32x16& acc, unsigned (&sg)[8]) {
#pragma unroll
  for (int i = 0; i < 16; i += 2) {
    const unsigned u = cvtpk(acc[i], acc[i + 1]);
    sg[i >> 1] = cvtpk(nv(silu_f(bflo(u))), nv(silu_f(bfhi(u))));
  }
  asm volatile("" : "+v"(sg[0]), "+v"(sg[1]), "+v"(sg[2]), "+v"(sg[3]), "+v"(sg[4]), "+v"(sg[5]), "+v"(sg[6]), "+v"(sg[7]));
}
DI void epi_u(const f32x16& acc, const unsigned (&sg)[8], char* dst) {
  float hv[16];
#pragma unroll
  for (int i = 0; i < 16; i += 2) { const unsigned u = cvtpk(acc[i], acc[i + 1]); hv[i] = nv(bflo(sg[i >> 1]) * bflo(u)); hv[i + 1] = nv(bfhi(sg[i >> 1]) * bfhi(u)); }
  st16(dst, __builtin_bit_cast(u32x4, pack8(hv))); st16(dst + 1024, __builtin_bit_cast(u32x4, pack8(hv + 8)));
}
#define PHASE_END(p) do { STAMP(2 + 2 * (p)); SB0(); BAR(); SB0(); STAMP(3 + 2 * (p)); } while (0)

__global__ __launch_bounds__(512, 2) void k1_kernel(const u16* __restrict__ x, const u16* __restrict__ wqkv, const u16* __restrict__ wphi,
                                                    const u16* __restrict__ wo, const u16* __restrict__ w1, const u16* __restrict__ w3,
                                                    const u16* __restrict__ w2, char* __restrict__ ws) {
  __shared__ __attribute__((aligned(16))) char smem[K1_LDS];
  const int tid = threadIdx.x, lane = tid & 63;
  const int wave = __builtin_amdgcn_readfirstlane(tid >> 6), grp = wave >> 2, wr = wave & 3;
  const int b = blockIdx.x, xcd = b & 7, idx = b >> 3;
  const int mt = (xcd & 1) * 8 + (idx & 7), ng = (xcd >> 1) * 4 + (idx >> 3);
  const int r32 = lane & 31, h = lane >> 5;
  const unsigned lds0 = (unsigned)(uintptr_t)smem;
  unsigned dummy0 = 0;
  if (wave == 0) {
    unsigned long long pc; unsigned len;
    asm volatile("s_getpc_b64 %0\n.Lk1_pc:\n\ts_mov_b32 %1, .Lk2_end-.Lk1_pc" : "=s"(pc), "=s"(len));
    const unsigned off = (lane * 8 + (idx & 7)) * 128;
    if (off < len) asm volatile("global_load_dword %0, %1, %2" : "+v"(dummy0) : "v"(off), "s"(pc) : "memory");
  }
  unsigned dummy1 = 0;
  if (grp == 0) asm volatile("global_load_dword %0, %1, %2" : "+v"(dummy1) : "v"(lane * 128), "s"((const char*)(x + (size_t)(mt * 128 + (idx >> 3) * 32 + wr * 8) * D_)) : "memory");
  const unsigned reg = lds0 + SLOT + (wr * 2 + grp) * 16384;
  {
    const char* xb = (const char*)(x + (size_t)(mt * 128 + wr * 32 + grp * 16) * D_);
    const int rr = lane >> 5, qd = (lane >> 3) & 3, pos = lane & 7;
#pragma unroll
    for (int cc = 0; cc < 2; ++cc)
#pragma unroll
      for (int i = 0; i < 8; ++i)
        glds16s(rr * 1024 + qd * 256 + (pos << 4), xb + i * 2048 + cc * 128, reg + cc * 8192 + i * 1024);
  }
  const int g0 = ng < 11 ? 3 * ng : 33 + 2 * (ng - 11);
  const bool full = ng < 11;
  const char* wK = (const char*)(wqkv + 512 + 32 * ng);
  const char* wG = (const char*)(w1 + 32 * g0);
  const char* wU = (const char*)(w3 + 32 * g0);
  const unsigned voffA = (lane >> 2) * 3072 + (lane & 3) * 16, voffF = (lane >> 2) * 2752 + (lane & 3) * 16;
  unsigned dummy = 0, dummy2 = 0, tagv;
  u32x4 wf, wfb;
  {
    tagv = __hip_atomic_load((GAS unsigned*)(ws + WS_EP + ng * 128), __ATOMIC_RELAXED, __HIP_MEMORY_SCOPE_AGENT);
    const u16* wp = wphi + (size_t)(2 * ng + (r32 >> 4)) * 256 + (4 * h) * 16 + (lane & 15);
#pragma unroll
    for (int j = 0; j < 4; ++j) {
      const int k0 = 8 * (j >> 1) + 2 * (j & 1);
      asm volatile("global_load_ushort %0, %2, off offset:%3\n\tglobal_load_ushort %1, %2, off offset:%4" : "=&v"(wf[j]), "=&v"(wfb[j]) : "v"(wp), "n"(k0 * 32), "n"(k0 * 32 + 32) : "memory");
    }
    if (grp) {
      const char* g = wK + (size_t)wr * 128 * 3072;
#pragma unroll
      for (int i = 0; i < 8; ++i) glds16s(voffA, g + i * 16 * 3072, lds0 + wr * 8192 + i * 1024);
      const int trow = (idx & 7) * 64 + lane;
      const int e = (wr == 3 && !full) ? 0 : wr - 1;
      const char* p = wr == 0 ? wK + 1024 + (size_t)trow * 3072 : wG + 64 * e + (size_t)trow * 2752;
      const char* p2 = wr == 0 ? wK - 1024 + (size_t)trow * 3072 : wU + 64 * e + (size_t)trow * 2752;
      asm volatile("global_load_dword %0, %1, off" : "+v"(dummy) : "v"(p) : "memory");
      asm volatile("global_load_dword %0, %1, off" : "+v"(dummy2) : "v"(p2) : "memory");
    }
  }
  bf16x8 xf[32];
  unsigned tag;
  {
    const int r16 = lane & 15, qd = lane >> 4;
    const unsigned rb = reg + (r16 >> 1) * 1024 + (r16 & 1) * 512 + qd * 128 + ((r16 & 7) << 4);
    u32x4 xr[16];
    float q0 = 0.f, q1 = 0.f;
    f32x4 sum4 = {0.f, 0.f, 0.f, 0.f};
    const u32x4 ones = {0x3f803f80u, 0x3f803f80u, 0x3f803f80u, 0x3f803f80u};
    if (grp) WAITV(27); else WAITV(17);
#pragma unroll
    for (int q = 0; q < 8; ++q) xr[q] = *(const LAS u32x4*)(uintptr_t)(rb ^ (q << 4));
#pragma unroll
    for (int q = 0; q < 8; ++q)
#pragma unroll
      for (int e = 0; e < 4; e += 2) {
        if (e == 0) sum4 = MFMA16(ones, xr[q], sum4);
        q0 = dot2(xr[q][e], xr[q][e], q0); q1 = dot2(xr[q][e + 1], xr[q][e + 1], q1);
      }
    asm volatile("" : "+v"(sum4), "+v"(q0), "+v"(q1));
    if (grp) WAITV(19); else WAITV(9);
#pragma unroll
    for (int q = 0; q < 8; ++q) xr[8 + q] = *(const LAS u32x4*)(uintptr_t)((rb ^ (q << 4)) + 8192);
#pragma unroll
    for (int q = 8; q < 16; ++q)
#pragma unroll
      for (int e = 0; e < 4; e += 2) {
        if (e == 0) sum4 = MFMA16(ones, xr[q], sum4);
        q0 = dot2(xr[q][e], xr[q][e], q0); q1 = dot2(xr[q][e + 1], xr[q][e + 1], q1);
      }
    const float mean = sum4[0] * (1.0f / 512.0f);
    const float var = fmaxf(rsum32(rsum16(q0 + q1)) * (1.0f / 512.0f) - mean * mean, 0.f);
    const float nmb = -bf16r(mean);
    const float rs = 1.0f / bf16r(sqrtf(bf16r(bf16r(var) + 1e-5f)));
    const unsigned wb = reg + qd * 4096 + r16 * 16 + (r16 & 3) * 512 + ((r16 >> 2) & 1) * 256;
#pragma unroll
    for (int q = 0; q < 16; ++q) {
      u32x4 o;
      center4(xr[q], nmb);
#pragma unroll
      for (int e = 0; e < 4; ++e) o[e] = cvtpk(bflo(xr[q][e]) * rs, bfhi(xr[q][e]) * rs);
      *(LAS u32x4*)(uintptr_t)((wb ^ ((q & 3) * 512 + ((q >> 2) & 1) * 256)) + (q >> 3) * 2048) = o;
    }
    SB0();
    WAITV(0);
    asm volatile("" : "+v"(dummy), "+v"(dummy0), "+v"(dummy1), "+v"(dummy2), "+v"(wf), "+v"(wfb));
#pragma unroll
    for (int j = 0; j < 4; ++j) wf[j] |= wfb[j] << 16;
    tag = __builtin_amdgcn_readfirstlane(tagv) + 1u;
    asm volatile("" : "+s"(tag));
    BAR();
    const unsigned xb = lds0 + SLOT * (1 + wr) + (r32 >> 4) * 16384 + (h * 16 + (r32 & 15)) * 16;
#pragma unroll
    for (int ks = 0; ks < 32; ++ks) xf[ks] = *(const LAS bf16x8*)(uintptr_t)(xb + ks * 512);
    BAR();
  }
  STAMP(1);
  const unsigned voffP = (lane >> 3) * 2752 + (((lane & 7) ^ ((lane >> 2) & 4)) << 4);
  const size_t rowP = (size_t)wave * 64 * 2752;
  const int i16 = lane & 15, blk = (lane >> 4) & 1;
  const unsigned tr0 = lds0 + (32 * h + (i16 >> 2)) * 64 + 8 * (4 * blk + (i16 & 3));
  const unsigned trP = lds0 + (32 * h + (i16 >> 2)) * 128 + ((((grp ^ 1) ^ (i16 >> 3)) & 1) << 6) + 8 * (4 * blk + (i16 & 3));
  const int mblk = mt * 4 + wr;
  const unsigned ldd = lds0 + wr * 8192;
  const size_t rowF = (size_t)wr * 128 * 2752, rowA = (size_t)wr * 128 * 3072;
  char* const hdst = ws + WS_AB + ((size_t)mblk * KS_W + 32 + 2 * g0) * 1024 + lane * 16;
  unsigned sg[8], pp[8];
  f32x16 acc, acc2;
  const int row = wr * 32 + r32, sw = (row >> 1) & 7;
  auto nofill = [](int) {};
  if (grp == 0) {
    mfma_tile<64>(acc, xf, tr0, false, nullptr, 0, 0, 0, nofill);
    PHASE_END(0);
    feat_proj(acc, wf, r32, pp);
#pragma unroll
    for (int s = 0; s < 2; ++s) {
      if (s) {
#pragma unroll
        for (int i = 0; i < 4; ++i) glds16s(voffP, wU + rowP + i * 8 * 2752, lds0 + wave * 8192 + i * 1024);
      }
      bf16x8 fp, fm;
      feat_soft(pp + 4 * s, fp, fm);
      if (s) {
#pragma unroll
        for (int i = 4; i < 8; ++i) glds16s(voffP, wU + rowP + i * 8 * 2752, lds0 + wave * 8192 + i * 1024);
      }
      const unsigned pk = lds0 + LDS_SCR + s * 8192 + row * 64;
      const u32x4 up = __builtin_bit_cast(u32x4, fp), um = __builtin_bit_cast(u32x4, fm);
      u32x2 t;
      t[0] = up[0]; t[1] = up[1]; *(LAS u32x2*)(uintptr_t)(pk + 8 * ((h) ^ sw)) = t;
      t[0] = up[2]; t[1] = up[3]; *(LAS u32x2*)(uintptr_t)(pk + 8 * ((2 + h) ^ sw)) = t;
      t[0] = um[0]; t[1] = um[1]; *(LAS u32x2*)(uintptr_t)(pk + 8 * ((4 + h) ^ sw)) = t;
      t[0] = um[2]; t[1] = um[3]; *(LAS u32x2*)(uintptr_t)(pk + 8 * ((6 + h) ^ sw)) = t;
      PHASE_END(1 + s);
    }
    mfma_tile<128>(acc, xf, trP + 2 * SLOT, false, nullptr, 0, 0, 0, nofill);
    WAITV(0);
    PHASE_END(3);
    {
      if (full) {
#pragma unroll
        for (int i = 0; i < 8; ++i) glds16s(voffF, wG + 128 + rowF + i * 16 * 2752, ldd + 3 * SLOT + i * 1024);
      }
      epi_g(acc, sg);
    }
    PHASE_END(4);
    u64 g1[8];
    const char* p1r = ws + WS_P1 + ((size_t)(ng * 16 + mt) * 16 + h * 8) * 1024 + blk * 512 + (wr * 16 + i16) * 8;
    mfma_tile<128>(acc, xf, trP, false, nullptr, 0, 0, 0, [&](int ks) { if (ks >= 8 && ks < 16) g1[ks - 8] = ld_gran(p1r + (ks - 8) * 1024); });
    PHASE_END(5);
    {
      for (unsigned spin = 0; spin < SPIN_MAX; ++spin) {
        bool ok = true;
#pragma unroll
        for (int j = 0; j < 8; ++j) ok &= (unsigned)(g1[j] >> 32) == tag;
        if (__all(ok)) break;
        __builtin_amdgcn_s_sleep(4);
#pragma unroll
        for (int j = 0; j < 8; ++j) g1[j] = ld_gran(p1r + j * 1024);
      }
      float a = 0.f;
#pragma unroll
      for (int j = 0; j < 8; ++j) a = nv(a + __uint_as_float((unsigned)g1[j]));
      a = rsum32(a);
      auto rr = __builtin_amdgcn_permlane16_swap(__float_as_uint(a), __float_as_uint(a), false, false);
      if (lane < 16) st_gran(ws + WS_P2 + (size_t)(ng * 16 + mt) * 512 + (wr * 16 + lane) * 8, cvtpk(__uint_as_float(rr[0]), __uint_as_float(rr[1])), tag);
      if (mt == 0 && wr == 0 && lane == 0) __hip_atomic_store((GAS unsigned*)(ws + WS_EP + ng * 128), tag, __ATOMIC_RELAXED, __HIP_MEMORY_SCOPE_AGENT);
      SB0();
      if (full) {
#pragma unroll
        for (int i = 0; i < 8; ++i) glds16s(voffF, wU + 128 + rowF + i * 16 * 2752, ldd + i * 1024);
      }
      epi_u(acc, sg, hdst + 2048);
    }
    PHASE_END(6);
    if (full) mfma_tile<64>(acc, xf, tr0 + 3 * SLOT, false, nullptr, 0, 0, 0, nofill);
    WAITV(0);
    PHASE_END(7);
    if (full) {
      mfma_tile<64>(acc2, xf, tr0, false, nullptr, 0, 0, 0, nofill);
      STAMP(18);
      epi_g(acc, sg);
      epi_u(acc2, sg, hdst + 4096);
    }
    STAMP(20);
  } else {
#pragma unroll
    for (int i = 0; i < 8; ++i) glds16s(voffA, wK + 1024 + rowA + i * 16 * 3072, ldd + SLOT + i * 1024);
#pragma unroll
    for (int i = 0; i < 16; ++i) glds16s(voffP, wG + rowF + i * 8 * 2752, lds0 + 2 * SLOT + wr * 16384 + i * 1024);
    u32x4 rta[2], rtb[2];
    const int gw = b * 8 + wr * 2;
#pragma unroll
    for (int f = 0; f < 2; ++f) {
      const int gc = gw + f < 16 * KS_W ? gw + f : 0, nb = gc / KS_W, ks = gc - nb * KS_W;
      const u16* src = (ks < 32 ? wo + (size_t)ks * 16 * D_ : w2 + (size_t)(ks - 32) * 16 * D_) + nb * 32 + r32 + (size_t)(4 * h) * D_;
#pragma unroll
      for (int j = 0; j < 4; ++j) {
        const int k0 = 8 * (j >> 1) + 2 * (j & 1);
        asm volatile("global_load_ushort %0, %2, off\n\tglobal_load_ushort %1, %2, off offset:1024" : "=&v"(rta[f][j]), "=&v"(rtb[f][j]) : "v"(src + (size_t)k0 * D_) : "memory");
      }
    }
    WAITV(32);
    PHASE_END(0);
    mfma_tile<64>(acc2, xf, tr0 + SLOT, false, nullptr, 0, 0, 0, nofill);
    WAITV(16);
    PHASE_END(1);
    {
      const unsigned vi = lds0 + LDS_SCR + 16384 + row * 64;
      mfma_tile<128>(acc, xf, trP + 2 * SLOT, true, wU + rowP, 8 * 2752, voffP, lds0 + wave * 8192, [&](int ks) {
        if (ks >= 1 && ks <= 4) {
          const int a = ks - 1;
          u32x2 t; t[0] = cvtpk(acc2[4 * a], acc2[4 * a + 1]); t[1] = cvtpk(acc2[4 * a + 2], acc2[4 * a + 3]);
          *(LAS u32x2*)(uintptr_t)(vi + 8 * ((2 * a + h) ^ sw)) = t;
        }
      });
    }
    PHASE_END(2);
    if (wr < 2) {
      f32x16 a2;
#pragma unroll
      for (int i = 0; i < 16; ++i) a2[i] = 0.f;
      const int rowq = 8 * h + (i16 >> 2);
      const int ca = 4 * blk + (lane & 3), cb = 4 * (blk ^ wr) + (lane & 3);
      const int sw0 = (rowq >> 1) & 7, sw1 = ((rowq + 4) >> 1) & 7;
      const unsigned pk = lds0 + LDS_SCR + wr * 8192 + rowq * 64, vi = lds0 + LDS_SCR + 16384 + rowq * 64;
      const unsigned pa0 = pk + 8 * (ca ^ sw0), pa1 = pk + 256 + 8 * (ca ^ sw1), pb0 = vi + 8 * (cb ^ sw0), pb1 = vi + 256 + 8 * (cb ^ sw1);
      __builtin_amdgcn_s_setprio(3);
#pragma unroll
      for (int ks = 0; ks < 8; ++ks) {
        bf16x8 af = trfrag(pa0 + ks * 1024, pa1 + ks * 1024);
        bf16x8 bfr = trfrag(pb0 + ks * 1024, pb1 + ks * 1024);
        if (blk) {
#pragma unroll
          for (int j = 0; j < 8; ++j) bfr[j] = (short)0x3f80;
        }
        a2 = MFMA(af, bfr, a2);
      }
      asm volatile("" : "+v"(a2));
      __builtin_amdgcn_s_setprio(PRIO_E);
      char* p1w = ws + WS_P1 + (((size_t)(ng * 16 + wr * 8) * 16 + mt) * 1024) + lane * 8;
#pragma unroll
      for (int i = 0; i < 16; ++i) st_gran(p1w + (size_t)(i >> 1) * 16384 + (i & 1) * 512, __float_as_uint(a2[i]), tag);
      WAITV(24);
    } else {
      WAITV(8);
    }
    {
      asm volatile("" : "+v"(rta[0]), "+v"(rta[1]), "+v"(rtb[0]), "+v"(rtb[1]));
#pragma unroll
      for (int f = 0; f < 2; ++f) {
#pragma unroll
        for (int j = 0; j < 4; ++j) rta[f][j] |= rtb[f][j] << 16;
        st16(ws + (gw + f < 16 * KS_W ? WS_WT + (size_t)(gw + f) * 1024 : WS_DUMP + f * 1024) + lane * 16, rta[f]);
      }
    }
    if (wr < 2) WAITV(18); else WAITV(2);
    PHASE_END(3);
    mfma_tile<128>(acc2, xf, trP, true, wK - 1024 + rowA, 16 * 3072, voffA, ldd + 2 * SLOT, nofill);
    PHASE_END(4);
    epi_g(acc, sg);
    epi_u(acc2, sg, hdst);
    WAITV(2);
    PHASE_END(5);
    mfma_tile<64>(acc, xf, tr0 + 2 * SLOT, false, nullptr, 0, 0, 0, nofill);
    PHASE_END(6);
    bf16x8 fp[2], fm[2];
    feat_proj(acc, wf, r32, pp);
    feat_soft(pp, fp[0], fm[0]);
    PHASE_END(7);
    const char* p2r = ws + WS_P2 + (size_t)(ng * 16) * 512 + lane * 8;
    u64 g2[16];
#pragma unroll
    for (int j = 0; j < 16; ++j) g2[j] = ld_gran(p2r + j * 512);
    SB0();
    feat_soft(pp + 4, fp[1], fm[1]);
    STAMP(18);
    for (unsigned spin = 0; spin < SPIN_MAX; ++spin) {
      bool ok = true;
#pragma unroll
      for (int j = 0; j < 16; ++j) ok &= (unsigned)(g2[j] >> 32) == tag;
      if (__all(ok)) break;
      __builtin_amdgcn_s_sleep(4);
#pragma unroll
      for (int j = 0; j < 16; ++j) g2[j] = ld_gran(p2r + j * 512);
    }
    STAMP(19);
    __builtin_amdgcn_s_setprio(3);
#pragma unroll
    for (int s = 0; s < 2; ++s) {
      const u32x4 f0 = {(unsigned)g2[8 * s], (unsigned)g2[8 * s + 1], (unsigned)g2[8 * s + 2], (unsigned)g2[8 * s + 3]};
      const u32x4 f1 = {(unsigned)g2[8 * s + 4], (unsigned)g2[8 * s + 5], (unsigned)g2[8 * s + 6], (unsigned)g2[8 * s + 7]};
      f32x16 o;
#pragma unroll
      for (int q = 0; q < 16; ++q) o[q] = 0.f;
      o = MFMA(__builtin_bit_cast(bf16x8, f0), fp[s], o);
      o = MFMA(__builtin_bit_cast(bf16x8, f1), fm[s], o);
      const float rd = __builtin_amdgcn_rcpf(bf16r(bf16r(o[8]) + 1e-6f));
      float ov[8];
#pragma unroll
      for (int j = 0; j < 8; j += 2) { const unsigned u = cvtpk(o[j], o[j + 1]); ov[j] = nv(bflo(u) * rd); ov[j + 1] = nv(bfhi(u) * rd); }
      st16(ws + WS_AB + ((size_t)mblk * KS_W + 2 * ng + s) * 1024 + lane * 16, __builtin_bit_cast(u32x4, pack8(ov)));
    }
    STAMP(20);
  }
  STAMPR(31);
  STAMP_FLUSH(0);
  asm volatile(".Lk1_end:");
}

constexpr int XROW = 68;
__global__ __launch_bounds__(256, 1) void k2_kernel(const u16* __restrict__ x, const char* __restrict__ ws, u16* __restrict__ out) {
  __shared__ __attribute__((aligned(16))) float xch[4 * 64 * XROW];
  const int tid = threadIdx.x, lane = tid & 63;
  const int wave = __builtin_amdgcn_readfirstlane(tid >> 6);
  const int b = blockIdx.x, xcd = b & 7, idx = b >> 3;
  const int mt = xcd * 4 + (idx & 3), nt = idx >> 2;
  const int r32 = lane & 31, h = lane >> 5;
  STAMP(0); STAMPR(30);
  f32x16 acc[2][2];
#pragma unroll
  for (int n = 0; n < 2; ++n)
#pragma unroll
    for (int m = 0; m < 2; ++m)
#pragma unroll
      for (int i = 0; i < 16; ++i) acc[n][m][i] = 0.f;
  const int k0 = wave == 0 ? 0 : 32 + (wave - 1) * 29;
  const int nk = wave == 0 ? 32 : (wave == 3 ? 28 : 29);
  const char* wa = ws + WS_WT + ((size_t)(2 * nt) * KS_W + k0) * 1024 + lane * 16;
  const char* wb = wa + (size_t)KS_W * 1024;
  const char* ha = ws + WS_AB + ((size_t)(2 * mt) * KS_W + k0) * 1024 + lane * 16;
  const char* hb = ha + (size_t)KS_W * 1024;
  constexpr int P = 14;
  bf16x8 bufs[P][4];
  auto ld = [&](int i, bf16x8* d) {
    d[0] = *(const bf16x8*)(ha + i * 1024); d[1] = *(const bf16x8*)(hb + i * 1024);
    d[2] = *(const bf16x8*)(wa + i * 1024); d[3] = *(const bf16x8*)(wb + i * 1024);
  };
#pragma unroll
  for (int i = 0; i < P; ++i) ld(i, bufs[i]);
  const int erow = mt * 64 + (tid >> 2), ecol = nt * 64 + (tid & 3) * 16;
  const u32x4 xa = *(const u32x4*)(x + (size_t)erow * D_ + ecol), xb = *(const u32x4*)(x + (size_t)erow * D_ + ecol + 8);
  SB0();
#pragma unroll
  for (int i = 0; i < 28; ++i) {
    bf16x8* d = bufs[i % P];
    acc[0][0] = MFMA(d[2], d[0], acc[0][0]); acc[0][1] = MFMA(d[2], d[1], acc[0][1]);
    acc[1][0] = MFMA(d[3], d[0], acc[1][0]); acc[1][1] = MFMA(d[3], d[1], acc[1][1]);
    SB0();
    if (i + P < 28) ld(i + P, bufs[i % P]);
    else if (i + P < 32) { if (i + P < nk) ld(i + P, bufs[i % P]); }
    SB0();
  }
#pragma unroll
  for (int i = 28; i < 32; ++i) {
    if (i < nk) {
      bf16x8* d = bufs[i % P];
      acc[0][0] = MFMA(d[2], d[0], acc[0][0]); acc[0][1] = MFMA(d[2], d[1], acc[0][1]);
      acc[1][0] = MFMA(d[3], d[0], acc[1][0]); acc[1][1] = MFMA(d[3], d[1], acc[1][1]);
    }
  }
  STAMP(1);
  float* mine = xch + wave * 64 * XROW;
#pragma unroll
  for (int n = 0; n < 2; ++n)
#pragma unroll
    for (int m = 0; m < 2; ++m)
#pragma unroll
      for (int a = 0; a < 4; ++a) {
        f32x4 t = {acc[n][m][4 * a], acc[n][m][4 * a + 1], acc[n][m][4 * a + 2], acc[n][m][4 * a + 3]};
        *(f32x4*)(mine + (32 * m + r32) * XROW + 32 * n + 8 * a + 4 * h) = t;
      }
  __syncthreads();
  STAMP(2);
  const float* rp = xch + (tid >> 2) * XROW + (tid & 3) * 16;
  u32x4 res[2];
#pragma unroll
  for (int q = 0; q < 4; ++q) {
    const f32x4 s0 = *(const f32x4*)(rp + 4 * q), o0 = *(const f32x4*)(rp + 64 * XROW + 4 * q);
    const f32x4 o1 = *(const f32x4*)(rp + 128 * XROW + 4 * q), o2 = *(const f32x4*)(rp + 192 * XROW + 4 * q);
#pragma unroll
    for (int e = 0; e < 4; e += 2) {
      const unsigned su = cvtpk(s0[e], s0[e + 1]);
      const unsigned ou = cvtpk((o0[e] + o1[e]) + o2[e], (o0[e + 1] + o1[e + 1]) + o2[e + 1]);
      const unsigned gu = cvtpk(silu_f(bflo(ou)), silu_f(bfhi(ou)));
      const unsigned tu = cvtpk(bflo(su) * bflo(gu), bfhi(su) * bfhi(gu));
      const unsigned xw = (q < 2 ? xa : xb)[(q & 1) * 2 + (e >> 1)];
      res[q >> 1][(q & 1) * 2 + (e >> 1)] = cvtpk(bflo(xw) + bflo(tu), bfhi(xw) + bfhi(tu));
    }
  }
  *(u32x4*)(out + (size_t)erow * D_ + ecol) = res[0];
  *(u32x4*)(out + (size_t)erow * D_ + ecol + 8) = res[1];
  STAMP(3); STAMPR(31);
  asm volatile(".Lk2_end:");
}

extern "C" void kernel_launch(void* const* d_in, const int* in_sizes, int n_in, void* d_out, int out_size, void* d_ws, size_t ws_size,
                              hipStream_t stream) {
  const u16* x = (const u16*)d_in[0];
  const u16* wqkv = (const u16*)d_in[1];
  const u16* wphi = (const u16*)d_in[2];
  const u16* wo = (const u16*)d_in[3];
  const u16* w1 = (const u16*)d_in[5];
  const u16* w3 = (const u16*)d_in[6];
  const u16* w2 = (const u16*)d_in[7];
  char* ws = (char*)d_ws;
  k1_kernel<<<256, 512, 0, stream>>>(x, wqkv, wphi, wo, w1, w3, w2, ws);
  k2_kernel<<<256, 256, 0, stream>>>(x, ws, (u16*)d_out);
}
```

```cpp
#include <hip/hip_runtime.h>
#include <hip/hip_bf16.h>
#include <stdint.h>

typedef __attribute__((ext_vector_type(8))) short bf16x8;
typedef __attribute__((ext_vector_type(4))) short s16x4;
typedef __attribute__((ext_vector_type(16))) float f32x16;
typedef __attribute__((ext_vector_type(4))) float f32x4;
typedef __attribute__((ext_vector_type(4))) unsigned u32x4;
typedef __attribute__((ext_vector_type(2))) unsigned u32x2;
typedef unsigned short u16;
#define LAS __attribute__((address_space(3)))
#define DI __device__ __forceinline__
#define MFMA(a, b, c) __builtin_amdgcn_mfma_f32_32x32x16_bf16((a), (b), (c), 0, 0, 0)
#define MFMA16(a, b, c) __builtin_amdgcn_mfma_f32_16x16x32_bf16(__builtin_bit_cast(bf16x8, (a)), __builtin_bit_cast(bf16x8, (b)), (c), 0, 0, 0)

constexpr int S_ = 2048, D_ = 512, NH = 32, F_ = 1376;
constexpr int KS_F = F_ / 16;
constexpr int KS_W = 32 + KS_F;
constexpr size_t WS_AB = 0;
constexpr size_t WS_WT = WS_AB + (size_t)64 * KS_W * 1024;
constexpr size_t WS_P1 = WS_WT + (size_t)16 * KS_W * 1024;
constexpr size_t WS_P2 = WS_P1 + (size_t)16 * 16 * 16 * 1024;
constexpr size_t WS_EP = WS_P2 + (size_t)16 * 16 * 512;
constexpr size_t WS_DUMP = WS_EP + 16 * 128;
constexpr size_t WS_END = WS_DUMP + 2048;

typedef __attribute__((ext_vector_type(2))) __bf16 bf16x2_t;
typedef __attribute__((ext_vector_type(2))) float f32x2;
DI unsigned cvtpk(float lo, float hi) { f32x2 v = {lo, hi}; return __builtin_bit_cast(unsigned, __builtin_convertvector(v, bf16x2_t)); }
DI float bflo(unsigned u) { return __uint_as_float(u << 16); }
DI float bfhi(unsigned u) { return __uint_as_float(u & 0xffff0000u); }
DI float bf16r(float x) { return bflo(cvtpk(x, x)); }
DI void glds16(const void* g, unsigned lds_base) {
  unsigned sv;
  asm volatile("s_mov_b32 %0, m0\n\ts_mov_b32 m0, %2\n\ts_nop 0\n\tglobal_load_lds_dwordx4 %1, off\n\ts_mov_b32 m0, %0"
               : "=&s"(sv) : "v"(g), "s"(lds_base) : "memory");
}
typedef short v4i16_t __attribute__((ext_vector_type(4)));
DI s16x4 trrd(unsigned a) { return __builtin_bit_cast(s16x4, __builtin_amdgcn_ds_read_tr16_b64_v4i16((LAS v4i16_t*)(uintptr_t)a)); }
DI bf16x8 trfrag(unsigned lo, unsigned hi) { s16x4 a = trrd(lo), b = trrd(hi); return __builtin_shufflevector(a, b, 0, 1, 2, 3, 4, 5, 6, 7); }
DI bf16x8 pack8(const float* v) {
  u32x4 p; p[0] = cvtpk(v[0], v[1]); p[1] = cvtpk(v[2], v[3]); p[2] = cvtpk(v[4], v[5]); p[3] = cvtpk(v[6], v[7]);
  return __builtin_bit_cast(bf16x8, p);
}
DI float xhalf(float v) {
  auto rr = __builtin_amdgcn_permlane32_swap(__float_as_uint(v), __float_as_uint(v), false, false);
  unsigned a = rr[0], b = rr[1];
  return __uint_as_float((threadIdx.x & 32) ? a : b);
}
template <int C> DI float dppror(float v) { return __uint_as_float(__builtin_amdgcn_update_dpp(0u, __float_as_uint(v), 0x120 + C, 0xf, 0xf, false)); }
DI float row16sum(float v) { v += dppror<8>(v); v += dppror<4>(v); v += dppror<2>(v); v += dppror<1>(v); return v; }
DI float silu_f(float x) { return x * __builtin_amdgcn_rcpf(1.0f + __builtin_amdgcn_exp2f(-1.4426950408889634f * x)); }
#define STAMP(i)
#define STAMPR(i)
#define STAMP_FLUSH(base)
#define BAR() asm volatile("s_waitcnt lgkmcnt(0)\n\ts_barrier" ::: "memory")
#define WAITV0() asm volatile("s_waitcnt vmcnt(0)" ::: "memory")

enum { T_NONE = 0, T_Q = 1, T_K = 2, T_V = 3, T_G = 4, T_U = 5 };
#define TD(ty, ix) ((unsigned long long)(((ty) << 6) | (ix)))
#define SB0() __builtin_amdgcn_sched_barrier(0)
#define GAS __attribute__((address_space(1)))
typedef unsigned long long u64;
DI float dot2(unsigned a, unsigned b, float c) {
  return __builtin_amdgcn_fdot2_f32_bf16(__builtin_bit_cast(bf16x2_t, a), __builtin_bit_cast(bf16x2_t, b), c, false);
}
DI void st_gran(void* p, unsigned val, unsigned tag) { __hip_atomic_store((GAS u64*)p, ((u64)tag << 32) | val, __ATOMIC_RELAXED, __HIP_MEMORY_SCOPE_AGENT); }
DI u64 ld_gran(const void* p) { return __hip_atomic_load((GAS u64*)p, __ATOMIC_RELAXED, __HIP_MEMORY_SCOPE_AGENT); }
constexpr unsigned SPIN_MAX = 1u << 16;

constexpr int SLOT = 32768;
constexpr int LDS_SCR = 4 * SLOT;
constexpr int LDS_DBG = LDS_SCR + 28672;
constexpr int K1_LDS = 5 * SLOT;

DI void glds16s(unsigned voff, const void* sbase, unsigned lds_base) {
  unsigned sv;
  asm volatile("s_mov_b32 %0, m0\n\ts_mov_b32 m0, %3\n\ts_nop 0\n\tglobal_load_lds_dwordx4 %1, %2\n\ts_mov_b32 m0, %0"
               : "=&s"(sv) : "v"(voff), "s"(sbase), "s"(lds_base) : "memory");
}
DI void center4(u32x4& a, float c) {
  unsigned t0, t1, t2, t3, t4, t5, t6, t7;
  asm("v_dot2_f32_bf16 %0, %8, %12, %14\n\tv_dot2_f32_bf16 %1, %8, %13, %14\n\tv_dot2_f32_bf16 %2, %9, %12, %14\n\tv_dot2_f32_bf16 %3, %9, %13, %14\n\t"
      "v_dot2_f32_bf16 %4, %10, %12, %14\n\tv_dot2_f32_bf16 %5, %10, %13, %14\n\tv_dot2_f32_bf16 %6, %11, %12, %14\n\tv_dot2_f32_bf16 %7, %11, %13, %14\n\t"
      "v_cvt_pk_bf16_f32 %0, %0, %1\n\tv_cvt_pk_bf16_f32 %2, %2, %3\n\tv_cvt_pk_bf16_f32 %4, %4, %5\n\tv_cvt_pk_bf16_f32 %6, %6, %7"
      : "=&v"(t0), "=&v"(t1), "=&v"(t2), "=&v"(t3), "=&v"(t4), "=&v"(t5), "=&v"(t6), "=&v"(t7)
      : "v"(a[0]), "v"(a[1]), "v"(a[2]), "v"(a[3]), "s"(0x00003f80u), "s"(0x3f800000u), "v"(c));
  a[0] = t0; a[1] = t2; a[2] = t4; a[3] = t6;
}
DI float rsum32(float v) { auto rr = __builtin_amdgcn_permlane32_swap(__float_as_uint(v), __float_as_uint(v), false, false); return __uint_as_float(rr[0]) + __uint_as_float(rr[1]); }
DI float rsum16(float v) { auto rr = __builtin_amdgcn_permlane16_swap(__float_as_uint(v), __float_as_uint(v), false, false); return __uint_as_float(rr[0]) + __uint_as_float(rr[1]); }
DI float rmax32(float v) { auto rr = __builtin_amdgcn_permlane32_swap(__float_as_uint(v), __float_as_uint(v), false, false); return fmaxf(__uint_as_float(rr[0]), __uint_as_float(rr[1])); }
DI float rmin32(float v) { auto rr = __builtin_amdgcn_permlane32_swap(__float_as_uint(v), __float_as_uint(v), false, false); return fminf(__uint_as_float(rr[0]), __uint_as_float(rr[1])); }
DI float nv(float x) { asm("" : "+v"(x)); return x; }
#define WAITV(n) asm volatile("s_waitcnt vmcnt(" #n ")" ::: "memory")

#define PRIO_M 2
#define PFD 4
#define LDSP 4
#define PRIO_E 0
template <int RB, class F>
DI void mfma_tile(f32x16& acc, const bf16x8 (&xf)[32], unsigned sb, bool ld, const char* g, unsigned gstep, unsigned voff, unsigned dst, F filler) {
  __builtin_amdgcn_s_setprio(PRIO_M);
  bf16x8 wq[PFD];
#pragma unroll
  for (int ks = 0; ks < PFD; ++ks) { const int o = ((ks >> 2) * 64 + (ks & 3) * 8) * RB; wq[ks] = trfrag(sb + o, sb + o + 4 * RB); }
#pragma unroll
  for (int i = 0; i < 16; ++i) acc[i] = 0.f;
  SB0();
#pragma unroll
  for (int ks = 0; ks < 32; ++ks) {
    acc = MFMA(wq[ks % PFD], xf[ks], acc);
    if (ks + PFD < 32) { const int o = (((ks + PFD) >> 2) * 64 + ((ks + PFD) & 3) * 8) * RB; wq[ks % PFD] = trfrag(sb + o, sb + o + 4 * RB); }
    if ((ks % LDSP) == 0 && ks < 8 * LDSP) { if (ld) glds16s(voff, g + (size_t)(ks / LDSP) * gstep, dst + (ks / LDSP) * 1024); }
    filler(ks);
    SB0();
  }
  __builtin_amdgcn_s_setprio(PRIO_E);
  asm volatile("" : "+v"(acc));
}
DI void feat_proj(const f32x16& acc, const u32x4& wf, int r32, unsigned (&pp)[8]) {
  float av[16];
#pragma unroll
  for (int i = 0; i < 16; ++i) av[i] = acc[i];
  const bf16x8 qb0 = pack8(av), qb1 = pack8(av + 8);
  const u32x4 z4 = {0u, 0u, 0u, 0u};
  const bf16x8 a0 = __builtin_bit_cast(bf16x8, (r32 < 16) ? wf : z4), a1 = __builtin_bit_cast(bf16x8, (r32 < 16) ? z4 : wf);
  f32x16 pq;
#pragma unroll
  for (int i = 0; i < 16; ++i) pq[i] = 0.f;
  SB0();
  __builtin_amdgcn_s_setprio(3);
  pq = MFMA(a0, qb0, pq);
  pq = MFMA(a1, qb1, pq);
  SB0();
  __builtin_amdgcn_s_setprio(PRIO_E);
#pragma unroll
  for (int j = 0; j < 8; ++j) pp[j] = cvtpk(pq[2 * j], pq[2 * j + 1]);
  asm volatile("" : "+v"(pp[0]), "+v"(pp[1]), "+v"(pp[2]), "+v"(pp[3]), "+v"(pp[4]), "+v"(pp[5]), "+v"(pp[6]), "+v"(pp[7]));
}
DI void feat_soft(const unsigned* pp, bf16x8& fp, bf16x8& fm) {
  float pv[8];
#pragma unroll
  for (int j = 0; j < 4; ++j) { pv[2 * j] = bflo(pp[j]); pv[2 * j + 1] = bfhi(pp[j]); }
  float mx = fmaxf(fmaxf(fmaxf(pv[0], pv[1]), pv[2]), fmaxf(fmaxf(pv[3], pv[4]), pv[5])), mn = fminf(fminf(fminf(pv[0], pv[1]), pv[2]), fminf(fminf(pv[3], pv[4]), pv[5]));
  mx = fmaxf(fmaxf(mx, pv[6]), pv[7]); mn = fminf(fminf(mn, pv[6]), pv[7]);
  mx = rmax32(mx); mn = rmin32(mn);
  const float L2E = 1.4426950408889634f, mxl = nv(-mx * L2E), mnl = nv(mn * L2E);
  float ep[8], em[8], sp = 0.f, sm = 0.f;
#pragma unroll
  for (int j = 0; j < 8; ++j) {
    ep[j] = __builtin_amdgcn_exp2f(nv(fmaf(pv[j], L2E, mxl))); sp = nv(sp + ep[j]);
    em[j] = __builtin_amdgcn_exp2f(nv(fmaf(pv[j], -L2E, mnl))); sm = nv(sm + em[j]);
  }
  sp = nv(rsum32(sp)); sm = nv(rsum32(sm));
  const float rp = __builtin_amdgcn_rcpf(sp), rm = __builtin_amdgcn_rcpf(sm);
#pragma unroll
  for (int j = 0; j < 8; ++j) { ep[j] = nv(ep[j] * rp); em[j] = nv(em[j] * rm); }
  fp = pack8(ep); fm = pack8(em);
  asm volatile("" : "+v"(fp), "+v"(fm));
}
#define WT_STORE 1
DI void st16(void* p, u32x4 v) {
  asm volatile("global_store_dwordx4 %0, %1, off sc1\n\ts_nop 1" :: "v"(p), "v"(v) : "memory");
}
DI void epi_g(const f32x16& acc, unsigned (&sg)[8]) {
#pragma unroll
  for (int i = 0; i < 16; i += 2) {
    const unsigned u = cvtpk(acc[i], acc[i + 1]);
    sg[i >> 1] = cvtpk(nv(silu_f(bflo(u))), nv(silu_f(bfhi(u))));
  }
  asm volatile("" : "+v"(sg[0]), "+v"(sg[1]), "+v"(sg[2]), "+v"(sg[3]), "+v"(sg[4]), "+v"(sg[5]), "+v"(sg[6]), "+v"(sg[7]));
}
DI void epi_u(const f32x16& acc, const unsigned (&sg)[8], char* dst) {
  float hv[16];
#pragma unroll
  for (int i = 0; i < 16; i += 2) { const unsigned u = cvtpk(acc[i], acc[i + 1]); hv[i] = nv(bflo(sg[i >> 1]) * bflo(u)); hv[i + 1] = nv(bfhi(sg[i >> 1]) * bfhi(u)); }
  st16(dst, __builtin_bit_cast(u32x4, pack8(hv))); st16(dst + 1024, __builtin_bit_cast(u32x4, pack8(hv + 8)));
}
#define PHASE_END(p) do { STAMP(2 + 2 * (p)); SB0(); BAR(); SB0(); STAMP(3 + 2 * (p)); } while (0)

__global__ __launch_bounds__(512, 2) void k1_kernel(const u16* __restrict__ x, const u16* __restrict__ wqkv, const u16* __restrict__ wphi,
                                                    const u16* __restrict__ wo, const u16* __restrict__ w1, const u16* __restrict__ w3,
                                                    const u16* __restrict__ w2, char* __restrict__ ws) {
  __shared__ __attribute__((aligned(16))) char smem[K1_LDS];
  const int tid = threadIdx.x, lane = tid & 63;
  const int wave = __builtin_amdgcn_readfirstlane(tid >> 6), grp = wave >> 2, wr = wave & 3;
  const int b = blockIdx.x, xcd = b & 7, idx = b >> 3;
  const int mt = (xcd & 1) * 8 + (idx & 7), ng = (xcd >> 1) * 4 + (idx >> 3);
  const int r32 = lane & 31, h = lane >> 5;
  const unsigned lds0 = (unsigned)(uintptr_t)smem;
  unsigned dummy0 = 0;
  if (wave == 0) {
    unsigned long long pc; unsigned len;
    asm volatile("s_getpc_b64 %0\n.Lk1_pc:\n\ts_mov_b32 %1, .Lk2_end-.Lk1_pc" : "=s"(pc), "=s"(len));
    const unsigned off = (lane * 8 + (idx & 7)) * 128;
    if (off < len) asm volatile("global_load_dword %0, %1, %2" : "+v"(dummy0) : "v"(off), "s"(pc) : "memory");
  }
  unsigned dummy1 = 0;
#define XTOUCH 0
  if (XTOUCH && grp == 0) asm volatile("global_load_dword %0, %1, %2" : "+v"(dummy1) : "v"(lane * 128), "s"((const char*)(x + (size_t)(mt * 128 + (idx >> 3) * 32 + wr * 8) * D_)) : "memory");
  const unsigned reg = lds0 + SLOT + (wr * 2 + grp) * 16384;
  {
    const char* xb = (const char*)(x + (size_t)(mt * 128 + wr * 32 + grp * 16) * D_);
    const int rr = lane >> 5, qd = (lane >> 3) & 3, pos = lane & 7;
#pragma unroll
    for (int cc = 0; cc < 2; ++cc)
#pragma unroll
      for (int i = 0; i < 8; ++i)
        glds16s(rr * 1024 + qd * 256 + (pos << 4), xb + i * 2048 + cc * 128, reg + cc * 8192 + i * 1024);
  }
  const int g0 = ng < 11 ? 3 * ng : 33 + 2 * (ng - 11);
  const bool full = ng < 11;
  const char* wK = (const char*)(wqkv + 512 + 32 * ng);
  const char* wG = (const char*)(w1 + 32 * g0);
  const char* wU = (const char*)(w3 + 32 * g0);
  const unsigned voffA = (lane >> 2) * 3072 + (lane & 3) * 16, voffF = (lane >> 2) * 2752 + (lane & 3) * 16;
  unsigned dummy = 0, dummy2 = 0, tagv;
  u32x4 wf, wfb;
  {
    tagv = __hip_atomic_load((GAS unsigned*)(ws + WS_EP + ng * 128), __ATOMIC_RELAXED, __HIP_MEMORY_SCOPE_AGENT);
    const u16* wp = wphi + (size_t)(2 * ng + (r32 >> 4)) * 256 + (4 * h) * 16 + (lane & 15);
#pragma unroll
    for (int j = 0; j < 4; ++j) {
      const int k0 = 8 * (j >> 1) + 2 * (j & 1);
      asm volatile("global_load_ushort %0, %2, off offset:%3\n\tglobal_load_ushort %1, %2, off offset:%4" : "=&v"(wf[j]), "=&v"(wfb[j]) : "v"(wp), "n"(k0 * 32), "n"(k0 * 32 + 32) : "memory");
    }
    if (grp) {
      const char* g = wK + (size_t)wr * 128 * 3072;
#pragma unroll
      for (int i = 0; i < 8; ++i) glds16s(voffA, g + i * 16 * 3072, lds0 + wr * 8192 + i * 1024);
      const int trow = (idx & 7) * 64 + lane;
      const int e = (wr == 3 && !full) ? 0 : wr - 1;
      const char* p = wr == 0 ? wK + 1024 + (size_t)trow * 3072 : wG + 64 * e + (size_t)trow * 2752;
      const char* p2 = wr == 0 ? wK - 1024 + (size_t)trow * 3072 : wU + 64 * e + (size_t)trow * 2752;
      asm volatile("global_load_dword %0, %1, off" : "+v"(dummy) : "v"(p) : "memory");
      asm volatile("global_load_dword %0, %1, off" : "+v"(dummy2) : "v"(p2) : "memory");
    }
  }
  bf16x8 xf[32];
  unsigned tag;
  {
    const int r16 = lane & 15, qd = lane >> 4;
    const unsigned rb = reg + (r16 >> 1) * 1024 + (r16 & 1) * 512 + qd * 128 + ((r16 & 7) << 4);
    u32x4 xr[16];
    float q0 = 0.f, q1 = 0.f;
    f32x4 sum4 = {0.f, 0.f, 0.f, 0.f};
    const u32x4 ones = {0x3f803f80u, 0x3f803f80u, 0x3f803f80u, 0x3f803f80u};
    if (grp) WAITV(27); else WAITV(17);
#pragma unroll
    for (int q = 0; q < 8; ++q) xr[q] = *(const LAS u32x4*)(uintptr_t)(rb ^ (q << 4));
#pragma unroll
    for (int q = 0; q < 8; ++q)
#pragma unroll
      for (int e = 0; e < 4; e += 2) {
        if (e == 0) sum4 = MFMA16(ones, xr[q], sum4);
        q0 = dot2(xr[q][e], xr[q][e], q0); q1 = dot2(xr[q][e + 1], xr[q][e + 1], q1);
      }
    asm volatile("" : "+v"(sum4), "+v"(q0), "+v"(q1));
    if (grp) WAITV(19); else WAITV(9);
#pragma unroll
    for (int q = 0; q < 8; ++q) xr[8 + q] = *(const LAS u32x4*)(uintptr_t)((rb ^ (q << 4)) + 8192);
#pragma unroll
    for (int q = 8; q < 16; ++q)
#pragma unroll
      for (int e = 0; e < 4; e += 2) {
        if (e == 0) sum4 = MFMA16(ones, xr[q], sum4);
        q0 = dot2(xr[q][e], xr[q][e], q0); q1 = dot2(xr[q][e + 1], xr[q][e + 1], q1);
      }
    const float mean = sum4[0] * (1.0f / 512.0f);
    const float var = fmaxf(rsum32(rsum16(q0 + q1)) * (1.0f / 512.0f) - mean * mean, 0.f);
    const float nmb = -bf16r(mean);
    const float rs = 1.0f / bf16r(sqrtf(bf16r(bf16r(var) + 1e-5f)));
    const unsigned wb = reg + qd * 4096 + r16 * 16 + (r16 & 3) * 512 + ((r16 >> 2) & 1) * 256;
#pragma unroll
    for (int q = 0; q < 16; ++q) {
      u32x4 o;
      center4(xr[q], nmb);
#pragma unroll
      for (int e = 0; e < 4; ++e) o[e] = cvtpk(bflo(xr[q][e]) * rs, bfhi(xr[q][e]) * rs);
      *(LAS u32x4*)(uintptr_t)((wb ^ ((q & 3) * 512 + ((q >> 2) & 1) * 256)) + (q >> 3) * 2048) = o;
    }
    SB0();
    WAITV(0);
    asm volatile("" : "+v"(dummy), "+v"(dummy0), "+v"(dummy1), "+v"(dummy2), "+v"(wf), "+v"(wfb));
#pragma unroll
    for (int j = 0; j < 4; ++j) wf[j] |= wfb[j] << 16;
    tag = __builtin_amdgcn_readfirstlane(tagv) + 1u;
    asm volatile("" : "+s"(tag));
    BAR();
    const unsigned xb = lds0 + SLOT * (1 + wr) + (r32 >> 4) * 16384 + (h * 16 + (r32 & 15)) * 16;
#pragma unroll
    for (int ks = 0; ks < 32; ++ks) xf[ks] = *(const LAS bf16x8*)(uintptr_t)(xb + ks * 512);
    BAR();
  }
  STAMP(1);
  const unsigned voffP = (lane >> 3) * 2752 + (((lane & 7) ^ ((lane >> 2) & 4)) << 4);
  const size_t rowP = (size_t)wave * 64 * 2752;
  const int i16 = lane & 15, blk = (lane >> 4) & 1;
  const unsigned tr0 = lds0 + (32 * h + (i16 >> 2)) * 64 + 8 * (4 * blk + (i16 & 3));
  const unsigned trP = lds0 + (32 * h + (i16 >> 2)) * 128 + (((grp ^ (i16 >> 3)) & 1) << 6) + 8 * (4 * blk + (i16 & 3));
  const int mblk = mt * 4 + wr;
  const unsigned ldd = lds0 + wr * 8192;
  const size_t rowF = (size_t)wr * 128 * 2752, rowA = (size_t)wr * 128 * 3072;
  char* const hdst = ws + WS_AB + ((size_t)mblk * KS_W + 32 + 2 * g0) * 1024 + lane * 16;
  unsigned sg[8], pp[8];
  f32x16 acc, acc2;
  const int row = wr * 32 + r32, sw = (row >> 1) & 7;
  auto nofill = [](int) {};
  if (grp == 0) {
    mfma_tile<64>(acc, xf, tr0, true, wK - 1024 + rowA, 16 * 3072, voffA, ldd + 2 * SLOT, nofill);
    PHASE_END(0);
    feat_proj(acc, wf, r32, pp);
#pragma unroll
    for (int s = 0; s < 2; ++s) {
      if (s) {
#pragma unroll
        for (int i = 0; i < 4; ++i) glds16s(voffP, wG + rowP + i * 8 * 2752, lds0 + wave * 8192 + i * 1024);
      }
      bf16x8 fp, fm;
      feat_soft(pp + 4 * s, fp, fm);
      if (s) {
#pragma unroll
        for (int i = 4; i < 8; ++i) glds16s(voffP, wG + rowP + i * 8 * 2752, lds0 + wave * 8192 + i * 1024);
      }
      const unsigned pk = lds0 + LDS_SCR + s * 8192 + row * 64;
      const u32x4 up = __builtin_bit_cast(u32x4, fp), um = __builtin_bit_cast(u32x4, fm);
      u32x2 t;
      t[0] = up[0]; t[1] = up[1]; *(LAS u32x2*)(uintptr_t)(pk + 8 * ((h) ^ sw)) = t;
      t[0] = up[2]; t[1] = up[3]; *(LAS u32x2*)(uintptr_t)(pk + 8 * ((2 + h) ^ sw)) = t;
      t[0] = um[0]; t[1] = um[1]; *(LAS u32x2*)(uintptr_t)(pk + 8 * ((4 + h) ^ sw)) = t;
      t[0] = um[2]; t[1] = um[3]; *(LAS u32x2*)(uintptr_t)(pk + 8 * ((6 + h) ^ sw)) = t;
      if (s == 0) { SB0(); WAITV(0); }
      PHASE_END(1 + s);
    }
    if (full) mfma_tile<64>(acc2, xf, tr0 + 3 * SLOT, false, nullptr, 0, 0, 0, nofill);
    WAITV(0);
    PHASE_END(3);
    mfma_tile<128>(acc, xf, trP, true, wU + rowP, 8 * 2752, voffP, lds0 + 2 * SLOT + wave * 8192, nofill);
    PHASE_END(4);
    epi_g(acc, sg);
    WAITV(0);
    PHASE_END(5);
    mfma_tile<128>(acc, xf, trP + 2 * SLOT, full, wU + 128 + rowF, 16 * 2752, voffF, ldd, nofill);
    PHASE_END(6);
    epi_u(acc, sg, hdst);
    if (full) epi_g(acc2, sg);
    WAITV(2);
    PHASE_END(7);
    if (full) {
      mfma_tile<64>(acc, xf, tr0, false, nullptr, 0, 0, 0, nofill);
      STAMP(18);
      epi_u(acc, sg, hdst + 4096);
    }
    STAMP(20);
  } else {
#pragma unroll
    for (int i = 0; i < 8; ++i) glds16s(voffA, wK + 1024 + rowA + i * 16 * 3072, ldd + SLOT + i * 1024);
    unsigned d0 = 0, d1 = 0;
    {
      const int tr = idx * 59 + (lane < 59 ? lane : 58);
      const char* p = (tr < 512 ? (const char*)wo + (size_t)tr * 1024 : (const char*)w2 + (size_t)(tr - 512) * 1024) + wr * 256;
      asm volatile("global_load_dword %0, %2, off\n\tglobal_load_dword %1, %2, off offset:128" : "+v"(d0), "+v"(d1) : "v"(p) : "memory");
    }
    WAITV(2);
    PHASE_END(0);
    mfma_tile<64>(acc2, xf, tr0 + SLOT, full, wG + 128 + rowF, 16 * 2752, voffF, ldd + 3 * SLOT, nofill);
    PHASE_END(1);
    {
      const unsigned vi = lds0 + LDS_SCR + 16384 + row * 64;
      mfma_tile<64>(acc, xf, tr0 + 2 * SLOT, true, wG + rowP, 8 * 2752, voffP, lds0 + wave * 8192, [&](int ks) {
        if (ks >= 1 && ks <= 4) {
          const int a = ks - 1;
          u32x2 t; t[0] = cvtpk(acc2[4 * a], acc2[4 * a + 1]); t[1] = cvtpk(acc2[4 * a + 2], acc2[4 * a + 3]);
          *(LAS u32x2*)(uintptr_t)(vi + 8 * ((2 * a + h) ^ sw)) = t;
        }
      });
    }
    WAITV(8);
    asm volatile("" : "+v"(d0), "+v"(d1));
    PHASE_END(2);
    if (wr < 2) {
      f32x16 a2;
#pragma unroll
      for (int i = 0; i < 16; ++i) a2[i] = 0.f;
      const int rowq = 8 * h + (i16 >> 2);
      const int ca = 4 * blk + (lane & 3), cb = 4 * (blk ^ wr) + (lane & 3);
      const int sw0 = (rowq >> 1) & 7, sw1 = ((rowq + 4) >> 1) & 7;
      const unsigned pk = lds0 + LDS_SCR + wr * 8192 + rowq * 64, vi = lds0 + LDS_SCR + 16384 + rowq * 64;
      const unsigned pa0 = pk + 8 * (ca ^ sw0), pa1 = pk + 256 + 8 * (ca ^ sw1), pb0 = vi + 8 * (cb ^ sw0), pb1 = vi + 256 + 8 * (cb ^ sw1);
      __builtin_amdgcn_s_setprio(3);
#pragma unroll
      for (int ks = 0; ks < 8; ++ks) {
        bf16x8 af = trfrag(pa0 + ks * 1024, pa1 + ks * 1024);
        bf16x8 bfr = trfrag(pb0 + ks * 1024, pb1 + ks * 1024);
        if (blk) {
#pragma unroll
          for (int j = 0; j < 8; ++j) bfr[j] = (short)0x3f80;
        }
        a2 = MFMA(af, bfr, a2);
      }
      asm volatile("" : "+v"(a2));
      __builtin_amdgcn_s_setprio(PRIO_E);
      char* p1w = ws + WS_P1 + (((size_t)(ng * 16 + wr * 8) * 16 + mt) * 1024) + lane * 8;
#pragma unroll
      for (int i = 0; i < 16; ++i) st_gran(p1w + (size_t)(i >> 1) * 16384 + (i & 1) * 512, __float_as_uint(a2[i]), tag);
    }
    bf16x8 fp[2], fm[2];
    feat_proj(acc, wf, r32, pp);
    if (wr >= 2) feat_soft(pp, fp[0], fm[0]);
    if (wr < 2) WAITV(16); else WAITV(0);
    PHASE_END(3);
#pragma unroll
    for (int i = 0; i < 4; ++i) glds16s(voffP, wU + rowP + i * 8 * 2752, lds0 + 2 * SLOT + wave * 8192 + i * 1024);
    if (wr < 2) feat_soft(pp, fp[0], fm[0]);
#pragma unroll
    for (int i = 4; i < 8; ++i) glds16s(voffP, wU + rowP + i * 8 * 2752, lds0 + 2 * SLOT + wave * 8192 + i * 1024);
    if (wr >= 2) feat_soft(pp + 4, fp[1], fm[1]);
    PHASE_END(4);
    u64 g1[8];
    const char* p1r = ws + WS_P1 + ((size_t)(ng * 16 + mt) * 16 + h * 8) * 1024 + blk * 512 + (wr * 16 + i16) * 8;
    mfma_tile<128>(acc, xf, trP, false, nullptr, 0, 0, 0, [&](int ks) { if (ks >= 16 && ks < 24) g1[ks - 16] = ld_gran(p1r + (ks - 16) * 1024); });
    WAITV(8);
    PHASE_END(5);
    {
      for (unsigned spin = 0; spin < SPIN_MAX; ++spin) {
        bool ok = true;
#pragma unroll
        for (int j = 0; j < 8; ++j) ok &= (unsigned)(g1[j] >> 32) == tag;
        if (__all(ok)) break;
        __builtin_amdgcn_s_sleep(4);
#pragma unroll
        for (int j = 0; j < 8; ++j) g1[j] = ld_gran(p1r + j * 1024);
      }
      float a = 0.f;
#pragma unroll
      for (int j = 0; j < 8; ++j) a = nv(a + __uint_as_float((unsigned)g1[j]));
      a = rsum32(a);
      auto rr = __builtin_amdgcn_permlane16_swap(__float_as_uint(a), __float_as_uint(a), false, false);
      if (lane < 16) st_gran(ws + WS_P2 + (size_t)(ng * 16 + mt) * 512 + (wr * 16 + lane) * 8, cvtpk(__uint_as_float(rr[0]), __uint_as_float(rr[1])), tag);
      if (mt == 0 && wr == 0 && lane == 0) __hip_atomic_store((GAS unsigned*)(ws + WS_EP + ng * 128), tag, __ATOMIC_RELAXED, __HIP_MEMORY_SCOPE_AGENT);
      SB0();
      epi_g(acc, sg);
    }
    PHASE_END(6);
    mfma_tile<128>(acc, xf, trP + 2 * SLOT, false, nullptr, 0, 0, 0, nofill);
    PHASE_END(7);
    const char* p2r = ws + WS_P2 + (size_t)(ng * 16) * 512 + lane * 8;
    u64 g2[16];
#pragma unroll
    for (int j = 0; j < 16; ++j) g2[j] = ld_gran(p2r + j * 512);
    SB0();
    epi_u(acc, sg, hdst + 2048);
    if (wr < 2) feat_soft(pp + 4, fp[1], fm[1]);
    STAMP(18);
    for (unsigned spin = 0; spin < SPIN_MAX; ++spin) {
      bool ok = true;
#pragma unroll
      for (int j = 0; j < 16; ++j) ok &= (unsigned)(g2[j] >> 32) == tag;
      if (__all(ok)) break;
      __builtin_amdgcn_s_sleep(4);
#pragma unroll
      for (int j = 0; j < 16; ++j) g2[j] = ld_gran(p2r + j * 512);
    }
    STAMP(19);
    __builtin_amdgcn_s_setprio(3);
#pragma unroll
    for (int s = 0; s < 2; ++s) {
      const u32x4 f0 = {(unsigned)g2[8 * s], (unsigned)g2[8 * s + 1], (unsigned)g2[8 * s + 2], (unsigned)g2[8 * s + 3]};
      const u32x4 f1 = {(unsigned)g2[8 * s + 4], (unsigned)g2[8 * s + 5], (unsigned)g2[8 * s + 6], (unsigned)g2[8 * s + 7]};
      f32x16 o;
#pragma unroll
      for (int q = 0; q < 16; ++q) o[q] = 0.f;
      o = MFMA(__builtin_bit_cast(bf16x8, f0), fp[s], o);
      o = MFMA(__builtin_bit_cast(bf16x8, f1), fm[s], o);
      const float rd = __builtin_amdgcn_rcpf(bf16r(bf16r(o[8]) + 1e-6f));
      float ov[8];
#pragma unroll
      for (int j = 0; j < 8; j += 2) { const unsigned u = cvtpk(o[j], o[j + 1]); ov[j] = nv(bflo(u) * rd); ov[j + 1] = nv(bfhi(u) * rd); }
      st16(ws + WS_AB + ((size_t)mblk * KS_W + 2 * ng + s) * 1024 + lane * 16, __builtin_bit_cast(u32x4, pack8(ov)));
    }
    STAMP(20);
  }
  STAMPR(31);
  STAMP_FLUSH(0);
  asm volatile(".Lk1_end:");
}

constexpr int XROW = 68;
constexpr int P2 = 10;
template <int N> DI void waitv() { asm volatile("s_waitcnt vmcnt(%0)" :: "n"(N) : "memory"); }
__global__ __launch_bounds__(256, 1) void k2_kernel(const u16* __restrict__ x, const u16* __restrict__ wo, const u16* __restrict__ w2, const char* __restrict__ ws, u16* __restrict__ out) {
  __shared__ __attribute__((aligned(16))) char smem[4 * P2 * 4096];
  float* xch = (float*)smem;
  const int tid = threadIdx.x, lane = tid & 63;
  const int wave = __builtin_amdgcn_readfirstlane(tid >> 6);
  const int b = blockIdx.x, xcd = b & 7, idx = b >> 3;
  const int mt = (xcd & 1) * 16 + (xcd >> 1) * 4 + (idx & 3), nt = idx >> 2;
  const int r32 = lane & 31, h = lane >> 5;
  const int erow = mt * 64 + (tid >> 2), ecol = nt * 64 + (tid & 3) * 16;
  const u32x4 xa = *(const u32x4*)(x + (size_t)erow * D_ + ecol), xb = *(const u32x4*)(x + (size_t)erow * D_ + ecol + 8);
  f32x16 acc[2][2];
#pragma unroll
  for (int n = 0; n < 2; ++n)
#pragma unroll
    for (int m = 0; m < 2; ++m)
#pragma unroll
      for (int i = 0; i < 16; ++i) acc[n][m][i] = 0.f;
  const int k0 = wave == 0 ? 0 : 32 + (wave - 1) * 29;
  const int nk = wave == 0 ? 32 : (wave == 3 ? 28 : 29);
  const char* wsrc = (wave == 0 ? (const char*)wo : (const char*)w2 + (size_t)(k0 - 32) * 16384) + nt * 128;
  const char* ha = ws + WS_AB + ((size_t)(2 * mt) * KS_W + k0) * 1024;
  const char* hb = ha + (size_t)KS_W * 1024;
  constexpr int RS = P2 * 4096;
  const unsigned ring = (unsigned)(uintptr_t)smem + wave * RS;
  const unsigned lo = lane * 16, voffW = (lane >> 3) * 1024 + (((lane & 7) ^ ((lane >> 2) & 4)) << 4);
  const int i16 = lane & 15, blk = (lane >> 4) & 1;
  const unsigned tb0 = ring + 2048 + (4 * h + (i16 >> 2)) * 128 + (((i16 >> 3) & 1) << 6) + 8 * (4 * blk + (i16 & 3)), tb1 = tb0 ^ 64;
  auto issue = [&](int i) {
    const unsigned d = ring + (i % P2) * 4096;
    glds16s(lo, ha + i * 1024, d); glds16s(lo, hb + i * 1024, d + 1024);
    glds16s(voffW, wsrc + i * 16384, d + 2048); glds16s(voffW, wsrc + i * 16384 + 8192, d + 3072);
  };
  bf16x8 R[2][4];
  auto rd = [&](int i, bf16x8* d) {
    const int o = (i % P2) * 4096;
    d[0] = *(const LAS bf16x8*)(uintptr_t)(ring + lo + o); d[1] = *(const LAS bf16x8*)(uintptr_t)(ring + lo + o + 1024);
    d[2] = trfrag(tb0 + o, tb0 + o + 1024); d[3] = trfrag(tb1 + o, tb1 + o + 1024);
  };
#pragma unroll
  for (int i = 0; i < P2; ++i) issue(i);
  waitv<4 * (P2 - 1)>();
  rd(0, R[0]);
#pragma unroll
  for (int i = 0; i < 32; ++i) {
    if (i < 28 || i < nk) {
      if (i + 1 < 28 || i + 1 < nk) {
        if (i + 1 < 32) {
          constexpr int full_w = 4 * (P2 - 2);
          if (4 * (26 - i) >= full_w) waitv<full_w>();
          else if (i == 19) waitv<28>(); else if (i == 20) waitv<24>(); else if (i == 21) waitv<20>(); else if (i == 22) waitv<16>();
          else if (i == 23) waitv<12>(); else if (i == 24) waitv<8>(); else if (i == 25) waitv<4>(); else waitv<0>();
          rd(i + 1, R[(i + 1) & 1]);
        }
      }
      SB0();
      const bf16x8* d = R[i & 1];
      acc[0][0] = MFMA(d[2], d[0], acc[0][0]); acc[0][1] = MFMA(d[2], d[1], acc[0][1]);
      acc[1][0] = MFMA(d[3], d[0], acc[1][0]); acc[1][1] = MFMA(d[3], d[1], acc[1][1]);
      SB0();
      if (i + P2 < 28) issue(i + P2);
      else if (i + P2 < 32) { if (i + P2 < nk) issue(i + P2); }
      SB0();
    }
  }
  __syncthreads();
  STAMP(1);
  float* mine = xch + wave * 64 * XROW;
#pragma unroll
  for (int n = 0; n < 2; ++n)
#pragma unroll
    for (int m = 0; m < 2; ++m)
#pragma unroll
      for (int a = 0; a < 4; ++a) {
        f32x4 t = {acc[n][m][4 * a], acc[n][m][4 * a + 1], acc[n][m][4 * a + 2], acc[n][m][4 * a + 3]};
        *(f32x4*)(mine + (32 * m + r32) * XROW + 32 * n + 8 * a + 4 * h) = t;
      }
  __syncthreads();
  STAMP(2);
  const float* rp = xch + (tid >> 2) * XROW + (tid & 3) * 16;
  u32x4 res[2];
#pragma unroll
  for (int q = 0; q < 4; ++q) {
    const f32x4 s0 = *(const f32x4*)(rp + 4 * q), o0 = *(const f32x4*)(rp + 64 * XROW + 4 * q);
    const f32x4 o1 = *(const f32x4*)(rp + 128 * XROW + 4 * q), o2 = *(const f32x4*)(rp + 192 * XROW + 4 * q);
#pragma unroll
    for (int e = 0; e < 4; e += 2) {
      const unsigned su = cvtpk(s0[e], s0[e + 1]);
      const unsigned ou = cvtpk((o0[e] + o1[e]) + o2[e], (o0[e + 1] + o1[e + 1]) + o2[e + 1]);
      const unsigned gu = cvtpk(silu_f(bflo(ou)), silu_f(bfhi(ou)));
      const unsigned tu = cvtpk(bflo(su) * bflo(gu), bfhi(su) * bfhi(gu));
      const unsigned xw = (q < 2 ? xa : xb)[(q & 1) * 2 + (e >> 1)];
      res[q >> 1][(q & 1) * 2 + (e >> 1)] = cvtpk(bflo(xw) + bflo(tu), bfhi(xw) + bfhi(tu));
    }
  }
  *(u32x4*)(out + (size_t)erow * D_ + ecol) = res[0];
  *(u32x4*)(out + (size_t)erow * D_ + ecol + 8) = res[1];
  STAMP(3); STAMPR(31);
  asm volatile(".Lk2_end:");
}

extern "C" void kernel_launch(void* const* d_in, const int* in_sizes, int n_in, void* d_out, int out_size, void* d_ws, size_t ws_size,
                              hipStream_t stream) {
  const u16* x = (const u16*)d_in[0];
  const u16* wqkv = (const u16*)d_in[1];
  const u16* wphi = (const u16*)d_in[2];
  const u16* wo = (const u16*)d_in[3];
  const u16* w1 = (const u16*)d_in[5];
  const u16* w3 = (const u16*)d_in[6];
  const u16* w2 = (const u16*)d_in[7];
  char* ws = (char*)d_ws;
  k1_kernel<<<256, 512, 0, stream>>>(x, wqkv, wphi, wo, w1, w3, w2, ws);
  k2_kernel<<<256, 256, 0, stream>>>(x, wo, w2, ws, (u16*)d_out);
}
```

```cpp
#include <hip/hip_runtime.h>
#include <hip/hip_bf16.h>
#include <stdint.h>

typedef __attribute__((ext_vector_type(8))) short bf16x8;
typedef __attribute__((ext_vector_type(4))) short s16x4;
typedef __attribute__((ext_vector_type(16))) float f32x16;
typedef __attribute__((ext_vector_type(4))) float f32x4;
typedef __attribute__((ext_vector_type(4))) unsigned u32x4;
typedef __attribute__((ext_vector_type(2))) unsigned u32x2;
typedef unsigned short u16;
#define LAS __attribute__((address_space(3)))
#define DI __device__ __forceinline__
#define MFMA(a, b, c) __builtin_amdgcn_mfma_f32_32x32x16_bf16((a), (b), (c), 0, 0, 0)
#define MFMA16(a, b, c) __builtin_amdgcn_mfma_f32_16x16x32_bf16(__builtin_bit_cast(bf16x8, (a)), __builtin_bit_cast(bf16x8, (b)), (c), 0, 0, 0)

constexpr int S_ = 2048, D_ = 512, NH = 32, F_ = 1376;
constexpr int KS_F = F_ / 16;
constexpr int KS_W = 32 + KS_F;
constexpr size_t WS_AB = 0;
constexpr size_t WS_WT = WS_AB + (size_t)64 * KS_W * 1024;
constexpr size_t WS_P1 = WS_WT + (size_t)16 * KS_W * 1024;
constexpr size_t WS_P2 = WS_P1 + (size_t)16 * 16 * 16 * 1024;
constexpr size_t WS_EP = WS_P2 + (size_t)16 * 16 * 512;
constexpr size_t WS_DUMP = WS_EP + 16 * 128;
constexpr size_t WS_END = WS_DUMP + 2048;

typedef __attribute__((ext_vector_type(2))) __bf16 bf16x2_t;
typedef __attribute__((ext_vector_type(2))) float f32x2;
DI unsigned cvtpk(float lo, float hi) { f32x2 v = {lo, hi}; return __builtin_bit_cast(unsigned, __builtin_convertvector(v, bf16x2_t)); }
DI float bflo(unsigned u) { return __uint_as_float(u << 16); }
DI float bfhi(unsigned u) { return __uint_as_float(u & 0xffff0000u); }
DI float bf16r(float x) { return bflo(cvtpk(x, x)); }
DI void glds16(const void* g, unsigned lds_base) {
  unsigned sv;
  asm volatile("s_mov_b32 %0, m0\n\ts_mov_b32 m0, %2\n\ts_nop 0\n\tglobal_load_lds_dwordx4 %1, off\n\ts_mov_b32 m0, %0"
               : "=&s"(sv) : "v"(g), "s"(lds_base) : "memory");
}
typedef short v4i16_t __attribute__((ext_vector_type(4)));
DI s16x4 trrd(unsigned a) { return __builtin_bit_cast(s16x4, __builtin_amdgcn_ds_read_tr16_b64_v4i16((LAS v4i16_t*)(uintptr_t)a)); }
DI bf16x8 trfrag(unsigned lo, unsigned hi) { s16x4 a = trrd(lo), b = trrd(hi); return __builtin_shufflevector(a, b, 0, 1, 2, 3, 4, 5, 6, 7); }
DI bf16x8 pack8(const float* v) {
  u32x4 p; p[0] = cvtpk(v[0], v[1]); p[1] = cvtpk(v[2], v[3]); p[2] = cvtpk(v[4], v[5]); p[3] = cvtpk(v[6], v[7]);
  return __builtin_bit_cast(bf16x8, p);
}
DI float xhalf(float v) {
  auto rr = __builtin_amdgcn_permlane32_swap(__float_as_uint(v), __float_as_uint(v), false, false);
  unsigned a = rr[0], b = rr[1];
  return __uint_as_float((threadIdx.x & 32) ? a : b);
}
template <int C> DI float dppror(float v) { return __uint_as_float(__builtin_amdgcn_update_dpp(0u, __float_as_uint(v), 0x120 + C, 0xf, 0xf, false)); }
DI float row16sum(float v) { v += dppror<8>(v); v += dppror<4>(v); v += dppror<2>(v); v += dppror<1>(v); return v; }
DI float silu_f(float x) { return x * __builtin_amdgcn_rcpf(1.0f + __builtin_amdgcn_exp2f(-1.4426950408889634f * x)); }
#define STAMP(i)
#define STAMPR(i)
#define STAMP_FLUSH(base)
#define BAR() asm volatile("s_waitcnt lgkmcnt(0)\n\ts_barrier" ::: "memory")
#define WAITV0() asm volatile("s_waitcnt vmcnt(0)" ::: "memory")

enum { T_NONE = 0, T_Q = 1, T_K = 2, T_V = 3, T_G = 4, T_U = 5 };
#define TD(ty, ix) ((unsigned long long)(((ty) << 6) | (ix)))
#define SB0() __builtin_amdgcn_sched_barrier(0)
#define GAS __attribute__((address_space(1)))
typedef unsigned long long u64;
DI float dot2(unsigned a, unsigned b, float c) {
  return __builtin_amdgcn_fdot2_f32_bf16(__builtin_bit_cast(bf16x2_t, a), __builtin_bit_cast(bf16x2_t, b), c, false);
}
DI void st_gran(void* p, unsigned val, unsigned tag) { __hip_atomic_store((GAS u64*)p, ((u64)tag << 32) | val, __ATOMIC_RELAXED, __HIP_MEMORY_SCOPE_AGENT); }
DI u64 ld_gran(const void* p) { return __hip_atomic_load((GAS u64*)p, __ATOMIC_RELAXED, __HIP_MEMORY_SCOPE_AGENT); }
constexpr unsigned SPIN_MAX = 1u << 16;

constexpr int SLOT = 32768;
constexpr int LDS_SCR = 4 * SLOT;
constexpr int LDS_CS = LDS_SCR + 24576;
constexpr int LDS_ST = LDS_SCR + 29696;
constexpr int LDS_DBG = LDS_SCR + 30720;
constexpr int K1_LDS = 5 * SLOT;

DI void glds16s(unsigned voff, const void* sbase, unsigned lds_base) {
  unsigned sv;
  asm volatile("s_mov_b32 %0, m0\n\ts_mov_b32 m0, %3\n\ts_nop 0\n\tglobal_load_lds_dwordx4 %1, %2\n\ts_mov_b32 m0, %0"
               : "=&s"(sv) : "v"(voff), "s"(sbase), "s"(lds_base) : "memory");
}
DI void center4(u32x4& a, float c) {
  unsigned t0, t1, t2, t3, t4, t5, t6, t7;
  asm("v_dot2_f32_bf16 %0, %8, %12, %14\n\tv_dot2_f32_bf16 %1, %8, %13, %14\n\tv_dot2_f32_bf16 %2, %9, %12, %14\n\tv_dot2_f32_bf16 %3, %9, %13, %14\n\t"
      "v_dot2_f32_bf16 %4, %10, %12, %14\n\tv_dot2_f32_bf16 %5, %10, %13, %14\n\tv_dot2_f32_bf16 %6, %11, %12, %14\n\tv_dot2_f32_bf16 %7, %11, %13, %14\n\t"
      "v_cvt_pk_bf16_f32 %0, %0, %1\n\tv_cvt_pk_bf16_f32 %2, %2, %3\n\tv_cvt_pk_bf16_f32 %4, %4, %5\n\tv_cvt_pk_bf16_f32 %6, %6, %7"
      : "=&v"(t0), "=&v"(t1), "=&v"(t2), "=&v"(t3), "=&v"(t4), "=&v"(t5), "=&v"(t6), "=&v"(t7)
      : "v"(a[0]), "v"(a[1]), "v"(a[2]), "v"(a[3]), "s"(0x00003f80u), "s"(0x3f800000u), "v"(c));
  a[0] = t0; a[1] = t2; a[2] = t4; a[3] = t6;
}
DI float rsum32(float v) { auto rr = __builtin_amdgcn_permlane32_swap(__float_as_uint(v), __float_as_uint(v), false, false); return __uint_as_float(rr[0]) + __uint_as_float(rr[1]); }
DI float rsum16(float v) { auto rr = __builtin_amdgcn_permlane16_swap(__float_as_uint(v), __float_as_uint(v), false, false); return __uint_as_float(rr[0]) + __uint_as_float(rr[1]); }
DI float rmax32(float v) { auto rr = __builtin_amdgcn_permlane32_swap(__float_as_uint(v), __float_as_uint(v), false, false); return fmaxf(__uint_as_float(rr[0]), __uint_as_float(rr[1])); }
DI float rmin32(float v) { auto rr = __builtin_amdgcn_permlane32_swap(__float_as_uint(v), __float_as_uint(v), false, false); return fminf(__uint_as_float(rr[0]), __uint_as_float(rr[1])); }
DI float nv(float x) { asm("" : "+v"(x)); return x; }
#define WAITV(n) asm volatile("s_waitcnt vmcnt(" #n ")" ::: "memory")

#define PRIO_M 2
#define PFD 4
#define LDSP 4
#define PRIO_E 0
template <int RB, class F>
DI void mfma_tile(f32x16& acc, const bf16x8 (&xf)[32], unsigned sb, bool ld, const char* g, unsigned gstep, unsigned voff, unsigned dst, int wr, unsigned csw, F filler) {
  __builtin_amdgcn_s_setprio(PRIO_M);
  bf16x8 wq[PFD];
  float cs = 0.f;
#pragma unroll
  for (int ks = 0; ks < PFD; ++ks) { const int o = ((ks >> 2) * 64 + (ks & 3) * 8) * RB; wq[ks] = trfrag(sb + o, sb + o + 4 * RB); }
#pragma unroll
  for (int i = 0; i < 16; ++i) acc[i] = 0.f;
  SB0();
#pragma unroll
  for (int ks = 0; ks < 32; ++ks) {
    acc = MFMA(wq[ks % PFD], xf[ks], acc);
    if ((ks & 3) == wr) {
      const u32x4 w4 = __builtin_bit_cast(u32x4, wq[ks % PFD]);
      cs = dot2(w4[0], 0x3f803f80u, cs); cs = dot2(w4[1], 0x3f803f80u, cs); cs = dot2(w4[2], 0x3f803f80u, cs); cs = dot2(w4[3], 0x3f803f80u, cs);
    }
    if (ks + PFD < 32) { const int o = (((ks + PFD) >> 2) * 64 + ((ks + PFD) & 3) * 8) * RB; wq[ks % PFD] = trfrag(sb + o, sb + o + 4 * RB); }
    if ((ks % LDSP) == 0 && ks < 8 * LDSP) { if (ld) glds16s(voff, g + (size_t)(ks / LDSP) * gstep, dst + (ks / LDSP) * 1024); }
    filler(ks);
    SB0();
  }
  *(LAS unsigned*)(uintptr_t)csw = cvtpk(cs, cs - bf16r(cs));
  __builtin_amdgcn_s_setprio(PRIO_E);
  asm volatile("" : "+v"(acc));
}
DI void ln_mean(f32x16& acc, unsigned csr, unsigned m2) {
  const bf16x8 a = *(const LAS bf16x8*)(uintptr_t)csr;
  const u32x4 mb = {m2, m2, m2, m2};
  acc = MFMA(a, __builtin_bit_cast(bf16x8, mb), acc);
}
DI void ln_fix(f32x16& acc, unsigned csr, unsigned m2, float rs) {
  __builtin_amdgcn_s_setprio(3);
  ln_mean(acc, csr, m2);
  SB0();
  __builtin_amdgcn_s_setprio(PRIO_E);
#pragma unroll
  for (int i = 0; i < 16; ++i) acc[i] = nv(acc[i] * rs);
}
DI void feat_proj(const f32x16& acc, const u32x4& wf, int r32, unsigned (&pp)[8]) {
  float av[16];
#pragma unroll
  for (int i = 0; i < 16; ++i) av[i] = acc[i];
  const bf16x8 qb0 = pack8(av), qb1 = pack8(av + 8);
  const u32x4 z4 = {0u, 0u, 0u, 0u};
  const bf16x8 a0 = __builtin_bit_cast(bf16x8, (r32 < 16) ? wf : z4), a1 = __builtin_bit_cast(bf16x8, (r32 < 16) ? z4 : wf);
  f32x16 pq;
#pragma unroll
  for (int i = 0; i < 16; ++i) pq[i] = 0.f;
  SB0();
  __builtin_amdgcn_s_setprio(3);
  pq = MFMA(a0, qb0, pq);
  pq = MFMA(a1, qb1, pq);
  SB0();
  __builtin_amdgcn_s_setprio(PRIO_E);
#pragma unroll
  for (int j = 0; j < 8; ++j) pp[j] = cvtpk(pq[2 * j], pq[2 * j + 1]);
  asm volatile("" : "+v"(pp[0]), "+v"(pp[1]), "+v"(pp[2]), "+v"(pp[3]), "+v"(pp[4]), "+v"(pp[5]), "+v"(pp[6]), "+v"(pp[7]));
}
DI void feat_soft(const unsigned* pp, bf16x8& fp, bf16x8& fm) {
  float pv[8];
#pragma unroll
  for (int j = 0; j < 4; ++j) { pv[2 * j] = bflo(pp[j]); pv[2 * j + 1] = bfhi(pp[j]); }
  float mx = fmaxf(fmaxf(fmaxf(pv[0], pv[1]), pv[2]), fmaxf(fmaxf(pv[3], pv[4]), pv[5])), mn = fminf(fminf(fminf(pv[0], pv[1]), pv[2]), fminf(fminf(pv[3], pv[4]), pv[5]));
  mx = fmaxf(fmaxf(mx, pv[6]), pv[7]); mn = fminf(fminf(mn, pv[6]), pv[7]);
  mx = rmax32(mx); mn = rmin32(mn);
  const float L2E = 1.4426950408889634f, mxl = nv(-mx * L2E), mnl = nv(mn * L2E);
  float ep[8], em[8], sp = 0.f, sm = 0.f;
#pragma unroll
  for (int j = 0; j < 8; ++j) {
    ep[j] = __builtin_amdgcn_exp2f(nv(fmaf(pv[j], L2E, mxl))); sp = nv(sp + ep[j]);
    em[j] = __builtin_amdgcn_exp2f(nv(fmaf(pv[j], -L2E, mnl))); sm = nv(sm + em[j]);
  }
  sp = nv(rsum32(sp)); sm = nv(rsum32(sm));
  const float rp = __builtin_amdgcn_rcpf(sp), rm = __builtin_amdgcn_rcpf(sm);
#pragma unroll
  for (int j = 0; j < 8; ++j) { ep[j] = nv(ep[j] * rp); em[j] = nv(em[j] * rm); }
  fp = pack8(ep); fm = pack8(em);
  asm volatile("" : "+v"(fp), "+v"(fm));
}
#define WT_STORE 1
DI void st16(void* p, u32x4 v) {
  asm volatile("global_store_dwordx4 %0, %1, off sc1\n\ts_nop 1" :: "v"(p), "v"(v) : "memory");
}
DI void epi_g(const f32x16& acc, unsigned (&sg)[8]) {
#pragma unroll
  for (int i = 0; i < 16; i += 2) {
    const unsigned u = cvtpk(acc[i], acc[i + 1]);
    sg[i >> 1] = cvtpk(nv(silu_f(bflo(u))), nv(silu_f(bfhi(u))));
  }
  asm volatile("" : "+v"(sg[0]), "+v"(sg[1]), "+v"(sg[2]), "+v"(sg[3]), "+v"(sg[4]), "+v"(sg[5]), "+v"(sg[6]), "+v"(sg[7]));
}
DI void epi_u(const f32x16& acc, const unsigned (&sg)[8], char* dst) {
  float hv[16];
#pragma unroll
  for (int i = 0; i < 16; i += 2) { const unsigned u = cvtpk(acc[i], acc[i + 1]); hv[i] = nv(bflo(sg[i >> 1]) * bflo(u)); hv[i + 1] = nv(bfhi(sg[i >> 1]) * bfhi(u)); }
  st16(dst, __builtin_bit_cast(u32x4, pack8(hv))); st16(dst + 1024, __builtin_bit_cast(u32x4, pack8(hv + 8)));
}
#define WF_MERGE(n) asm volatile("s_waitcnt vmcnt(" #n ")\n\tv_lshl_or_b32 %0, %8, 16, %4\n\tv_lshl_or_b32 %1, %9, 16, %5\n\tv_lshl_or_b32 %2, %10, 16, %6\n\tv_lshl_or_b32 %3, %11, 16, %7" \
  : "=&v"(wf[0]), "=&v"(wf[1]), "=&v"(wf[2]), "=&v"(wf[3]) : "v"(wfa[0]), "v"(wfa[1]), "v"(wfa[2]), "v"(wfa[3]), "v"(wfb[0]), "v"(wfb[1]), "v"(wfb[2]), "v"(wfb[3]) : "memory")
#define PHASE_END(p) do { STAMP(2 + 2 * (p)); SB0(); BAR(); SB0(); STAMP(3 + 2 * (p)); } while (0)

__global__ __launch_bounds__(512, 2) void k1_kernel(const u16* __restrict__ x, const u16* __restrict__ wqkv, const u16* __restrict__ wphi,
                                                    const u16* __restrict__ wo, const u16* __restrict__ w1, const u16* __restrict__ w3,
                                                    const u16* __restrict__ w2, char* __restrict__ ws) {
  __shared__ __attribute__((aligned(16))) char smem[K1_LDS];
  const int tid = threadIdx.x, lane = tid & 63;
  const int wave = __builtin_amdgcn_readfirstlane(tid >> 6), grp = wave >> 2, wr = wave & 3;
  const int b = blockIdx.x, xcd = b & 7, idx = b >> 3;
  const int mt = (xcd & 1) * 8 + (idx & 7), ng = (xcd >> 1) * 4 + (idx >> 3);
  const int r32 = lane & 31, h = lane >> 5;
  const unsigned lds0 = (unsigned)(uintptr_t)smem;
  unsigned dummy0 = 0;
  if (wave == 0) {
    unsigned long long pc; unsigned len;
    asm volatile("s_getpc_b64 %0\n.Lk1_pc:\n\ts_mov_b32 %1, .Lk2_end-.Lk1_pc" : "=s"(pc), "=s"(len));
    const unsigned off = (lane * 8 + (idx & 7)) * 128;
    if (off < len) asm volatile("global_load_dword %0, %1, %2" : "+v"(dummy0) : "v"(off), "s"(pc) : "memory");
  }
  {
    const char* xb = (const char*)(x + (size_t)(mt * 128 + wr * 32 + grp * 16) * D_);
    const unsigned reg = lds0 + SLOT * (1 + wr) + grp * 16384;
#pragma unroll
    for (int i = 0; i < 16; ++i) glds16s((lane ^ (i & 7)) << 4, xb + i * 1024, reg + i * 1024);
  }
  const int g0 = ng < 11 ? 3 * ng : 33 + 2 * (ng - 11);
  const bool full = ng < 11;
  const char* wK = (const char*)(wqkv + 512 + 32 * ng);
  const char* wG = (const char*)(w1 + 32 * g0);
  const char* wU = (const char*)(w3 + 32 * g0);
  const unsigned voffA = (lane >> 2) * 3072 + (lane & 3) * 16, voffF = (lane >> 2) * 2752 + (lane & 3) * 16;
  unsigned dummy = 0, dummy2 = 0, tagv = 0;
  unsigned wfa[4], wfb[4];
  u32x4 wf;
  {
    const char* g = wK + (size_t)wave * 64 * 3072;
#pragma unroll
    for (int i = 0; i < 4; ++i) glds16s(voffA, g + i * 16 * 3072, lds0 + wave * 4096 + i * 1024);
    const u16* wp = wphi + (size_t)(2 * ng + (r32 >> 4)) * 256 + (4 * h) * 16 + (lane & 15);
#pragma unroll
    for (int j = 0; j < 4; ++j) {
      const int k0 = 8 * (j >> 1) + 2 * (j & 1);
      asm volatile("global_load_ushort %0, %2, off offset:%3\n\tglobal_load_ushort %1, %2, off offset:%4" : "=&v"(wfa[j]), "=&v"(wfb[j]) : "v"(wp), "n"(k0 * 32), "n"(k0 * 32 + 32) : "memory");
    }
    if (grp) {
      const int trow = (idx & 7) * 64 + lane;
      const int e = (wr == 3 && !full) ? 0 : wr - 1;
      const char* p = wr == 0 ? wK + 1024 + (size_t)trow * 3072 : wG + 64 * e + (size_t)trow * 2752;
      const char* p2 = wr == 0 ? wK - 1024 + (size_t)trow * 3072 : wU + 64 * e + (size_t)trow * 2752;
      asm volatile("global_load_dword %0, %1, off" : "+v"(dummy) : "v"(p) : "memory");
      asm volatile("global_load_dword %0, %1, off" : "+v"(dummy2) : "v"(p2) : "memory");
      asm volatile("global_load_dword %0, %1, off sc1" : "+v"(tagv) : "v"(ws + WS_EP + ng * 128) : "memory");
    }
  }
  bf16x8 xf[32];
  {
    if (grp) WAITV(11); else WAITV(8);
    asm volatile("" :: "v"(dummy0));
    BAR();
    const unsigned xb = lds0 + SLOT * (1 + wr) + r32 * 1024;
#pragma unroll
    for (int ks = 0; ks < 32; ++ks) xf[ks] = *(const LAS bf16x8*)(uintptr_t)(xb + (((4 * h + (ks & 3)) ^ (r32 & 7)) << 4) + (ks >> 2) * 128);
    BAR();
  }
  STAMP(1);
  const unsigned voffP = (lane >> 3) * 2752 + (((lane & 7) ^ ((lane >> 2) & 4)) << 4);
  const size_t rowP = (size_t)wave * 64 * 2752;
  const int i16 = lane & 15, blk = (lane >> 4) & 1;
  const unsigned tr0 = lds0 + (32 * h + (i16 >> 2)) * 64 + 8 * (4 * blk + (i16 & 3));
  const unsigned trP = lds0 + (32 * h + (i16 >> 2)) * 128 + (((grp ^ (i16 >> 3)) & 1) << 6) + 8 * (4 * blk + (i16 & 3));
  const int mblk = mt * 4 + wr;
  const unsigned ldd = lds0 + wr * 8192;
  const size_t rowF = (size_t)wr * 128 * 2752, rowA = (size_t)wr * 128 * 3072;
  char* const hdst = ws + WS_AB + ((size_t)mblk * KS_W + 32 + 2 * g0) * 1024 + lane * 16;
  unsigned sg[8], pp[8];
  f32x16 acc, acc2;
  const int row = wr * 32 + r32, sw = (row >> 1) & 7;
  auto nofill = [](int) {};
  const unsigned csr = lds0 + LDS_CS + lane * 16, csw = csr + wr * 4;
  unsigned m2;
  float rs;
  if (grp == 0) {
    mfma_tile<64>(acc, xf, tr0, true, wK - 1024 + rowA, 16 * 3072, voffA, ldd + 2 * SLOT, wr, csw, nofill);
    PHASE_END(0);
    { const u32x2 t = *(const LAS u32x2*)(uintptr_t)(lds0 + LDS_ST + row * 8); m2 = t[0]; rs = __uint_as_float(t[1]); }
    WF_MERGE(8);
    ln_fix(acc, csr, m2, rs);
    feat_proj(acc, wf, r32, pp);
#pragma unroll
    for (int s = 0; s < 2; ++s) {
      if (s) {
#pragma unroll
        for (int i = 0; i < 4; ++i) glds16s(voffP, wG + rowP + i * 8 * 2752, lds0 + wave * 8192 + i * 1024);
      }
      bf16x8 fp, fm;
      feat_soft(pp + 4 * s, fp, fm);
      if (s) {
#pragma unroll
        for (int i = 4; i < 8; ++i) glds16s(voffP, wG + rowP + i * 8 * 2752, lds0 + wave * 8192 + i * 1024);
      }
      const unsigned pk = lds0 + LDS_SCR + s * 8192 + row * 64;
      const u32x4 up = __builtin_bit_cast(u32x4, fp), um = __builtin_bit_cast(u32x4, fm);
      u32x2 t;
      t[0] = up[0]; t[1] = up[1]; *(LAS u32x2*)(uintptr_t)(pk + 8 * ((h) ^ sw)) = t;
      t[0] = up[2]; t[1] = up[3]; *(LAS u32x2*)(uintptr_t)(pk + 8 * ((2 + h) ^ sw)) = t;
      t[0] = um[0]; t[1] = um[1]; *(LAS u32x2*)(uintptr_t)(pk + 8 * ((4 + h) ^ sw)) = t;
      t[0] = um[2]; t[1] = um[3]; *(LAS u32x2*)(uintptr_t)(pk + 8 * ((6 + h) ^ sw)) = t;
      if (s == 0) { SB0(); WAITV(0); }
      PHASE_END(1 + s);
    }
    unsigned d0 = 0, d1 = 0;
    const int tr = idx * 59 + (lane < 59 ? lane : 58);
    const char* tp = (tr < 512 ? (const char*)wo + (size_t)tr * 1024 : (const char*)w2 + (size_t)(tr - 512) * 1024) + wr * 256;
    asm volatile("global_load_dword %0, %1, off" : "+v"(d0) : "v"(tp) : "memory");
    if (full) mfma_tile<64>(acc2, xf, tr0 + 3 * SLOT, false, nullptr, 0, 0, 0, wr, csw + 3072, nofill);
    WAITV(1);
    PHASE_END(3);
    mfma_tile<128>(acc, xf, trP, true, wU + rowP, 8 * 2752, voffP, lds0 + 2 * SLOT + wave * 8192, wr, csw, nofill);
    PHASE_END(4);
    asm volatile("global_load_dword %0, %1, off offset:128" : "+v"(d1) : "v"(tp) : "memory");
    ln_fix(acc, csr, m2, rs);
    epi_g(acc, sg);
    WAITV(1);
    asm volatile("" :: "v"(d0));
    PHASE_END(5);
    mfma_tile<128>(acc, xf, trP + 2 * SLOT, full, wU + 128 + rowF, 16 * 2752, voffF, ldd, wr, csw + 2048, nofill);
    PHASE_END(6);
    ln_fix(acc, csr + 2048, m2, rs);
    epi_u(acc, sg, hdst);
    if (full) { ln_fix(acc2, csr + 3072, m2, rs); epi_g(acc2, sg); }
    WAITV(2);
    asm volatile("" :: "v"(d1));
    PHASE_END(7);
    if (full) mfma_tile<64>(acc, xf, tr0, false, nullptr, 0, 0, 0, wr, csw, nofill);
    STAMP(18);
    SB0(); BAR(); SB0();
    STAMP(19);
    if (full) { ln_fix(acc, csr, m2, rs); epi_u(acc, sg, hdst + 4096); }
    STAMP(20);
  } else {
#pragma unroll
    for (int i = 0; i < 8; ++i) glds16s(voffA, wK + 1024 + rowA + i * 16 * 3072, ldd + SLOT + i * 1024);
    {
      float s0 = 0.f, s1 = 0.f, q0 = 0.f, q1 = 0.f;
#pragma unroll
      for (int ks = 0; ks < 32; ++ks) {
        const u32x4 v = __builtin_bit_cast(u32x4, xf[ks]);
        s0 = dot2(v[0], 0x3f803f80u, s0); q0 = dot2(v[0], v[0], q0); s1 = dot2(v[1], 0x3f803f80u, s1); q1 = dot2(v[1], v[1], q1);
        s0 = dot2(v[2], 0x3f803f80u, s0); q0 = dot2(v[2], v[2], q0); s1 = dot2(v[3], 0x3f803f80u, s1); q1 = dot2(v[3], v[3], q1);
      }
      const float mean = rsum32(nv(s0 + s1)) * (1.0f / 512.0f);
      const float var = fmaxf(rsum32(nv(q0 + q1)) * (1.0f / 512.0f) - mean * mean, 0.f);
      m2 = cvtpk(-mean, -mean);
      rs = 1.0f / bf16r(sqrtf(bf16r(bf16r(var) + 1e-5f)));
      if (h == 0) { u32x2 t; t[0] = m2; t[1] = __float_as_uint(rs); *(LAS u32x2*)(uintptr_t)(lds0 + LDS_ST + row * 8) = t; }
    }
    SB0();
    WF_MERGE(0);
    unsigned tag;
    asm volatile("v_readfirstlane_b32 %0, %1" : "=s"(tag) : "v"(tagv), "v"(dummy), "v"(dummy2) : "memory");
    tag += 1u;
    PHASE_END(0);
    mfma_tile<64>(acc2, xf, tr0 + SLOT, full, wG + 128 + rowF, 16 * 2752, voffF, ldd + 3 * SLOT, wr, csw + 1024, nofill);
    PHASE_END(1);
    {
      const unsigned vi = lds0 + LDS_SCR + 16384 + row * 64;
      mfma_tile<64>(acc, xf, tr0 + 2 * SLOT, true, wG + rowP, 8 * 2752, voffP, lds0 + wave * 8192, wr, csw + 2048, [&](int ks) {
        if (ks == 0) ln_mean(acc2, csr + 1024, m2);
        if (ks >= 2 && ks <= 5) {
          const int a = ks - 2;
          u32x2 t; t[0] = cvtpk(nv(acc2[4 * a] * rs), nv(acc2[4 * a + 1] * rs)); t[1] = cvtpk(nv(acc2[4 * a + 2] * rs), nv(acc2[4 * a + 3] * rs));
          *(LAS u32x2*)(uintptr_t)(vi + 8 * ((2 * a + h) ^ sw)) = t;
        }
      });
    }
    WAITV(8);
    PHASE_END(2);
    if (wr < 2) {
      f32x16 a2;
#pragma unroll
      for (int i = 0; i < 16; ++i) a2[i] = 0.f;
      const int rowq = 8 * h + (i16 >> 2);
      const int ca = 4 * blk + (lane & 3), cb = 4 * (blk ^ wr) + (lane & 3);
      const int sw0 = (rowq >> 1) & 7, sw1 = ((rowq + 4) >> 1) & 7;
      const unsigned pk = lds0 + LDS_SCR + wr * 8192 + rowq * 64, vi = lds0 + LDS_SCR + 16384 + rowq * 64;
      const unsigned pa0 = pk + 8 * (ca ^ sw0), pa1 = pk + 256 + 8 * (ca ^ sw1), pb0 = vi + 8 * (cb ^ sw0), pb1 = vi + 256 + 8 * (cb ^ sw1);
      __builtin_amdgcn_s_setprio(3);
#pragma unroll
      for (int ks = 0; ks < 8; ++ks) {
        bf16x8 af = trfrag(pa0 + ks * 1024, pa1 + ks * 1024);
        bf16x8 bfr = trfrag(pb0 + ks * 1024, pb1 + ks * 1024);
        if (blk) {
#pragma unroll
          for (int j = 0; j < 8; ++j) bfr[j] = (short)0x3f80;
        }
        a2 = MFMA(af, bfr, a2);
      }
      asm volatile("" : "+v"(a2));
      __builtin_amdgcn_s_setprio(PRIO_E);
      char* p1w = ws + WS_P1 + (((size_t)(ng * 16 + wr * 8) * 16 + mt) * 1024) + lane * 8;
#pragma unroll
      for (int i = 0; i < 16; ++i) st_gran(p1w + (size_t)(i >> 1) * 16384 + (i & 1) * 512, __float_as_uint(a2[i]), tag);
    }
    bf16x8 fp[2], fm[2];
    ln_fix(acc, csr + 2048, m2, rs);
    feat_proj(acc, wf, r32, pp);
    if (wr >= 2) feat_soft(pp, fp[0], fm[0]);
    if (wr < 2) WAITV(16); else WAITV(0);
    PHASE_END(3);
#pragma unroll
    for (int i = 0; i < 4; ++i) glds16s(voffP, wU + rowP + i * 8 * 2752, lds0 + 2 * SLOT + wave * 8192 + i * 1024);
    if (wr < 2) feat_soft(pp, fp[0], fm[0]);
#pragma unroll
    for (int i = 4; i < 8; ++i) glds16s(voffP, wU + rowP + i * 8 * 2752, lds0 + 2 * SLOT + wave * 8192 + i * 1024);
    if (wr >= 2) feat_soft(pp + 4, fp[1], fm[1]);
    PHASE_END(4);
    u64 g1[8];
    const char* p1r = ws + WS_P1 + ((size_t)(ng * 16 + mt) * 16 + h * 8) * 1024 + blk * 512 + (wr * 16 + i16) * 8;
    mfma_tile<128>(acc, xf, trP, false, nullptr, 0, 0, 0, wr, csw + 1024, [&](int ks) { if (ks >= 16 && ks < 24) g1[ks - 16] = ld_gran(p1r + (ks - 16) * 1024); });
    WAITV(8);
    PHASE_END(5);
    {
      for (unsigned spin = 0; spin < SPIN_MAX; ++spin) {
        bool ok = true;
#pragma unroll
        for (int j = 0; j < 8; ++j) ok &= (unsigned)(g1[j] >> 32) == tag;
        if (__all(ok)) break;
        __builtin_amdgcn_s_sleep(4);
#pragma unroll
        for (int j = 0; j < 8; ++j) g1[j] = ld_gran(p1r + j * 1024);
      }
      float a = 0.f;
#pragma unroll
      for (int j = 0; j < 8; ++j) a = nv(a + __uint_as_float((unsigned)g1[j]));
      a = rsum32(a);
      auto rr = __builtin_amdgcn_permlane16_swap(__float_as_uint(a), __float_as_uint(a), false, false);
      if (lane < 16) st_gran(ws + WS_P2 + (size_t)(ng * 16 + mt) * 512 + (wr * 16 + lane) * 8, cvtpk(__uint_as_float(rr[0]), __uint_as_float(rr[1])), tag);
      if (mt == 0 && wr == 0 && lane == 0) __hip_atomic_store((GAS unsigned*)(ws + WS_EP + ng * 128), tag, __ATOMIC_RELAXED, __HIP_MEMORY_SCOPE_AGENT);
      SB0();
      ln_fix(acc, csr + 1024, m2, rs);
      epi_g(acc, sg);
    }
    PHASE_END(6);
    mfma_tile<128>(acc, xf, trP + 2 * SLOT, false, nullptr, 0, 0, 0, wr, csw + 4096, nofill);
    PHASE_END(7);
    const char* p2r = ws + WS_P2 + (size_t)(ng * 16) * 512 + lane * 8;
    u64 g2[16];
#pragma unroll
    for (int j = 0; j < 16; ++j) g2[j] = ld_gran(p2r + j * 512);
    SB0();
    ln_fix(acc, csr + 4096, m2, rs);
    epi_u(acc, sg, hdst + 2048);
    if (wr < 2) feat_soft(pp + 4, fp[1], fm[1]);
    STAMP(18);
    SB0(); BAR(); SB0();
    for (unsigned spin = 0; spin < SPIN_MAX; ++spin) {
      bool ok = true;
#pragma unroll
      for (int j = 0; j < 16; ++j) ok &= (unsigned)(g2[j] >> 32) == tag;
      if (__all(ok)) break;
      __builtin_amdgcn_s_sleep(4);
#pragma unroll
      for (int j = 0; j < 16; ++j) g2[j] = ld_gran(p2r + j * 512);
    }
    STAMP(19);
    __builtin_amdgcn_s_setprio(3);
#pragma unroll
    for (int s = 0; s < 2; ++s) {
      const u32x4 f0 = {(unsigned)g2[8 * s], (unsigned)g2[8 * s + 1], (unsigned)g2[8 * s + 2], (unsigned)g2[8 * s + 3]};
      const u32x4 f1 = {(unsigned)g2[8 * s + 4], (unsigned)g2[8 * s + 5], (unsigned)g2[8 * s + 6], (unsigned)g2[8 * s + 7]};
      f32x16 o;
#pragma unroll
      for (int q = 0; q < 16; ++q) o[q] = 0.f;
      o = MFMA(__builtin_bit_cast(bf16x8, f0), fp[s], o);
      o = MFMA(__builtin_bit_cast(bf16x8, f1), fm[s], o);
      const float rd = __builtin_amdgcn_rcpf(bf16r(bf16r(o[8]) + 1e-6f));
      float ov[8];
#pragma unroll
      for (int j = 0; j < 8; j += 2) { const unsigned u = cvtpk(o[j], o[j + 1]); ov[j] = nv(bflo(u) * rd); ov[j + 1] = nv(bfhi(u) * rd); }
      st16(ws + WS_AB + ((size_t)mblk * KS_W + 2 * ng + s) * 1024 + lane * 16, __builtin_bit_cast(u32x4, pack8(ov)));
    }
    STAMP(20);
  }
  STAMPR(31);
  STAMP_FLUSH(0);
  asm volatile(".Lk1_end:");
}

constexpr int XROW = 68;
constexpr int P2 = 10;
template <int N> DI void waitv() { asm volatile("s_waitcnt vmcnt(%0)" :: "n"(N) : "memory"); }
__global__ __launch_bounds__(256, 1) void k2_kernel(const u16* __restrict__ x, const u16* __restrict__ wo, const u16* __restrict__ w2, const char* __restrict__ ws, u16* __restrict__ out) {
  __shared__ __attribute__((aligned(16))) char smem[4 * P2 * 4096];
  float* xch = (float*)smem;
  const int tid = threadIdx.x, lane = tid & 63;
  const int wave = __builtin_amdgcn_readfirstlane(tid >> 6);
  const int b = blockIdx.x, xcd = b & 7, idx = b >> 3;
  const int mt = (xcd & 1) * 16 + (xcd >> 1) * 4 + (idx & 3), nt = idx >> 2;
  const int r32 = lane & 31, h = lane >> 5;
  const int erow = mt * 64 + (tid >> 2), ecol = nt * 64 + (tid & 3) * 16;
  const u32x4 xa = *(const u32x4*)(x + (size_t)erow * D_ + ecol), xb = *(const u32x4*)(x + (size_t)erow * D_ + ecol + 8);
  f32x16 acc[2][2];
#pragma unroll
  for (int n = 0; n < 2; ++n)
#pragma unroll
    for (int m = 0; m < 2; ++m)
#pragma unroll
      for (int i = 0; i < 16; ++i) acc[n][m][i] = 0.f;
  const int k0 = wave == 0 ? 0 : 32 + (wave - 1) * 29;
  const int nk = wave == 0 ? 32 : (wave == 3 ? 28 : 29);
  const char* wsrc = (wave == 0 ? (const char*)wo : (const char*)w2 + (size_t)(k0 - 32) * 16384) + nt * 128;
  const char* ha = ws + WS_AB + ((size_t)(2 * mt) * KS_W + k0) * 1024;
  const char* hb = ha + (size_t)KS_W * 1024;
  constexpr int RS = P2 * 4096;
  const unsigned ring = (unsigned)(uintptr_t)smem + wave * RS;
  const unsigned lo = lane * 16, voffW = (lane >> 3) * 1024 + (((lane & 7) ^ ((lane >> 2) & 4)) << 4);
  const int i16 = lane & 15, blk = (lane >> 4) & 1;
  const unsigned tb0 = ring + 2048 + (4 * h + (i16 >> 2)) * 128 + (((i16 >> 3) & 1) << 6) + 8 * (4 * blk + (i16 & 3)), tb1 = tb0 ^ 64;
  auto issue = [&](int i) {
    const unsigned d = ring + (i % P2) * 4096;
    glds16s(lo, ha + i * 1024, d); glds16s(lo, hb + i * 1024, d + 1024);
    glds16s(voffW, wsrc + i * 16384, d + 2048); glds16s(voffW, wsrc + i * 16384 + 8192, d + 3072);
  };
  bf16x8 R[2][4];
  auto rd = [&](int i, bf16x8* d) {
    const int o = (i % P2) * 4096;
    d[0] = *(const LAS bf16x8*)(uintptr_t)(ring + lo + o); d[1] = *(const LAS bf16x8*)(uintptr_t)(ring + lo + o + 1024);
    d[2] = trfrag(tb0 + o, tb0 + o + 1024); d[3] = trfrag(tb1 + o, tb1 + o + 1024);
  };
#pragma unroll
  for (int i = 0; i < P2; ++i) issue(i);
  waitv<4 * (P2 - 1)>();
  rd(0, R[0]);
#pragma unroll
  for (int i = 0; i < 32; ++i) {
    if (i < 28 || i < nk) {
      if (i + 1 < 28 || i + 1 < nk) {
        if (i + 1 < 32) {
          constexpr int full_w = 4 * (P2 - 2);
          if (4 * (26 - i) >= full_w) waitv<full_w>();
          else if (i == 19) waitv<28>(); else if (i == 20) waitv<24>(); else if (i == 21) waitv<20>(); else if (i == 22) waitv<16>();
          else if (i == 23) waitv<12>(); else if (i == 24) waitv<8>(); else if (i == 25) waitv<4>(); else waitv<0>();
          rd(i + 1, R[(i + 1) & 1]);
        }
      }
      SB0();
      const bf16x8* d = R[i & 1];
      acc[0][0] = MFMA(d[2], d[0], acc[0][0]); acc[0][1] = MFMA(d[2], d[1], acc[0][1]);
      acc[1][0] = MFMA(d[3], d[0], acc[1][0]); acc[1][1] = MFMA(d[3], d[1], acc[1][1]);
      SB0();
      if (i + P2 < 28) issue(i + P2);
      else if (i + P2 < 32) { if (i + P2 < nk) issue(i + P2); }
      SB0();
    }
  }
  __syncthreads();
  STAMP(1);
  float* mine = xch + wave * 64 * XROW;
#pragma unroll
  for (int n = 0; n < 2; ++n)
#pragma unroll
    for (int m = 0; m < 2; ++m)
#pragma unroll
      for (int a = 0; a < 4; ++a) {
        f32x4 t = {acc[n][m][4 * a], acc[n][m][4 * a + 1], acc[n][m][4 * a + 2], acc[n][m][4 * a + 3]};
        *(f32x4*)(mine + (32 * m + r32) * XROW + 32 * n + 8 * a + 4 * h) = t;
      }
  __syncthreads();
  STAMP(2);
  const float* rp = xch + (tid >> 2) * XROW + (tid & 3) * 16;
  u32x4 res[2];
#pragma unroll
  for (int q = 0; q < 4; ++q) {
    const f32x4 s0 = *(const f32x4*)(rp + 4 * q), o0 = *(const f32x4*)(rp + 64 * XROW + 4 * q);
    const f32x4 o1 = *(const f32x4*)(rp + 128 * XROW + 4 * q), o2 = *(const f32x4*)(rp + 192 * XROW + 4 * q);
#pragma unroll
    for (int e = 0; e < 4; e += 2) {
      const unsigned su = cvtpk(s0[e], s0[e + 1]);
      const unsigned ou = cvtpk((o0[e] + o1[e]) + o2[e], (o0[e + 1] + o1[e + 1]) + o2[e + 1]);
      const unsigned gu = cvtpk(silu_f(bflo(ou)), silu_f(bfhi(ou)));
      const unsigned tu = cvtpk(bflo(su) * bflo(gu), bfhi(su) * bfhi(gu));
      const unsigned xw = (q < 2 ? xa : xb)[(q & 1) * 2 + (e >> 1)];
      res[q >> 1][(q & 1) * 2 + (e >> 1)] = cvtpk(bflo(xw) + bflo(tu), bfhi(xw) + bfhi(tu));
    }
  }
  *(u32x4*)(out + (size_t)erow * D_ + ecol) = res[0];
  *(u32x4*)(out + (size_t)erow * D_ + ecol + 8) = res[1];
  STAMP(3); STAMPR(31);
  asm volatile(".Lk2_end:");
}

extern "C" void kernel_launch(void* const* d_in, const int* in_sizes, int n_in, void* d_out, int out_size, void* d_ws, size_t ws_size,
                              hipStream_t stream) {
  const u16* x = (const u16*)d_in[0];
  const u16* wqkv = (const u16*)d_in[1];
  const u16* wphi = (const u16*)d_in[2];
  const u16* wo = (const u16*)d_in[3];
  const u16* w1 = (const u16*)d_in[5];
  const u16* w3 = (const u16*)d_in[6];
  const u16* w2 = (const u16*)d_in[7];
  char* ws = (char*)d_ws;
  k1_kernel<<<256, 512, 0, stream>>>(x, wqkv, wphi, wo, w1, w3, w2, ws);
  k2_kernel<<<256, 256, 0, stream>>>(x, wo, w2, ws, (u16*)d_out);
}
```

```cpp
#include <hip/hip_runtime.h>
#include <hip/hip_bf16.h>
#include <stdint.h>

typedef __attribute__((ext_vector_type(8))) short bf16x8;
typedef __attribute__((ext_vector_type(4))) short s16x4;
typedef __attribute__((ext_vector_type(16))) float f32x16;
typedef __attribute__((ext_vector_type(4))) float f32x4;
typedef __attribute__((ext_vector_type(4))) unsigned u32x4;
typedef __attribute__((ext_vector_type(2))) unsigned u32x2;
typedef unsigned short u16;
#define LAS __attribute__((address_space(3)))
#define DI __device__ __forceinline__
#define MFMA(a, b, c) __builtin_amdgcn_mfma_f32_32x32x16_bf16((a), (b), (c), 0, 0, 0)
#define MFMA16(a, b, c) __builtin_amdgcn_mfma_f32_16x16x32_bf16(__builtin_bit_cast(bf16x8, (a)), __builtin_bit_cast(bf16x8, (b)), (c), 0, 0, 0)

constexpr int S_ = 2048, D_ = 512, NH = 32, F_ = 1376;
constexpr int KS_F = F_ / 16;
constexpr int KS_W = 32 + KS_F;
constexpr size_t WS_AB = 0;
constexpr size_t WS_WT = WS_AB + (size_t)64 * KS_W * 1024;
constexpr size_t WS_P1 = WS_WT + (size_t)16 * KS_W * 1024;
constexpr size_t WS_P2 = WS_P1 + (size_t)16 * 16 * 16 * 1024;
constexpr size_t WS_EP = WS_P2 + (size_t)16 * 16 * 512;
constexpr size_t WS_DUMP = WS_EP + 16 * 128;
constexpr size_t WS_END = WS_DUMP + 2048;

typedef __attribute__((ext_vector_type(2))) __bf16 bf16x2_t;
typedef __attribute__((ext_vector_type(2))) float f32x2;
DI unsigned cvtpk(float lo, float hi) { f32x2 v = {lo, hi}; return __builtin_bit_cast(unsigned, __builtin_convertvector(v, bf16x2_t)); }
DI float bflo(unsigned u) { return __uint_as_float(u << 16); }
DI float bfhi(unsigned u) { return __uint_as_float(u & 0xffff0000u); }
DI float bf16r(float x) { return bflo(cvtpk(x, x)); }
DI void glds16(const void* g, unsigned lds_base) {
  unsigned sv;
  asm volatile("s_mov_b32 %0, m0\n\ts_mov_b32 m0, %2\n\ts_nop 0\n\tglobal_load_lds_dwordx4 %1, off\n\ts_mov_b32 m0, %0"
               : "=&s"(sv) : "v"(g), "s"(lds_base) : "memory");
}
typedef short v4i16_t __attribute__((ext_vector_type(4)));
DI s16x4 trrd(unsigned a) { return __builtin_bit_cast(s16x4, __builtin_amdgcn_ds_read_tr16_b64_v4i16((LAS v4i16_t*)(uintptr_t)a)); }
DI bf16x8 trfrag(unsigned lo, unsigned hi) { s16x4 a = trrd(lo), b = trrd(hi); return __builtin_shufflevector(a, b, 0, 1, 2, 3, 4, 5, 6, 7); }
DI bf16x8 pack8(const float* v) {
  u32x4 p; p[0] = cvtpk(v[0], v[1]); p[1] = cvtpk(v[2], v[3]); p[2] = cvtpk(v[4], v[5]); p[3] = cvtpk(v[6], v[7]);
  return __builtin_bit_cast(bf16x8, p);
}
DI float xhalf(float v) {
  auto rr = __builtin_amdgcn_permlane32_swap(__float_as_uint(v), __float_as_uint(v), false, false);
  unsigned a = rr[0], b = rr[1];
  return __uint_as_float((threadIdx.x & 32) ? a : b);
}
template <int C> DI float dppror(float v) { return __uint_as_float(__builtin_amdgcn_update_dpp(0u, __float_as_uint(v), 0x120 + C, 0xf, 0xf, false)); }
DI float row16sum(float v) { v += dppror<8>(v); v += dppror<4>(v); v += dppror<2>(v); v += dppror<1>(v); return v; }
DI float silu_f(float x) { return x * __builtin_amdgcn_rcpf(1.0f + __builtin_amdgcn_exp2f(-1.4426950408889634f * x)); }
#define STAMP(i)
#define STAMPR(i)
#define STAMP_FLUSH(base)
#define BAR() asm volatile("s_waitcnt lgkmcnt(0)\n\ts_barrier" ::: "memory")
#define WAITV0() asm volatile("s_waitcnt vmcnt(0)" ::: "memory")

enum { T_NONE = 0, T_Q = 1, T_K = 2, T_V = 3, T_G = 4, T_U = 5 };
#define TD(ty, ix) ((unsigned long long)(((ty) << 6) | (ix)))
#define SB0() __builtin_amdgcn_sched_barrier(0)
#define GAS __attribute__((address_space(1)))
typedef unsigned long long u64;
DI float dot2(unsigned a, unsigned b, float c) {
  return __builtin_amdgcn_fdot2_f32_bf16(__builtin_bit_cast(bf16x2_t, a), __builtin_bit_cast(bf16x2_t, b), c, false);
}
DI void st_gran(void* p, unsigned val, unsigned tag) { __hip_atomic_store((GAS u64*)p, ((u64)tag << 32) | val, __ATOMIC_RELAXED, __HIP_MEMORY_SCOPE_AGENT); }
DI u64 ld_gran(const void* p) { return __hip_atomic_load((GAS u64*)p, __ATOMIC_RELAXED, __HIP_MEMORY_SCOPE_AGENT); }
constexpr unsigned SPIN_MAX = 1u << 16;

constexpr int SLOT = 32768;
constexpr int LDS_SCR = 4 * SLOT;
constexpr int LDS_DBG = LDS_SCR + 28672;
constexpr int K1_LDS = 5 * SLOT;

DI void glds16s(unsigned voff, const void* sbase, unsigned lds_base) {
  unsigned sv;
  asm volatile("s_mov_b32 %0, m0\n\ts_mov_b32 m0, %3\n\ts_nop 0\n\tglobal_load_lds_dwordx4 %1, %2\n\ts_mov_b32 m0, %0"
               : "=&s"(sv) : "v"(voff), "s"(sbase), "s"(lds_base) : "memory");
}
DI void center4(u32x4& a, float c) {
  unsigned t0, t1, t2, t3, t4, t5, t6, t7;
  asm("v_dot2_f32_bf16 %0, %8, %12, %14\n\tv_dot2_f32_bf16 %1, %8, %13, %14\n\tv_dot2_f32_bf16 %2, %9, %12, %14\n\tv_dot2_f32_bf16 %3, %9, %13, %14\n\t"
      "v_dot2_f32_bf16 %4, %10, %12, %14\n\tv_dot2_f32_bf16 %5, %10, %13, %14\n\tv_dot2_f32_bf16 %6, %11, %12, %14\n\tv_dot2_f32_bf16 %7, %11, %13, %14\n\t"
      "v_cvt_pk_bf16_f32 %0, %0, %1\n\tv_cvt_pk_bf16_f32 %2, %2, %3\n\tv_cvt_pk_bf16_f32 %4, %4, %5\n\tv_cvt_pk_bf16_f32 %6, %6, %7"
      : "=&v"(t0), "=&v"(t1), "=&v"(t2), "=&v"(t3), "=&v"(t4), "=&v"(t5), "=&v"(t6), "=&v"(t7)
      : "v"(a[0]), "v"(a[1]), "v"(a[2]), "v"(a[3]), "s"(0x00003f80u), "s"(0x3f800000u), "v"(c));
  a[0] = t0; a[1] = t2; a[2] = t4; a[3] = t6;
}
DI float rsum32(float v) { auto rr = __builtin_amdgcn_permlane32_swap(__float_as_uint(v), __float_as_uint(v), false, false); return __uint_as_float(rr[0]) + __uint_as_float(rr[1]); }
DI float rsum16(float v) { auto rr = __builtin_amdgcn_permlane16_swap(__float_as_uint(v), __float_as_uint(v), false, false); return __uint_as_float(rr[0]) + __uint_as_float(rr[1]); }
DI float rmax32(float v) { auto rr = __builtin_amdgcn_permlane32_swap(__float_as_uint(v), __float_as_uint(v), false, false); return fmaxf(__uint_as_float(rr[0]), __uint_as_float(rr[1])); }
DI float rmin32(float v) { auto rr = __builtin_amdgcn_permlane32_swap(__float_as_uint(v), __float_as_uint(v), false, false); return fminf(__uint_as_float(rr[0]), __uint_as_float(rr[1])); }
DI float nv(float x) { asm("" : "+v"(x)); return x; }
#define WAITV(n) asm volatile("s_waitcnt vmcnt(" #n ")" ::: "memory")

#define PRIO_M 2
#define PFD 4
#define LDSP 4
#define PRIO_E 0
template <int RB, int L0 = 0, int LS = LDSP, class F>
DI void mfma_tile(f32x16& acc, const bf16x8 (&xf)[32], unsigned sb, bool ld, const char* g, unsigned gstep, unsigned voff, unsigned dst, F filler) {
  __builtin_amdgcn_s_setprio(PRIO_M);
  bf16x8 wq[PFD];
#pragma unroll
  for (int ks = 0; ks < PFD; ++ks) { const int o = ((ks >> 2) * 64 + (ks & 3) * 8) * RB; wq[ks] = trfrag(sb + o, sb + o + 4 * RB); }
#pragma unroll
  for (int i = 0; i < 16; ++i) acc[i] = 0.f;
  SB0();
#pragma unroll
  for (int ks = 0; ks < 32; ++ks) {
    acc = MFMA(wq[ks % PFD], xf[ks], acc);
    if (ks + PFD < 32) { const int o = (((ks + PFD) >> 2) * 64 + ((ks + PFD) & 3) * 8) * RB; wq[ks % PFD] = trfrag(sb + o, sb + o + 4 * RB); }
    if (ks >= L0 && ((ks - L0) % LS) == 0 && ks < L0 + 8 * LS) { if (ld) glds16s(voff, g + (size_t)((ks - L0) / LS) * gstep, dst + ((ks - L0) / LS) * 1024); }
    filler(ks);
    SB0();
  }
  __builtin_amdgcn_s_setprio(PRIO_E);
  asm volatile("" : "+v"(acc));
}
DI void feat_proj(const f32x16& acc, const u32x4& wf, int r32, unsigned (&pp)[8]) {
  float av[16];
#pragma unroll
  for (int i = 0; i < 16; ++i) av[i] = acc[i];
  const bf16x8 qb0 = pack8(av), qb1 = pack8(av + 8);
  const u32x4 z4 = {0u, 0u, 0u, 0u};
  const bf16x8 a0 = __builtin_bit_cast(bf16x8, (r32 < 16) ? wf : z4), a1 = __builtin_bit_cast(bf16x8, (r32 < 16) ? z4 : wf);
  f32x16 pq;
#pragma unroll
  for (int i = 0; i < 16; ++i) pq[i] = 0.f;
  SB0();
  __builtin_amdgcn_s_setprio(3);
  pq = MFMA(a0, qb0, pq);
  pq = MFMA(a1, qb1, pq);
  SB0();
  __builtin_amdgcn_s_setprio(PRIO_E);
#pragma unroll
  for (int j = 0; j < 8; ++j) pp[j] = cvtpk(pq[2 * j], pq[2 * j + 1]);
  asm volatile("" : "+v"(pp[0]), "+v"(pp[1]), "+v"(pp[2]), "+v"(pp[3]), "+v"(pp[4]), "+v"(pp[5]), "+v"(pp[6]), "+v"(pp[7]));
}
DI void feat_soft(const unsigned* pp, bf16x8& fp, bf16x8& fm) {
  float pv[8];
#pragma unroll
  for (int j = 0; j < 4; ++j) { pv[2 * j] = bflo(pp[j]); pv[2 * j + 1] = bfhi(pp[j]); }
  float mx = fmaxf(fmaxf(fmaxf(pv[0], pv[1]), pv[2]), fmaxf(fmaxf(pv[3], pv[4]), pv[5])), mn = fminf(fminf(fminf(pv[0], pv[1]), pv[2]), fminf(fminf(pv[3], pv[4]), pv[5]));
  mx = fmaxf(fmaxf(mx, pv[6]), pv[7]); mn = fminf(fminf(mn, pv[6]), pv[7]);
  mx = rmax32(mx); mn = rmin32(mn);
  const float L2E = 1.4426950408889634f, mxl = nv(-mx * L2E), mnl = nv(mn * L2E);
  float ep[8], em[8], sp = 0.f, sm = 0.f;
#pragma unroll
  for (int j = 0; j < 8; ++j) {
    ep[j] = __builtin_amdgcn_exp2f(nv(fmaf(pv[j], L2E, mxl))); sp = nv(sp + ep[j]);
    em[j] = __builtin_amdgcn_exp2f(nv(fmaf(pv[j], -L2E, mnl))); sm = nv(sm + em[j]);
  }
  sp = nv(rsum32(sp)); sm = nv(rsum32(sm));
  const float rp = __builtin_amdgcn_rcpf(sp), rm = __builtin_amdgcn_rcpf(sm);
#pragma unroll
  for (int j = 0; j < 8; ++j) { ep[j] = nv(ep[j] * rp); em[j] = nv(em[j] * rm); }
  fp = pack8(ep); fm = pack8(em);
  asm volatile("" : "+v"(fp), "+v"(fm));
}
#define WT_STORE 1
DI void st16(void* p, u32x4 v) {
  asm volatile("global_store_dwordx4 %0, %1, off sc1\n\ts_nop 1" :: "v"(p), "v"(v) : "memory");
}
DI void epi_g(const f32x16& acc, unsigned (&sg)[8]) {
#pragma unroll
  for (int i = 0; i < 16; i += 2) {
    const unsigned u = cvtpk(acc[i], acc[i + 1]);
    sg[i >> 1] = cvtpk(nv(silu_f(bflo(u))), nv(silu_f(bfhi(u))));
  }
  asm volatile("" : "+v"(sg[0]), "+v"(sg[1]), "+v"(sg[2]), "+v"(sg[3]), "+v"(sg[4]), "+v"(sg[5]), "+v"(sg[6]), "+v"(sg[7]));
}
DI void epi_u(const f32x16& acc, const unsigned (&sg)[8], char* dst) {
  float hv[16];
#pragma unroll
  for (int i = 0; i < 16; i += 2) { const unsigned u = cvtpk(acc[i], acc[i + 1]); hv[i] = nv(bflo(sg[i >> 1]) * bflo(u)); hv[i + 1] = nv(bfhi(sg[i >> 1]) * bfhi(u)); }
  st16(dst, __builtin_bit_cast(u32x4, pack8(hv))); st16(dst + 1024, __builtin_bit_cast(u32x4, pack8(hv + 8)));
}
DI void o_head(u64 (&g2)[8], const char* p2r, unsigned tag, const bf16x8& fp, const bf16x8& fm, char* dst) {
  for (unsigned spin = 0; spin < SPIN_MAX; ++spin) {
    bool ok = true;
#pragma unroll
    for (int j = 0; j < 8; ++j) ok &= (unsigned)(g2[j] >> 32) == tag;
    if (__all(ok)) break;
    __builtin_amdgcn_s_sleep(4);
#pragma unroll
    for (int j = 0; j < 8; ++j) g2[j] = ld_gran(p2r + j * 512);
  }
  __builtin_amdgcn_s_setprio(3);
  const u32x4 f0 = {(unsigned)g2[0], (unsigned)g2[1], (unsigned)g2[2], (unsigned)g2[3]};
  const u32x4 f1 = {(unsigned)g2[4], (unsigned)g2[5], (unsigned)g2[6], (unsigned)g2[7]};
  f32x16 o;
#pragma unroll
  for (int q = 0; q < 16; ++q) o[q] = 0.f;
  o = MFMA(__builtin_bit_cast(bf16x8, f0), fp, o);
  o = MFMA(__builtin_bit_cast(bf16x8, f1), fm, o);
  const float rd = __builtin_amdgcn_rcpf(bf16r(bf16r(o[8]) + 1e-6f));
  float ov[8];
#pragma unroll
  for (int j = 0; j < 8; j += 2) { const unsigned u = cvtpk(o[j], o[j + 1]); ov[j] = nv(bflo(u) * rd); ov[j + 1] = nv(bfhi(u) * rd); }
  __builtin_amdgcn_s_setprio(PRIO_E);
  st16(dst, __builtin_bit_cast(u32x4, pack8(ov)));
}
#define PHASE_END(p) do { STAMP(2 + 2 * (p)); SB0(); BAR(); SB0(); STAMP(3 + 2 * (p)); } while (0)

__global__ __launch_bounds__(512, 2) void k1_kernel(const u16* __restrict__ x, const u16* __restrict__ wqkv, const u16* __restrict__ wphi,
                                                    const u16* __restrict__ wo, const u16* __restrict__ w1, const u16* __restrict__ w3,
                                                    const u16* __restrict__ w2, char* __restrict__ ws) {
  __shared__ __attribute__((aligned(16))) char smem[K1_LDS];
  const int tid = threadIdx.x, lane = tid & 63;
  const int wave = __builtin_amdgcn_readfirstlane(tid >> 6), grp = wave >> 2, wr = wave & 3;
  const int b = blockIdx.x, xcd = b & 7, idx = b >> 3;
  const int mt = (xcd & 1) * 8 + (idx & 7), ng = (xcd >> 1) * 4 + (idx >> 3);
  const int r32 = lane & 31, h = lane >> 5;
  const unsigned lds0 = (unsigned)(uintptr_t)smem;
  unsigned dummy0 = 0;
  if (wave == 0) {
    unsigned long long pc; unsigned len;
    asm volatile("s_getpc_b64 %0\n.Lk1_pc:\n\ts_mov_b32 %1, .Lk2_end-.Lk1_pc" : "=s"(pc), "=s"(len));
    const unsigned off = (lane * 8 + (idx & 7)) * 128;
    if (off < len) asm volatile("global_load_dword %0, %1, %2" : "+v"(dummy0) : "v"(off), "s"(pc) : "memory");
  }
  unsigned dummy1 = 0;
#define XTOUCH 0
  if (XTOUCH && grp == 0) asm volatile("global_load_dword %0, %1, %2" : "+v"(dummy1) : "v"(lane * 128), "s"((const char*)(x + (size_t)(mt * 128 + (idx >> 3) * 32 + wr * 8) * D_)) : "memory");
  const unsigned reg = lds0 + SLOT + (wr * 2 + grp) * 16384;
  {
    const char* xb = (const char*)(x + (size_t)(mt * 128 + wr * 32 + grp * 16) * D_);
    const int rr = lane >> 5, qd = (lane >> 3) & 3, pos = lane & 7;
#pragma unroll
    for (int cc = 0; cc < 2; ++cc)
#pragma unroll
      for (int i = 0; i < 8; ++i)
        glds16s(rr * 1024 + qd * 256 + (pos << 4), xb + i * 2048 + cc * 128, reg + cc * 8192 + i * 1024);
  }
  const int g0 = ng < 11 ? 3 * ng : 33 + 2 * (ng - 11);
  const bool full = ng < 11;
  const char* wK = (const char*)(wqkv + 512 + 32 * ng);
  const char* wG = (const char*)(w1 + 32 * g0);
  const char* wU = (const char*)(w3 + 32 * g0);
  const unsigned voffA = (lane >> 2) * 3072 + (lane & 3) * 16, voffF = (lane >> 2) * 2752 + (lane & 3) * 16;
  unsigned dummy = 0, dummy2 = 0, tagv;
  u32x4 wf, wfb;
  {
    tagv = __hip_atomic_load((GAS unsigned*)(ws + WS_EP + ng * 128), __ATOMIC_RELAXED, __HIP_MEMORY_SCOPE_AGENT);
    const u16* wp = wphi + (size_t)(2 * ng + (r32 >> 4)) * 256 + (4 * h) * 16 + (lane & 15);
#pragma unroll
    for (int j = 0; j < 4; ++j) {
      const int k0 = 8 * (j >> 1) + 2 * (j & 1);
      asm volatile("global_load_ushort %0, %2, off offset:%3\n\tglobal_load_ushort %1, %2, off offset:%4" : "=&v"(wf[j]), "=&v"(wfb[j]) : "v"(wp), "n"(k0 * 32), "n"(k0 * 32 + 32) : "memory");
    }
    if (grp) {
      const char* g = wK + (size_t)wr * 128 * 3072;
#pragma unroll
      for (int i = 0; i < 8; ++i) glds16s(voffA, g + i * 16 * 3072, lds0 + wr * 8192 + i * 1024);
      const int trow = (idx & 7) * 64 + lane;
      const int e = (wr == 3 && !full) ? 0 : wr - 1;
      const char* p = wr == 0 ? wK + 1024 + (size_t)trow * 3072 : wG + 64 * e + (size_t)trow * 2752;
      const char* p2 = wr == 0 ? wK - 1024 + (size_t)trow * 3072 : wU + 64 * e + (size_t)trow * 2752;
      asm volatile("global_load_dword %0, %1, off" : "+v"(dummy) : "v"(p) : "memory");
      asm volatile("global_load_dword %0, %1, off" : "+v"(dummy2) : "v"(p2) : "memory");
    }
  }
  bf16x8 xf[32];
  unsigned tag;
  {
    const int r16 = lane & 15, qd = lane >> 4;
    const unsigned rb = reg + (r16 >> 1) * 1024 + (r16 & 1) * 512 + qd * 128 + ((r16 & 7) << 4);
    u32x4 xr[16];
    float q0 = 0.f, q1 = 0.f;
    f32x4 sum4 = {0.f, 0.f, 0.f, 0.f};
    const u32x4 ones = {0x3f803f80u, 0x3f803f80u, 0x3f803f80u, 0x3f803f80u};
    if (grp) WAITV(27); else WAITV(17);
#pragma unroll
    for (int q = 0; q < 8; ++q) xr[q] = *(const LAS u32x4*)(uintptr_t)(rb ^ (q << 4));
#pragma unroll
    for (int q = 0; q < 8; ++q)
#pragma unroll
      for (int e = 0; e < 4; e += 2) {
        if (e == 0) sum4 = MFMA16(ones, xr[q], sum4);
        q0 = dot2(xr[q][e], xr[q][e], q0); q1 = dot2(xr[q][e + 1], xr[q][e + 1], q1);
      }
    asm volatile("" : "+v"(sum4), "+v"(q0), "+v"(q1));
    if (grp) WAITV(19); else WAITV(9);
#pragma unroll
    for (int q = 0; q < 8; ++q) xr[8 + q] = *(const LAS u32x4*)(uintptr_t)((rb ^ (q << 4)) + 8192);
#pragma unroll
    for (int q = 8; q < 16; ++q)
#pragma unroll
      for (int e = 0; e < 4; e += 2) {
        if (e == 0) sum4 = MFMA16(ones, xr[q], sum4);
        q0 = dot2(xr[q][e], xr[q][e], q0); q1 = dot2(xr[q][e + 1], xr[q][e + 1], q1);
      }
    const float mean = sum4[0] * (1.0f / 512.0f);
    const float var = fmaxf(rsum32(rsum16(q0 + q1)) * (1.0f / 512.0f) - mean * mean, 0.f);
    const float nmb = -bf16r(mean);
    const float rs = 1.0f / bf16r(sqrtf(bf16r(bf16r(var) + 1e-5f)));
    const unsigned wb = reg + qd * 4096 + r16 * 16 + (r16 & 3) * 512 + ((r16 >> 2) & 1) * 256;
#pragma unroll
    for (int q = 0; q < 16; ++q) {
      u32x4 o;
      center4(xr[q], nmb);
#pragma unroll
      for (int e = 0; e < 4; ++e) o[e] = cvtpk(bflo(xr[q][e]) * rs, bfhi(xr[q][e]) * rs);
      *(LAS u32x4*)(uintptr_t)((wb ^ ((q & 3) * 512 + ((q >> 2) & 1) * 256)) + (q >> 3) * 2048) = o;
    }
    SB0();
    WAITV(0);
    asm volatile("" : "+v"(dummy), "+v"(dummy0), "+v"(dummy1), "+v"(dummy2), "+v"(wf), "+v"(wfb));
#pragma unroll
    for (int j = 0; j < 4; ++j) wf[j] |= wfb[j] << 16;
    tag = __builtin_amdgcn_readfirstlane(tagv) + 1u;
    asm volatile("" : "+s"(tag));
    BAR();
    const unsigned xb = lds0 + SLOT * (1 + wr) + (r32 >> 4) * 16384 + (h * 16 + (r32 & 15)) * 16;
#pragma unroll
    for (int ks = 0; ks < 32; ++ks) xf[ks] = *(const LAS bf16x8*)(uintptr_t)(xb + ks * 512);
    BAR();
  }
  STAMP(1);
  const unsigned voffP = (lane >> 3) * 2752 + (((lane & 7) ^ ((lane >> 2) & 4)) << 4);
  const size_t rowP = (size_t)wave * 64 * 2752;
  const int i16 = lane & 15, blk = (lane >> 4) & 1;
  const unsigned tr0 = lds0 + (32 * h + (i16 >> 2)) * 64 + 8 * (4 * blk + (i16 & 3));
  const unsigned trP = lds0 + (32 * h + (i16 >> 2)) * 128 + (((grp ^ (i16 >> 3)) & 1) << 6) + 8 * (4 * blk + (i16 & 3));
  const int mblk = mt * 4 + wr;
  const unsigned ldd = lds0 + wr * 8192;
  const size_t rowF = (size_t)wr * 128 * 2752, rowA = (size_t)wr * 128 * 3072;
  char* const hdst = ws + WS_AB + ((size_t)mblk * KS_W + 32 + 2 * g0) * 1024 + lane * 16;
  unsigned sg[8], pp[8];
  f32x16 acc, acc2;
  const int row = wr * 32 + r32, sw = (row >> 1) & 7;
  auto nofill = [](int) {};
  const unsigned ppx = lds0 + LDS_SCR + 24576 + wr * 1024 + lane * 16;
  const char* p2r = ws + WS_P2 + (size_t)(ng * 16 + (grp ? 0 : 8)) * 512 + lane * 8;
  char* const odst = ws + WS_AB + ((size_t)mblk * KS_W + 2 * ng + (grp ? 0 : 1)) * 1024 + lane * 16;
  u64 g2[8];
  bf16x8 fq, fn;
  if (grp == 0) {
#define QL0 16
    mfma_tile<64, QL0, 2>(acc, xf, tr0, true, wK - 1024 + rowA, 16 * 3072, voffA, ldd + 2 * SLOT, nofill);
    PHASE_END(0);
    feat_proj(acc, wf, r32, pp);
#pragma unroll
    for (int s = 0; s < 2; ++s) {
      if (s) {
#pragma unroll
        for (int i = 0; i < 4; ++i) glds16s(voffP, wG + rowP + i * 8 * 2752, lds0 + wave * 8192 + i * 1024);
      }
      bf16x8 fp, fm;
      feat_soft(pp + 4 * s, fp, fm);
      if (s) {
#pragma unroll
        for (int i = 4; i < 8; ++i) glds16s(voffP, wG + rowP + i * 8 * 2752, lds0 + wave * 8192 + i * 1024);
      }
      const unsigned pk = lds0 + LDS_SCR + s * 8192 + row * 64;
      const u32x4 up = __builtin_bit_cast(u32x4, fp), um = __builtin_bit_cast(u32x4, fm);
      u32x2 t;
      t[0] = up[0]; t[1] = up[1]; *(LAS u32x2*)(uintptr_t)(pk + 8 * ((h) ^ sw)) = t;
      t[0] = up[2]; t[1] = up[3]; *(LAS u32x2*)(uintptr_t)(pk + 8 * ((2 + h) ^ sw)) = t;
      t[0] = um[0]; t[1] = um[1]; *(LAS u32x2*)(uintptr_t)(pk + 8 * ((4 + h) ^ sw)) = t;
      t[0] = um[2]; t[1] = um[3]; *(LAS u32x2*)(uintptr_t)(pk + 8 * ((6 + h) ^ sw)) = t;
      if (s == 0) { SB0(); WAITV(0); }
      PHASE_END(1 + s);
    }
#define TOUCHW 1
    unsigned d0 = 0, d1 = 0;
    const int tr = idx * 59 + (lane < 59 ? lane : 58);
    const char* tp = (tr < 512 ? (const char*)wo + (size_t)tr * 1024 : (const char*)w2 + (size_t)(tr - 512) * 1024) + wr * 256;
    if (TOUCHW == 1) asm volatile("global_load_dword %0, %1, off" : "+v"(d0) : "v"(tp) : "memory");
    if (full) mfma_tile<64>(acc2, xf, tr0 + 3 * SLOT, false, nullptr, 0, 0, 0, nofill);
    if (TOUCHW == 1) WAITV(1); else WAITV(0);
    PHASE_END(3);
    mfma_tile<128>(acc, xf, trP, true, wU + rowP, 8 * 2752, voffP, lds0 + 2 * SLOT + wave * 8192, nofill);
    PHASE_END(4);
    if (TOUCHW == 2) asm volatile("global_load_dword %0, %1, off" : "+v"(d0) : "v"(tp) : "memory");
    if (TOUCHW) asm volatile("global_load_dword %0, %1, off offset:128" : "+v"(d1) : "v"(tp) : "memory");
    epi_g(acc, sg);
    { const u32x4 t = *(const LAS u32x4*)(uintptr_t)ppx; pp[0] = t[0]; pp[1] = t[1]; pp[2] = t[2]; pp[3] = t[3]; }
    feat_soft(pp, fq, fn);
    if (TOUCHW == 2) WAITV(2); else if (TOUCHW == 1) WAITV(1); else WAITV(0);
    PHASE_END(5);
    mfma_tile<128>(acc, xf, trP + 2 * SLOT, full, wU + 128 + rowF, 16 * 2752, voffF, ldd, nofill);
    PHASE_END(6);
    epi_u(acc, sg, hdst);
    if (full) epi_g(acc2, sg);
    WAITV(2);
    asm volatile("" :: "v"(d0), "v"(d1));
    PHASE_END(7);
    if (full) {
      mfma_tile<64>(acc, xf, tr0, false, nullptr, 0, 0, 0, [&](int ks) { if (ks >= 16 && ks < 24) g2[ks - 16] = ld_gran(p2r + (ks - 16) * 512); });
    } else {
#pragma unroll
      for (int j = 0; j < 8; ++j) g2[j] = ld_gran(p2r + j * 512);
    }
    STAMP(18);
    o_head(g2, p2r, tag, fq, fn, odst);
    STAMP(19);
    if (full) epi_u(acc, sg, hdst + 4096);
    STAMP(20);
  } else {
#pragma unroll
    for (int i = 0; i < 8; ++i) glds16s(voffA, wK + 1024 + rowA + i * 16 * 3072, ldd + SLOT + i * 1024);
    WAITV(0);
    PHASE_END(0);
    mfma_tile<64>(acc2, xf, tr0 + SLOT, full, wG + 128 + rowF, 16 * 2752, voffF, ldd + 3 * SLOT, nofill);
    PHASE_END(1);
    {
      const unsigned vi = lds0 + LDS_SCR + 16384 + row * 64;
      mfma_tile<64>(acc, xf, tr0 + 2 * SLOT, true, wG + rowP, 8 * 2752, voffP, lds0 + wave * 8192, [&](int ks) {
        if (ks >= 1 && ks <= 4) {
          const int a = ks - 1;
          u32x2 t; t[0] = cvtpk(acc2[4 * a], acc2[4 * a + 1]); t[1] = cvtpk(acc2[4 * a + 2], acc2[4 * a + 3]);
          *(LAS u32x2*)(uintptr_t)(vi + 8 * ((2 * a + h) ^ sw)) = t;
        }
      });
    }
    WAITV(8);
    PHASE_END(2);
    if (wr < 2) {
      f32x16 a2;
#pragma unroll
      for (int i = 0; i < 16; ++i) a2[i] = 0.f;
      const int rowq = 8 * h + (i16 >> 2);
      const int ca = 4 * blk + (lane & 3), cb = 4 * (blk ^ wr) + (lane & 3);
      const int sw0 = (rowq >> 1) & 7, sw1 = ((rowq + 4) >> 1) & 7;
      const unsigned pk = lds0 + LDS_SCR + wr * 8192 + rowq * 64, vi = lds0 + LDS_SCR + 16384 + rowq * 64;
      const unsigned pa0 = pk + 8 * (ca ^ sw0), pa1 = pk + 256 + 8 * (ca ^ sw1), pb0 = vi + 8 * (cb ^ sw0), pb1 = vi + 256 + 8 * (cb ^ sw1);
      __builtin_amdgcn_s_setprio(3);
#pragma unroll
      for (int ks = 0; ks < 8; ++ks) {
        bf16x8 af = trfrag(pa0 + ks * 1024, pa1 + ks * 1024);
        bf16x8 bfr = trfrag(pb0 + ks * 1024, pb1 + ks * 1024);
        if (blk) {
#pragma unroll
          for (int j = 0; j < 8; ++j) bfr[j] = (short)0x3f80;
        }
        a2 = MFMA(af, bfr, a2);
      }
      asm volatile("" : "+v"(a2));
      __builtin_amdgcn_s_setprio(PRIO_E);
      char* p1w = ws + WS_P1 + (((size_t)(ng * 16 + wr * 8) * 16 + mt) * 1024) + lane * 8;
#pragma unroll
      for (int i = 0; i < 16; ++i) st_gran(p1w + (size_t)(i >> 1) * 16384 + (i & 1) * 512, __float_as_uint(a2[i]), tag);
    }
    feat_proj(acc, wf, r32, pp);
    { const u32x4 t = {pp[4], pp[5], pp[6], pp[7]}; *(LAS u32x4*)(uintptr_t)ppx = t; }
    if (wr >= 2) feat_soft(pp, fq, fn);
    if (wr < 2) WAITV(16); else WAITV(0);
    PHASE_END(3);
#pragma unroll
    for (int i = 0; i < 4; ++i) glds16s(voffP, wU + rowP + i * 8 * 2752, lds0 + 2 * SLOT + wave * 8192 + i * 1024);
    if (wr < 2) feat_soft(pp, fq, fn);
#pragma unroll
    for (int i = 4; i < 8; ++i) glds16s(voffP, wU + rowP + i * 8 * 2752, lds0 + 2 * SLOT + wave * 8192 + i * 1024);
    PHASE_END(4);
    u64 g1[8];
    const char* p1r = ws + WS_P1 + ((size_t)(ng * 16 + mt) * 16 + h * 8) * 1024 + blk * 512 + (wr * 16 + i16) * 8;
    mfma_tile<128>(acc, xf, trP, false, nullptr, 0, 0, 0, [&](int ks) { if (ks >= 16 && ks < 24) g1[ks - 16] = ld_gran(p1r + (ks - 16) * 1024); });
    WAITV(8);
    PHASE_END(5);
    {
      for (unsigned spin = 0; spin < SPIN_MAX; ++spin) {
        bool ok = true;
#pragma unroll
        for (int j = 0; j < 8; ++j) ok &= (unsigned)(g1[j] >> 32) == tag;
        if (__all(ok)) break;
        __builtin_amdgcn_s_sleep(4);
#pragma unroll
        for (int j = 0; j < 8; ++j) g1[j] = ld_gran(p1r + j * 1024);
      }
      float a = 0.f;
#pragma unroll
      for (int j = 0; j < 8; ++j) a = nv(a + __uint_as_float((unsigned)g1[j]));
      a = rsum32(a);
      auto rr = __builtin_amdgcn_permlane16_swap(__float_as_uint(a), __float_as_uint(a), false, false);
      if (lane < 16) st_gran(ws + WS_P2 + (size_t)(ng * 16 + mt) * 512 + (wr * 16 + lane) * 8, cvtpk(__uint_as_float(rr[0]), __uint_as_float(rr[1])), tag);
      if (mt == 0 && wr == 0 && lane == 0) __hip_atomic_store((GAS unsigned*)(ws + WS_EP + ng * 128), tag, __ATOMIC_RELAXED, __HIP_MEMORY_SCOPE_AGENT);
      SB0();
      epi_g(acc, sg);
    }
    PHASE_END(6);
    mfma_tile<128>(acc, xf, trP + 2 * SLOT, false, nullptr, 0, 0, 0, [&](int ks) { if (ks >= 20 && ks < 28) g2[ks - 20] = ld_gran(p2r + (ks - 20) * 512); });
    PHASE_END(7);
    o_head(g2, p2r, tag, fq, fn, odst);
    STAMP(19);
    epi_u(acc, sg, hdst + 2048);
    STAMP(20);
  }
  STAMPR(31);
  STAMP_FLUSH(0);
  asm volatile(".Lk1_end:");
}

constexpr int XROW = 68;
constexpr int P2 = 10;
template <int N> DI void waitv() { asm volatile("s_waitcnt vmcnt(%0)" :: "n"(N) : "memory"); }
__global__ __launch_bounds__(256, 1) void k2_kernel(const u16* __restrict__ x, const u16* __restrict__ wo, const u16* __restrict__ w2, const char* __restrict__ ws, u16* __restrict__ out) {
  __shared__ __attribute__((aligned(16))) char smem[4 * P2 * 4096];
  float* xch = (float*)smem;
  const int tid = threadIdx.x, lane = tid & 63;
  const int wave = __builtin_amdgcn_readfirstlane(tid >> 6);
  const int b = blockIdx.x, xcd = b & 7, idx = b >> 3;
  const int mt = (xcd & 1) * 16 + (xcd >> 1) * 4 + (idx & 3), nt = idx >> 2;
  const int r32 = lane & 31, h = lane >> 5;
  const int erow = mt * 64 + (tid >> 2), ecol = nt * 64 + (tid & 3) * 16;
  const u32x4 xa = *(const u32x4*)(x + (size_t)erow * D_ + ecol), xb = *(const u32x4*)(x + (size_t)erow * D_ + ecol + 8);
  f32x16 acc[2][2];
#pragma unroll
  for (int n = 0; n < 2; ++n)
#pragma unroll
    for (int m = 0; m < 2; ++m)
#pragma unroll
      for (int i = 0; i < 16; ++i) acc[n][m][i] = 0.f;
  const int k0 = wave == 0 ? 0 : 32 + (wave - 1) * 29;
  const int nk = wave == 0 ? 32 : (wave == 3 ? 28 : 29);
  const char* wsrc = (wave == 0 ? (const char*)wo : (const char*)w2 + (size_t)(k0 - 32) * 16384) + nt * 128;
  const char* ha = ws + WS_AB + ((size_t)(2 * mt) * KS_W + k0) * 1024;
  const char* hb = ha + (size_t)KS_W * 1024;
  constexpr int RS = P2 * 4096;
  const unsigned ring = (unsigned)(uintptr_t)smem + wave * RS;
  const unsigned lo = lane * 16, voffW = (lane >> 3) * 1024 + (((lane & 7) ^ ((lane >> 2) & 4)) << 4);
  const int i16 = lane & 15, blk = (lane >> 4) & 1;
  const unsigned tb0 = ring + 2048 + (4 * h + (i16 >> 2)) * 128 + (((i16 >> 3) & 1) << 6) + 8 * (4 * blk + (i16 & 3)), tb1 = tb0 ^ 64;
  auto issue = [&](int i) {
    const unsigned d = ring + (i % P2) * 4096;
    glds16s(lo, ha + i * 1024, d); glds16s(lo, hb + i * 1024, d + 1024);
    glds16s(voffW, wsrc + i * 16384, d + 2048); glds16s(voffW, wsrc + i * 16384 + 8192, d + 3072);
  };
  bf16x8 R[2][4];
  auto rd = [&](int i, bf16x8* d) {
    const int o = (i % P2) * 4096;
    d[0] = *(const LAS bf16x8*)(uintptr_t)(ring + lo + o); d[1] = *(const LAS bf16x8*)(uintptr_t)(ring + lo + o + 1024);
    d[2] = trfrag(tb0 + o, tb0 + o + 1024); d[3] = trfrag(tb1 + o, tb1 + o + 1024);
  };
#pragma unroll
  for (int i = 0; i < P2; ++i) issue(i);
  waitv<4 * (P2 - 1)>();
  rd(0, R[0]);
#pragma unroll
  for (int i = 0; i < 32; ++i) {
    if (i < 28 || i < nk) {
      if (i + 1 < 28 || i + 1 < nk) {
        if (i + 1 < 32) {
          constexpr int full_w = 4 * (P2 - 2);
          if (4 * (26 - i) >= full_w) waitv<full_w>();
          else if (i == 19) waitv<28>(); else if (i == 20) waitv<24>(); else if (i == 21) waitv<20>(); else if (i == 22) waitv<16>();
          else if (i == 23) waitv<12>(); else if (i == 24) waitv<8>(); else if (i == 25) waitv<4>(); else waitv<0>();
          rd(i + 1, R[(i + 1) & 1]);
        }
      }
      SB0();
      const bf16x8* d = R[i & 1];
      acc[0][0] = MFMA(d[2], d[0], acc[0][0]); acc[0][1] = MFMA(d[2], d[1], acc[0][1]);
      acc[1][0] = MFMA(d[3], d[0], acc[1][0]); acc[1][1] = MFMA(d[3], d[1], acc[1][1]);
      SB0();
      if (i + P2 < 28) issue(i + P2);
      else if (i + P2 < 32) { if (i + P2 < nk) issue(i + P2); }
      SB0();
    }
  }
  __syncthreads();
  STAMP(1);
  float* mine = xch + wave * 64 * XROW;
#pragma unroll
  for (int n = 0; n < 2; ++n)
#pragma unroll
    for (int m = 0; m < 2; ++m)
#pragma unroll
      for (int a = 0; a < 4; ++a) {
        f32x4 t = {acc[n][m][4 * a], acc[n][m][4 * a + 1], acc[n][m][4 * a + 2], acc[n][m][4 * a + 3]};
        *(f32x4*)(mine + (32 * m + r32) * XROW + 32 * n + 8 * a + 4 * h) = t;
      }
  __syncthreads();
  STAMP(2);
  const float* rp = xch + (tid >> 2) * XROW + (tid & 3) * 16;
  u32x4 res[2];
#pragma unroll
  for (int q = 0; q < 4; ++q) {
    const f32x4 s0 = *(const f32x4*)(rp + 4 * q), o0 = *(const f32x4*)(rp + 64 * XROW + 4 * q);
    const f32x4 o1 = *(const f32x4*)(rp + 128 * XROW + 4 * q), o2 = *(const f32x4*)(rp + 192 * XROW + 4 * q);
#pragma unroll
    for (int e = 0; e < 4; e += 2) {
      const unsigned su = cvtpk(s0[e], s0[e + 1]);
      const unsigned ou = cvtpk((o0[e] + o1[e]) + o2[e], (o0[e + 1] + o1[e + 1]) + o2[e + 1]);
      const unsigned gu = cvtpk(silu_f(bflo(ou)), silu_f(bfhi(ou)));
      const unsigned tu = cvtpk(bflo(su) * bflo(gu), bfhi(su) * bfhi(gu));
      const unsigned xw = (q < 2 ? xa : xb)[(q & 1) * 2 + (e >> 1)];
      res[q >> 1][(q & 1) * 2 + (e >> 1)] = cvtpk(bflo(xw) + bflo(tu), bfhi(xw) + bfhi(tu));
    }
  }
  *(u32x4*)(out + (size_t)erow * D_ + ecol) = res[0];
  *(u32x4*)(out + (size_t)erow * D_ + ecol + 8) = res[1];
  STAMP(3); STAMPR(31);
  asm volatile(".Lk2_end:");
}

extern "C" void kernel_launch(void* const* d_in, const int* in_sizes, int n_in, void* d_out, int out_size, void* d_ws, size_t ws_size,
                              hipStream_t stream) {
  const u16* x = (const u16*)d_in[0];
  const u16* wqkv = (const u16*)d_in[1];
  const u16* wphi = (const u16*)d_in[2];
  const u16* wo = (const u16*)d_in[3];
  const u16* w1 = (const u16*)d_in[5];
  const u16* w3 = (const u16*)d_in[6];
  const u16* w2 = (const u16*)d_in[7];
  char* ws = (char*)d_ws;
  k1_kernel<<<256, 512, 0, stream>>>(x, wqkv, wphi, wo, w1, w3, w2, ws);
  k2_kernel<<<256, 256, 0, stream>>>(x, wo, w2, ws, (u16*)d_out);
}
```

```cpp
#include <hip/hip_runtime.h>
#include <hip/hip_bf16.h>
#include <stdint.h>

typedef __attribute__((ext_vector_type(8))) short bf16x8;
typedef __attribute__((ext_vector_type(4))) short s16x4;
typedef __attribute__((ext_vector_type(16))) float f32x16;
typedef __attribute__((ext_vector_type(4))) float f32x4;
typedef __attribute__((ext_vector_type(4))) unsigned u32x4;
typedef __attribute__((ext_vector_type(2))) unsigned u32x2;
typedef unsigned short u16;
#define LAS __attribute__((address_space(3)))
#define DI __device__ __forceinline__
#define MFMA(a, b, c) __builtin_amdgcn_mfma_f32_32x32x16_bf16((a), (b), (c), 0, 0, 0)
#define MFMA16(a, b, c) __builtin_amdgcn_mfma_f32_16x16x32_bf16(__builtin_bit_cast(bf16x8, (a)), __builtin_bit_cast(bf16x8, (b)), (c), 0, 0, 0)

constexpr int S_ = 2048, D_ = 512, NH = 32, F_ = 1376;
constexpr int KS_F = F_ / 16;
constexpr int KS_W = 32 + KS_F;
constexpr size_t WS_AB = 0;
constexpr size_t WS_WT = WS_AB + (size_t)64 * KS_W * 1024;
constexpr size_t WS_P1 = WS_WT + (size_t)16 * KS_W * 1024;
constexpr size_t WS_P2 = WS_P1 + (size_t)16 * 16 * 16 * 1024;
constexpr size_t WS_EP = WS_P2 + (size_t)16 * 16 * 512;
constexpr size_t WS_DUMP = WS_EP + 16 * 128;
constexpr size_t WS_END = WS_DUMP + 2048;

typedef __attribute__((ext_vector_type(2))) __bf16 bf16x2_t;
typedef __attribute__((ext_vector_type(2))) float f32x2;
DI unsigned cvtpk(float lo, float hi) { f32x2 v = {lo, hi}; return __builtin_bit_cast(unsigned, __builtin_convertvector(v, bf16x2_t)); }
DI float bflo(unsigned u) { return __uint_as_float(u << 16); }
DI float bfhi(unsigned u) { return __uint_as_float(u & 0xffff0000u); }
DI float bf16r(float x) { return bflo(cvtpk(x, x)); }
DI void glds16(const void* g, unsigned lds_base) {
  unsigned sv;
  asm volatile("s_mov_b32 %0, m0\n\ts_mov_b32 m0, %2\n\ts_nop 0\n\tglobal_load_lds_dwordx4 %1, off\n\ts_mov_b32 m0, %0"
               : "=&s"(sv) : "v"(g), "s"(lds_base) : "memory");
}
typedef short v4i16_t __attribute__((ext_vector_type(4)));
DI s16x4 trrd(unsigned a) { return __builtin_bit_cast(s16x4, __builtin_amdgcn_ds_read_tr16_b64_v4i16((LAS v4i16_t*)(uintptr_t)a)); }
DI bf16x8 trfrag(unsigned lo, unsigned hi) { s16x4 a = trrd(lo), b = trrd(hi); return __builtin_shufflevector(a, b, 0, 1, 2, 3, 4, 5, 6, 7); }
DI bf16x8 pack8(const float* v) {
  u32x4 p; p[0] = cvtpk(v[0], v[1]); p[1] = cvtpk(v[2], v[3]); p[2] = cvtpk(v[4], v[5]); p[3] = cvtpk(v[6], v[7]);
  return __builtin_bit_cast(bf16x8, p);
}
DI float xhalf(float v) {
  auto rr = __builtin_amdgcn_permlane32_swap(__float_as_uint(v), __float_as_uint(v), false, false);
  unsigned a = rr[0], b = rr[1];
  return __uint_as_float((threadIdx.x & 32) ? a : b);
}
template <int C> DI float dppror(float v) { return __uint_as_float(__builtin_amdgcn_update_dpp(0u, __float_as_uint(v), 0x120 + C, 0xf, 0xf, false)); }
DI float row16sum(float v) { v += dppror<8>(v); v += dppror<4>(v); v += dppror<2>(v); v += dppror<1>(v); return v; }
DI float silu_f(float x) { return x * __builtin_amdgcn_rcpf(1.0f + __builtin_amdgcn_exp2f(-1.4426950408889634f * x)); }
#define STAMP(i)
#define STAMPR(i)
#define STAMP_FLUSH(base)
#define BAR() asm volatile("s_waitcnt lgkmcnt(0)\n\ts_barrier" ::: "memory")
#define WAITV0() asm volatile("s_waitcnt vmcnt(0)" ::: "memory")

enum { T_NONE = 0, T_Q = 1, T_K = 2, T_V = 3, T_G = 4, T_U = 5 };
#define TD(ty, ix) ((unsigned long long)(((ty) << 6) | (ix)))
#define SB0() __builtin_amdgcn_sched_barrier(0)
#define GAS __attribute__((address_space(1)))
typedef unsigned long long u64;
DI float dot2(unsigned a, unsigned b, float c) {
  return __builtin_amdgcn_fdot2_f32_bf16(__builtin_bit_cast(bf16x2_t, a), __builtin_bit_cast(bf16x2_t, b), c, false);
}
DI void st_gran(void* p, unsigned val, unsigned tag) { __hip_atomic_store((GAS u64*)p, ((u64)tag << 32) | val, __ATOMIC_RELAXED, __HIP_MEMORY_SCOPE_AGENT); }
DI u64 ld_gran(const void* p) { return __hip_atomic_load((GAS u64*)p, __ATOMIC_RELAXED, __HIP_MEMORY_SCOPE_AGENT); }
constexpr unsigned SPIN_MAX = 1u << 16;

constexpr int SLOT = 32768;
constexpr int LDS_SCR = 4 * SLOT;
constexpr int LDS_DBG = LDS_SCR + 28672;
constexpr int K1_LDS = 5 * SLOT;

DI void glds16s(unsigned voff, const void* sbase, unsigned lds_base) {
  unsigned sv;
  asm volatile("s_mov_b32 %0, m0\n\ts_mov_b32 m0, %3\n\ts_nop 0\n\tglobal_load_lds_dwordx4 %1, %2\n\ts_mov_b32 m0, %0"
               : "=&s"(sv) : "v"(voff), "s"(sbase), "s"(lds_base) : "memory");
}
DI void center4(u32x4& a, float c) {
  unsigned t0, t1, t2, t3, t4, t5, t6, t7;
  asm("v_dot2_f32_bf16 %0, %8, %12, %14\n\tv_dot2_f32_bf16 %1, %8, %13, %14\n\tv_dot2_f32_bf16 %2, %9, %12, %14\n\tv_dot2_f32_bf16 %3, %9, %13, %14\n\t"
      "v_dot2_f32_bf16 %4, %10, %12, %14\n\tv_dot2_f32_bf16 %5, %10, %13, %14\n\tv_dot2_f32_bf16 %6, %11, %12, %14\n\tv_dot2_f32_bf16 %7, %11, %13, %14\n\t"
      "v_cvt_pk_bf16_f32 %0, %0, %1\n\tv_cvt_pk_bf16_f32 %2, %2, %3\n\tv_cvt_pk_bf16_f32 %4, %4, %5\n\tv_cvt_pk_bf16_f32 %6, %6, %7"
      : "=&v"(t0), "=&v"(t1), "=&v"(t2), "=&v"(t3), "=&v"(t4), "=&v"(t5), "=&v"(t6), "=&v"(t7)
      : "v"(a[0]), "v"(a[1]), "v"(a[2]), "v"(a[3]), "s"(0x00003f80u), "s"(0x3f800000u), "v"(c));
  a[0] = t0; a[1] = t2; a[2] = t4; a[3] = t6;
}
DI float rsum32(float v) { auto rr = __builtin_amdgcn_permlane32_swap(__float_as_uint(v), __float_as_uint(v), false, false); return __uint_as_float(rr[0]) + __uint_as_float(rr[1]); }
DI float rsum16(float v) { auto rr = __builtin_amdgcn_permlane16_swap(__float_as_uint(v), __float_as_uint(v), false, false); return __uint_as_float(rr[0]) + __uint_as_float(rr[1]); }
DI float rmax32(float v) { auto rr = __builtin_amdgcn_permlane32_swap(__float_as_uint(v), __float_as_uint(v), false, false); return fmaxf(__uint_as_float(rr[0]), __uint_as_float(rr[1])); }
DI float rmin32(float v) { auto rr = __builtin_amdgcn_permlane32_swap(__float_as_uint(v), __float_as_uint(v), false, false); return fminf(__uint_as_float(rr[0]), __uint_as_float(rr[1])); }
DI float nv(float x) { asm("" : "+v"(x)); return x; }
#define WAITV(n) asm volatile("s_waitcnt vmcnt(" #n ")" ::: "memory")

#define PRIO_M 2
#define PFD 4
#define LDSP 4
#define PRIO_E 0
template <int RB, int L0 = 0, int LS = LDSP, class F>
DI void mfma_tile(f32x16& acc, const bf16x8 (&xf)[32], unsigned sb, bool ld, const char* g, unsigned gstep, unsigned voff, unsigned dst, F filler) {
  __builtin_amdgcn_s_setprio(PRIO_M);
  bf16x8 wq[PFD];
#pragma unroll
  for (int ks = 0; ks < PFD; ++ks) { const int o = ((ks >> 2) * 64 + (ks & 3) * 8) * RB; wq[ks] = trfrag(sb + o, sb + o + 4 * RB); }
#pragma unroll
  for (int i = 0; i < 16; ++i) acc[i] = 0.f;
  SB0();
#pragma unroll
  for (int ks = 0; ks < 32; ++ks) {
    acc = MFMA(wq[ks % PFD], xf[ks], acc);
    if (ks + PFD < 32) { const int o = (((ks + PFD) >> 2) * 64 + ((ks + PFD) & 3) * 8) * RB; wq[ks % PFD] = trfrag(sb + o, sb + o + 4 * RB); }
    if (ks >= L0 && ((ks - L0) % LS) == 0 && ks < L0 + 8 * LS) { if (ld) glds16s(voff, g + (size_t)((ks - L0) / LS) * gstep, dst + ((ks - L0) / LS) * 1024); }
    filler(ks);
    SB0();
  }
  __builtin_amdgcn_s_setprio(PRIO_E);
  asm volatile("" : "+v"(acc));
}
DI void feat_proj(const f32x16& acc, const u32x4& wf, int r32, unsigned (&pp)[8]) {
  float av[16];
#pragma unroll
  for (int i = 0; i < 16; ++i) av[i] = acc[i];
  const bf16x8 qb0 = pack8(av), qb1 = pack8(av + 8);
  const u32x4 z4 = {0u, 0u, 0u, 0u};
  const bf16x8 a0 = __builtin_bit_cast(bf16x8, (r32 < 16) ? wf : z4), a1 = __builtin_bit_cast(bf16x8, (r32 < 16) ? z4 : wf);
  f32x16 pq;
#pragma unroll
  for (int i = 0; i < 16; ++i) pq[i] = 0.f;
  SB0();
  __builtin_amdgcn_s_setprio(3);
  pq = MFMA(a0, qb0, pq);
  pq = MFMA(a1, qb1, pq);
  SB0();
  __builtin_amdgcn_s_setprio(PRIO_E);
#pragma unroll
  for (int j = 0; j < 8; ++j) pp[j] = cvtpk(pq[2 * j], pq[2 * j + 1]);
  asm volatile("" : "+v"(pp[0]), "+v"(pp[1]), "+v"(pp[2]), "+v"(pp[3]), "+v"(pp[4]), "+v"(pp[5]), "+v"(pp[6]), "+v"(pp[7]));
}
DI void feat_soft(const unsigned* pp, bf16x8& fp, bf16x8& fm) {
  float pv[8];
#pragma unroll
  for (int j = 0; j < 4; ++j) { pv[2 * j] = bflo(pp[j]); pv[2 * j + 1] = bfhi(pp[j]); }
  float mx = fmaxf(fmaxf(fmaxf(pv[0], pv[1]), pv[2]), fmaxf(fmaxf(pv[3], pv[4]), pv[5])), mn = fminf(fminf(fminf(pv[0], pv[1]), pv[2]), fminf(fminf(pv[3], pv[4]), pv[5]));
  mx = fmaxf(fmaxf(mx, pv[6]), pv[7]); mn = fminf(fminf(mn, pv[6]), pv[7]);
  mx = rmax32(mx); mn = rmin32(mn);
  const float L2E = 1.4426950408889634f, mxl = nv(-mx * L2E), mnl = nv(mn * L2E);
  float ep[8], em[8], sp = 0.f, sm = 0.f;
#pragma unroll
  for (int j = 0; j < 8; ++j) {
    ep[j] = __builtin_amdgcn_exp2f(nv(fmaf(pv[j], L2E, mxl))); sp = nv(sp + ep[j]);
    em[j] = __builtin_amdgcn_exp2f(nv(fmaf(pv[j], -L2E, mnl))); sm = nv(sm + em[j]);
  }
  sp = nv(rsum32(sp)); sm = nv(rsum32(sm));
  const float rp = __builtin_amdgcn_rcpf(sp), rm = __builtin_amdgcn_rcpf(sm);
#pragma unroll
  for (int j = 0; j < 8; ++j) { ep[j] = nv(ep[j] * rp); em[j] = nv(em[j] * rm); }
  fp = pack8(ep); fm = pack8(em);
  asm volatile("" : "+v"(fp), "+v"(fm));
}
#define WT_STORE 1
DI void st16(void* p, u32x4 v) {
  asm volatile("global_store_dwordx4 %0, %1, off sc1\n\ts_nop 1" :: "v"(p), "v"(v) : "memory");
}
DI void epi_g(const f32x16& acc, unsigned (&sg)[8]) {
#pragma unroll
  for (int i = 0; i < 16; i += 2) {
    const unsigned u = cvtpk(acc[i], acc[i + 1]);
    sg[i >> 1] = cvtpk(nv(silu_f(bflo(u))), nv(silu_f(bfhi(u))));
  }
  asm volatile("" : "+v"(sg[0]), "+v"(sg[1]), "+v"(sg[2]), "+v"(sg[3]), "+v"(sg[4]), "+v"(sg[5]), "+v"(sg[6]), "+v"(sg[7]));
}
DI void epi_u(const f32x16& acc, const unsigned (&sg)[8], char* dst) {
  float hv[16];
#pragma unroll
  for (int i = 0; i < 16; i += 2) { const unsigned u = cvtpk(acc[i], acc[i + 1]); hv[i] = nv(bflo(sg[i >> 1]) * bflo(u)); hv[i + 1] = nv(bfhi(sg[i >> 1]) * bfhi(u)); }
  st16(dst, __builtin_bit_cast(u32x4, pack8(hv))); st16(dst + 1024, __builtin_bit_cast(u32x4, pack8(hv + 8)));
}
DI void o_head(u64 (&g2)[8], const char* p2r, unsigned tag, const bf16x8& fp, const bf16x8& fm, char* dst) {
  for (unsigned spin = 0; spin < SPIN_MAX; ++spin) {
    bool ok = true;
#pragma unroll
    for (int j = 0; j < 8; ++j) ok &= (unsigned)(g2[j] >> 32) == tag;
    if (__all(ok)) break;
    __builtin_amdgcn_s_sleep(4);
#pragma unroll
    for (int j = 0; j < 8; ++j) g2[j] = ld_gran(p2r + j * 512);
  }
  __builtin_amdgcn_s_setprio(3);
  const u32x4 f0 = {(unsigned)g2[0], (unsigned)g2[1], (unsigned)g2[2], (unsigned)g2[3]};
  const u32x4 f1 = {(unsigned)g2[4], (unsigned)g2[5], (unsigned)g2[6], (unsigned)g2[7]};
  f32x16 o;
#pragma unroll
  for (int q = 0; q < 16; ++q) o[q] = 0.f;
  o = MFMA(__builtin_bit_cast(bf16x8, f0), fp, o);
  o = MFMA(__builtin_bit_cast(bf16x8, f1), fm, o);
  const float rd = __builtin_amdgcn_rcpf(bf16r(bf16r(o[8]) + 1e-6f));
  float ov[8];
#pragma unroll
  for (int j = 0; j < 8; j += 2) { const unsigned u = cvtpk(o[j], o[j + 1]); ov[j] = nv(bflo(u) * rd); ov[j + 1] = nv(bfhi(u) * rd); }
  __builtin_amdgcn_s_setprio(PRIO_E);
  st16(dst, __builtin_bit_cast(u32x4, pack8(ov)));
}
#define PHASE_END(p) do { STAMP(2 + 2 * (p)); SB0(); BAR(); SB0(); STAMP(3 + 2 * (p)); } while (0)

__global__ __launch_bounds__(512, 2) void k1_kernel(const u16* __restrict__ x, const u16* __restrict__ wqkv, const u16* __restrict__ wphi,
                                                    const u16* __restrict__ wo, const u16* __restrict__ w1, const u16* __restrict__ w3,
                                                    const u16* __restrict__ w2, char* __restrict__ ws) {
  __shared__ __attribute__((aligned(16))) char smem[K1_LDS];
  const int tid = threadIdx.x, lane = tid & 63;
  const int wave = __builtin_amdgcn_readfirstlane(tid >> 6), grp = wave >> 2, wr = wave & 3;
  const int b = blockIdx.x, xcd = b & 7, idx = b >> 3;
  const int mt = (xcd & 1) * 8 + (idx & 7), ng = (xcd >> 1) * 4 + (idx >> 3);
  const int r32 = lane & 31, h = lane >> 5;
  const unsigned lds0 = (unsigned)(uintptr_t)smem;
  unsigned dummy0 = 0;
  if (wave == 0) {
    unsigned long long pc; unsigned len;
    asm volatile("s_getpc_b64 %0\n.Lk1_pc:\n\ts_mov_b32 %1, .Lk2_end-.Lk1_pc" : "=s"(pc), "=s"(len));
    const unsigned off = (lane * 8 + (idx & 7)) * 128;
    if (off < len) asm volatile("global_load_dword %0, %1, %2" : "+v"(dummy0) : "v"(off), "s"(pc) : "memory");
  }
  unsigned dummy1 = 0;
#define XTOUCH 0
  if (XTOUCH && grp == 0) asm volatile("global_load_dword %0, %1, %2" : "+v"(dummy1) : "v"(lane * 128), "s"((const char*)(x + (size_t)(mt * 128 + (idx >> 3) * 32 + wr * 8) * D_)) : "memory");
  const unsigned reg = lds0 + SLOT + (wr * 2 + grp) * 16384;
  {
    const char* xb = (const char*)(x + (size_t)(mt * 128 + wr * 32 + grp * 16) * D_);
    const int rr = lane >> 5, qd = (lane >> 3) & 3, pos = lane & 7;
#pragma unroll
    for (int cc = 0; cc < 2; ++cc)
#pragma unroll
      for (int i = 0; i < 8; ++i)
        glds16s(rr * 1024 + qd * 256 + (pos << 4), xb + i * 2048 + cc * 128, reg + cc * 8192 + i * 1024);
  }
  const int g0 = ng < 11 ? 3 * ng : 33 + 2 * (ng - 11);
  const bool full = ng < 11;
  const char* wK = (const char*)(wqkv + 512 + 32 * ng);
  const char* wG = (const char*)(w1 + 32 * g0);
  const char* wU = (const char*)(w3 + 32 * g0);
  const unsigned voffA = (lane >> 2) * 3072 + (lane & 3) * 16, voffF = (lane >> 2) * 2752 + (lane & 3) * 16;
  unsigned dummy = 0, dummy2 = 0, tagv;
  u32x4 wf, wfb;
  {
    tagv = __hip_atomic_load((GAS unsigned*)(ws + WS_EP + ng * 128), __ATOMIC_RELAXED, __HIP_MEMORY_SCOPE_AGENT);
    const u16* wp = wphi + (size_t)(2 * ng + (r32 >> 4)) * 256 + (4 * h) * 16 + (lane & 15);
#pragma unroll
    for (int j = 0; j < 4; ++j) {
      const int k0 = 8 * (j >> 1) + 2 * (j & 1);
      asm volatile("global_load_ushort %0, %2, off offset:%3\n\tglobal_load_ushort %1, %2, off offset:%4" : "=&v"(wf[j]), "=&v"(wfb[j]) : "v"(wp), "n"(k0 * 32), "n"(k0 * 32 + 32) : "memory");
    }
#define TLBW 1
    if (TLBW && !grp) {
      const size_t ab = WS_AB + (size_t)(mt * 4 + wr) * KS_W * 1024;
      const size_t o = lane == 1 ? ab + (size_t)(32 + 2 * g0) * 1024 : lane == 2 ? WS_P1 + ((size_t)(ng * 16 + wr * 4) * 16 + mt) * 1024
                     : lane == 3 ? WS_P1 + (size_t)(ng * 16 + mt) * 16384 : lane == 4 ? WS_P2 + (size_t)(ng * 16 + mt) * 512 : ab + (size_t)(2 * ng) * 1024;
      asm volatile("global_load_dword %0, %1, off sc1" : "+v"(dummy) : "v"(ws + o) : "memory");
    }
    if (grp) {
      const char* g = wK + (size_t)wr * 128 * 3072;
#pragma unroll
      for (int i = 0; i < 8; ++i) glds16s(voffA, g + i * 16 * 3072, lds0 + wr * 8192 + i * 1024);
      const int trow = (idx & 7) * 64 + lane;
      const int e = (wr == 3 && !full) ? 0 : wr - 1;
      const char* p = wr == 0 ? wK + 1024 + (size_t)trow * 3072 : wG + 64 * e + (size_t)trow * 2752;
      const char* p2 = wr == 0 ? wK - 1024 + (size_t)trow * 3072 : wU + 64 * e + (size_t)trow * 2752;
      asm volatile("global_load_dword %0, %1, off" : "+v"(dummy) : "v"(p) : "memory");
      asm volatile("global_load_dword %0, %1, off" : "+v"(dummy2) : "v"(p2) : "memory");
    }
  }
  bf16x8 xf[32];
  unsigned tag;
  {
    const int r16 = lane & 15, qd = lane >> 4;
    const unsigned rb = reg + (r16 >> 1) * 1024 + (r16 & 1) * 512 + qd * 128 + ((r16 & 7) << 4);
    u32x4 xr[16];
    float q0 = 0.f, q1 = 0.f;
    f32x4 sum4 = {0.f, 0.f, 0.f, 0.f};
    const u32x4 ones = {0x3f803f80u, 0x3f803f80u, 0x3f803f80u, 0x3f803f80u};
    if (grp) WAITV(27); else if (TLBW) WAITV(18); else WAITV(17);
#pragma unroll
    for (int q = 0; q < 8; ++q) xr[q] = *(const LAS u32x4*)(uintptr_t)(rb ^ (q << 4));
#pragma unroll
    for (int q = 0; q < 8; ++q)
#pragma unroll
      for (int e = 0; e < 4; e += 2) {
        if (e == 0) sum4 = MFMA16(ones, xr[q], sum4);
        q0 = dot2(xr[q][e], xr[q][e], q0); q1 = dot2(xr[q][e + 1], xr[q][e + 1], q1);
      }
    asm volatile("" : "+v"(sum4), "+v"(q0), "+v"(q1));
    if (grp) WAITV(19); else if (TLBW) WAITV(10); else WAITV(9);
#pragma unroll
    for (int q = 0; q < 8; ++q) xr[8 + q] = *(const LAS u32x4*)(uintptr_t)((rb ^ (q << 4)) + 8192);
#pragma unroll
    for (int q = 8; q < 16; ++q)
#pragma unroll
      for (int e = 0; e < 4; e += 2) {
        if (e == 0) sum4 = MFMA16(ones, xr[q], sum4);
        q0 = dot2(xr[q][e], xr[q][e], q0); q1 = dot2(xr[q][e + 1], xr[q][e + 1], q1);
      }
    const float mean = sum4[0] * (1.0f / 512.0f);
    const float var = fmaxf(rsum32(rsum16(q0 + q1)) * (1.0f / 512.0f) - mean * mean, 0.f);
    const float nmb = -bf16r(mean);
    const float rs = 1.0f / bf16r(sqrtf(bf16r(bf16r(var) + 1e-5f)));
    const unsigned wb = reg + qd * 4096 + r16 * 16 + (r16 & 3) * 512 + ((r16 >> 2) & 1) * 256;
#pragma unroll
    for (int q = 0; q < 16; ++q) {
      u32x4 o;
      center4(xr[q], nmb);
#pragma unroll
      for (int e = 0; e < 4; ++e) o[e] = cvtpk(bflo(xr[q][e]) * rs, bfhi(xr[q][e]) * rs);
      *(LAS u32x4*)(uintptr_t)((wb ^ ((q & 3) * 512 + ((q >> 2) & 1) * 256)) + (q >> 3) * 2048) = o;
    }
    SB0();
    WAITV(0);
    asm volatile("" : "+v"(dummy), "+v"(dummy0), "+v"(dummy1), "+v"(dummy2), "+v"(wf), "+v"(wfb));
#pragma unroll
    for (int j = 0; j < 4; ++j) wf[j] |= wfb[j] << 16;
    tag = __builtin_amdgcn_readfirstlane(tagv) + 1u;
    asm volatile("" : "+s"(tag));
    BAR();
    const unsigned xb = lds0 + SLOT * (1 + wr) + (r32 >> 4) * 16384 + (h * 16 + (r32 & 15)) * 16;
#pragma unroll
    for (int ks = 0; ks < 32; ++ks) xf[ks] = *(const LAS bf16x8*)(uintptr_t)(xb + ks * 512);
    BAR();
  }
  STAMP(1);
  const unsigned voffP = (lane >> 3) * 2752 + (((lane & 7) ^ ((lane >> 2) & 4)) << 4);
  const size_t rowP = (size_t)wave * 64 * 2752;
  const int i16 = lane & 15, blk = (lane >> 4) & 1;
  const unsigned tr0 = lds0 + (32 * h + (i16 >> 2)) * 64 + 8 * (4 * blk + (i16 & 3));
  const unsigned trP = lds0 + (32 * h + (i16 >> 2)) * 128 + (((grp ^ (i16 >> 3)) & 1) << 6) + 8 * (4 * blk + (i16 & 3));
  const int mblk = mt * 4 + wr;
  const unsigned ldd = lds0 + wr * 8192;
  const size_t rowF = (size_t)wr * 128 * 2752, rowA = (size_t)wr * 128 * 3072;
  char* const hdst = ws + WS_AB + ((size_t)mblk * KS_W + 32 + 2 * g0) * 1024 + lane * 16;
  unsigned sg[8], pp[8];
  f32x16 acc, acc2;
  const int row = wr * 32 + r32, sw = (row >> 1) & 7;
  auto nofill = [](int) {};
  const unsigned ppx = lds0 + LDS_SCR + 24576 + wr * 1024 + lane * 16;
  const char* p2r = ws + WS_P2 + (size_t)(ng * 16 + (grp ? 0 : 8)) * 512 + lane * 8;
  char* const odst = ws + WS_AB + ((size_t)mblk * KS_W + 2 * ng + (grp ? 0 : 1)) * 1024 + lane * 16;
  u64 g2[8];
  bf16x8 fq, fn;
  if (grp == 0) {
#define QL0 16
    mfma_tile<64, QL0, 2>(acc, xf, tr0, true, wK - 1024 + rowA, 16 * 3072, voffA, ldd + 2 * SLOT, nofill);
    PHASE_END(0);
    feat_proj(acc, wf, r32, pp);
#pragma unroll
    for (int s = 0; s < 2; ++s) {
      if (s) {
#pragma unroll
        for (int i = 0; i < 4; ++i) glds16s(voffP, wG + rowP + i * 8 * 2752, lds0 + wave * 8192 + i * 1024);
      }
      bf16x8 fp, fm;
      feat_soft(pp + 4 * s, fp, fm);
      if (s) {
#pragma unroll
        for (int i = 4; i < 8; ++i) glds16s(voffP, wG + rowP + i * 8 * 2752, lds0 + wave * 8192 + i * 1024);
      }
      const unsigned pk = lds0 + LDS_SCR + s * 8192 + row * 64;
      const u32x4 up = __builtin_bit_cast(u32x4, fp), um = __builtin_bit_cast(u32x4, fm);
      u32x2 t;
      t[0] = up[0]; t[1] = up[1]; *(LAS u32x2*)(uintptr_t)(pk + 8 * ((h) ^ sw)) = t;
      t[0] = up[2]; t[1] = up[3]; *(LAS u32x2*)(uintptr_t)(pk + 8 * ((2 + h) ^ sw)) = t;
      t[0] = um[0]; t[1] = um[1]; *(LAS u32x2*)(uintptr_t)(pk + 8 * ((4 + h) ^ sw)) = t;
      t[0] = um[2]; t[1] = um[3]; *(LAS u32x2*)(uintptr_t)(pk + 8 * ((6 + h) ^ sw)) = t;
      if (s == 0) { SB0(); WAITV(0); }
      PHASE_END(1 + s);
    }
#define TOUCHW 1
    unsigned d0 = 0, d1 = 0;
    const int tr = idx * 59 + (lane < 59 ? lane : 58);
    const char* tp = (tr < 512 ? (const char*)wo + (size_t)tr * 1024 : (const char*)w2 + (size_t)(tr - 512) * 1024) + wr * 256;
    if (TOUCHW == 1) asm volatile("global_load_dword %0, %1, off" : "+v"(d0) : "v"(tp) : "memory");
    if (full) mfma_tile<64>(acc2, xf, tr0 + 3 * SLOT, false, nullptr, 0, 0, 0, nofill);
    if (TOUCHW == 1) WAITV(1); else WAITV(0);
    PHASE_END(3);
    mfma_tile<128>(acc, xf, trP, true, wU + rowP, 8 * 2752, voffP, lds0 + 2 * SLOT + wave * 8192, nofill);
    PHASE_END(4);
    if (TOUCHW == 2) asm volatile("global_load_dword %0, %1, off" : "+v"(d0) : "v"(tp) : "memory");
    if (TOUCHW) asm volatile("global_load_dword %0, %1, off offset:128" : "+v"(d1) : "v"(tp) : "memory");
    epi_g(acc, sg);
    { const u32x4 t = *(const LAS u32x4*)(uintptr_t)ppx; pp[0] = t[0]; pp[1] = t[1]; pp[2] = t[2]; pp[3] = t[3]; }
    feat_soft(pp, fq, fn);
    if (TOUCHW == 2) WAITV(2); else if (TOUCHW == 1) WAITV(1); else WAITV(0);
    PHASE_END(5);
    mfma_tile<128>(acc, xf, trP + 2 * SLOT, full, wU + 128 + rowF, 16 * 2752, voffF, ldd, nofill);
    PHASE_END(6);
    epi_u(acc, sg, hdst);
    if (full) epi_g(acc2, sg);
    WAITV(2);
    asm volatile("" :: "v"(d0), "v"(d1));
    PHASE_END(7);
    if (full) {
      mfma_tile<64>(acc, xf, tr0, false, nullptr, 0, 0, 0, [&](int ks) { if (ks >= 16 && ks < 24) g2[ks - 16] = ld_gran(p2r + (ks - 16) * 512); });
    } else {
#pragma unroll
      for (int j = 0; j < 8; ++j) g2[j] = ld_gran(p2r + j * 512);
    }
    STAMP(18);
    o_head(g2, p2r, tag, fq, fn, odst);
    STAMP(19);
    if (full) epi_u(acc, sg, hdst + 4096);
    STAMP(20);
  } else {
#pragma unroll
    for (int i = 0; i < 8; ++i) glds16s(voffA, wK + 1024 + rowA + i * 16 * 3072, ldd + SLOT + i * 1024);
    WAITV(0);
    PHASE_END(0);
    mfma_tile<64>(acc2, xf, tr0 + SLOT, full, wG + 128 + rowF, 16 * 2752, voffF, ldd + 3 * SLOT, nofill);
    PHASE_END(1);
    {
      const unsigned vi = lds0 + LDS_SCR + 16384 + row * 64;
      mfma_tile<64>(acc, xf, tr0 + 2 * SLOT, true, wG + rowP, 8 * 2752, voffP, lds0 + wave * 8192, [&](int ks) {
        if (ks >= 1 && ks <= 4) {
          const int a = ks - 1;
          u32x2 t; t[0] = cvtpk(acc2[4 * a], acc2[4 * a + 1]); t[1] = cvtpk(acc2[4 * a + 2], acc2[4 * a + 3]);
          *(LAS u32x2*)(uintptr_t)(vi + 8 * ((2 * a + h) ^ sw)) = t;
        }
      });
    }
    WAITV(8);
    PHASE_END(2);
    if (wr < 2) {
      f32x16 a2;
#pragma unroll
      for (int i = 0; i < 16; ++i) a2[i] = 0.f;
      const int rowq = 8 * h + (i16 >> 2);
      const int ca = 4 * blk + (lane & 3), cb = 4 * (blk ^ wr) + (lane & 3);
      const int sw0 = (rowq >> 1) & 7, sw1 = ((rowq + 4) >> 1) & 7;
      const unsigned pk = lds0 + LDS_SCR + wr * 8192 + rowq * 64, vi = lds0 + LDS_SCR + 16384 + rowq * 64;
      const unsigned pa0 = pk + 8 * (ca ^ sw0), pa1 = pk + 256 + 8 * (ca ^ sw1), pb0 = vi + 8 * (cb ^ sw0), pb1 = vi + 256 + 8 * (cb ^ sw1);
      __builtin_amdgcn_s_setprio(3);
#pragma unroll
      for (int ks = 0; ks < 8; ++ks) {
        bf16x8 af = trfrag(pa0 + ks * 1024, pa1 + ks * 1024);
        bf16x8 bfr = trfrag(pb0 + ks * 1024, pb1 + ks * 1024);
        if (blk) {
#pragma unroll
          for (int j = 0; j < 8; ++j) bfr[j] = (short)0x3f80;
        }
        a2 = MFMA(af, bfr, a2);
      }
      asm volatile("" : "+v"(a2));
      __builtin_amdgcn_s_setprio(PRIO_E);
      char* p1w = ws + WS_P1 + (((size_t)(ng * 16 + wr * 8) * 16 + mt) * 1024) + lane * 8;
#pragma unroll
      for (int i = 0; i < 16; ++i) st_gran(p1w + (size_t)(i >> 1) * 16384 + (i & 1) * 512, __float_as_uint(a2[i]), tag);
    }
    feat_proj(acc, wf, r32, pp);
    { const u32x4 t = {pp[4], pp[5], pp[6], pp[7]}; *(LAS u32x4*)(uintptr_t)ppx = t; }
    if (wr >= 2) feat_soft(pp, fq, fn);
    if (wr < 2) WAITV(16); else WAITV(0);
    PHASE_END(3);
#pragma unroll
    for (int i = 0; i < 4; ++i) glds16s(voffP, wU + rowP + i * 8 * 2752, lds0 + 2 * SLOT + wave * 8192 + i * 1024);
    if (wr < 2) feat_soft(pp, fq, fn);
#pragma unroll
    for (int i = 4; i < 8; ++i) glds16s(voffP, wU + rowP + i * 8 * 2752, lds0 + 2 * SLOT + wave * 8192 + i * 1024);
    PHASE_END(4);
    u64 g1[8];
    const char* p1r = ws + WS_P1 + ((size_t)(ng * 16 + mt) * 16 + h * 8) * 1024 + blk * 512 + (wr * 16 + i16) * 8;
    mfma_tile<128>(acc, xf, trP, false, nullptr, 0, 0, 0, [&](int ks) { if (ks >= 16 && ks < 24) g1[ks - 16] = ld_gran(p1r + (ks - 16) * 1024); });
    WAITV(8);
    PHASE_END(5);
    {
      for (unsigned spin = 0; spin < SPIN_MAX; ++spin) {
        bool ok = true;
#pragma unroll
        for (int j = 0; j < 8; ++j) ok &= (unsigned)(g1[j] >> 32) == tag;
        if (__all(ok)) break;
        __builtin_amdgcn_s_sleep(4);
#pragma unroll
        for (int j = 0; j < 8; ++j) g1[j] = ld_gran(p1r + j * 1024);
      }
      float a = 0.f;
#pragma unroll
      for (int j = 0; j < 8; ++j) a = nv(a + __uint_as_float((unsigned)g1[j]));
      a = rsum32(a);
      auto rr = __builtin_amdgcn_permlane16_swap(__float_as_uint(a), __float_as_uint(a), false, false);
      if (lane < 16) st_gran(ws + WS_P2 + (size_t)(ng * 16 + mt) * 512 + (wr * 16 + lane) * 8, cvtpk(__uint_as_float(rr[0]), __uint_as_float(rr[1])), tag);
      if (mt == 0 && wr == 0 && lane == 0) __hip_atomic_store((GAS unsigned*)(ws + WS_EP + ng * 128), tag, __ATOMIC_RELAXED, __HIP_MEMORY_SCOPE_AGENT);
      SB0();
      epi_g(acc, sg);
    }
    PHASE_END(6);
    mfma_tile<128>(acc, xf, trP + 2 * SLOT, false, nullptr, 0, 0, 0, [&](int ks) { if (ks >= 20 && ks < 28) g2[ks - 20] = ld_gran(p2r + (ks - 20) * 512); });
    PHASE_END(7);
    o_head(g2, p2r, tag, fq, fn, odst);
    STAMP(19);
    epi_u(acc, sg, hdst + 2048);
    STAMP(20);
  }
  STAMPR(31);
  STAMP_FLUSH(0);
  asm volatile(".Lk1_end:");
}

constexpr int XROW = 68;
constexpr int P2 = 10;
template <int N> DI void waitv() { asm volatile("s_waitcnt vmcnt(%0)" :: "n"(N) : "memory"); }
__global__ __launch_bounds__(256, 1) void k2_kernel(const u16* __restrict__ x, const u16* __restrict__ wo, const u16* __restrict__ w2, const char* __restrict__ ws, u16* __restrict__ out) {
  __shared__ __attribute__((aligned(16))) char smem[4 * P2 * 4096];
  float* xch = (float*)smem;
  const int tid = threadIdx.x, lane = tid & 63;
  const int wave = __builtin_amdgcn_readfirstlane(tid >> 6);
  const int b = blockIdx.x, xcd = b & 7, idx = b >> 3;
  const int mt = (xcd & 1) * 16 + (xcd >> 1) * 4 + (idx & 3), nt = idx >> 2;
  const int r32 = lane & 31, h = lane >> 5;
  const int erow = mt * 64 + (tid >> 2), ecol = nt * 64 + (tid & 3) * 16;
  const u32x4 xa = *(const u32x4*)(x + (size_t)erow * D_ + ecol), xb = *(const u32x4*)(x + (size_t)erow * D_ + ecol + 8);
  unsigned dummy = 0;
  f32x16 acc[2][2];
#pragma unroll
  for (int n = 0; n < 2; ++n)
#pragma unroll
    for (int m = 0; m < 2; ++m)
#pragma unroll
      for (int i = 0; i < 16; ++i) acc[n][m][i] = 0.f;
  const int k0 = wave == 0 ? 0 : 32 + (wave - 1) * 29;
  const int nk = wave == 0 ? 32 : (wave == 3 ? 28 : 29);
  const char* wsrc = (wave == 0 ? (const char*)wo : (const char*)w2 + (size_t)(k0 - 32) * 16384) + nt * 128;
  const char* ha = ws + WS_AB + ((size_t)(2 * mt) * KS_W + k0) * 1024;
  const char* hb = ha + (size_t)KS_W * 1024;
  constexpr int RS = P2 * 4096;
  const unsigned ring = (unsigned)(uintptr_t)smem + wave * RS;
  const unsigned lo = lane * 16, voffW = (lane >> 3) * 1024 + (((lane & 7) ^ ((lane >> 2) & 4)) << 4);
  const int i16 = lane & 15, blk = (lane >> 4) & 1;
  const unsigned tb0 = ring + 2048 + (4 * h + (i16 >> 2)) * 128 + (((i16 >> 3) & 1) << 6) + 8 * (4 * blk + (i16 & 3)), tb1 = tb0 ^ 64;
  auto issue = [&](int i) {
    const unsigned d = ring + (i % P2) * 4096;
    glds16s(lo, ha + i * 1024, d); glds16s(lo, hb + i * 1024, d + 1024);
    glds16s(voffW, wsrc + i * 16384, d + 2048); glds16s(voffW, wsrc + i * 16384 + 8192, d + 3072);
  };
  bf16x8 R[2][4];
  auto rd = [&](int i, bf16x8* d) {
    const int o = (i % P2) * 4096;
    d[0] = *(const LAS bf16x8*)(uintptr_t)(ring + lo + o); d[1] = *(const LAS bf16x8*)(uintptr_t)(ring + lo + o + 1024);
    d[2] = trfrag(tb0 + o, tb0 + o + 1024); d[3] = trfrag(tb1 + o, tb1 + o + 1024);
  };
#pragma unroll
  for (int i = 0; i < P2; ++i) issue(i);
  waitv<4 * (P2 - 1)>();
  if (TLBW) asm volatile("global_load_dword %0, %1, off" : "+v"(dummy) : "v"(out + (size_t)erow * D_ + ecol) : "memory");
  rd(0, R[0]);
#pragma unroll
  for (int i = 0; i < 32; ++i) {
    if (i < 28 || i < nk) {
      if (i + 1 < 28 || i + 1 < nk) {
        if (i + 1 < 32) {
          constexpr int full_w = 4 * (P2 - 2);
          if (4 * (26 - i) >= full_w) waitv<full_w>();
          else if (i == 19) waitv<28>(); else if (i == 20) waitv<24>(); else if (i == 21) waitv<20>(); else if (i == 22) waitv<16>();
          else if (i == 23) waitv<12>(); else if (i == 24) waitv<8>(); else if (i == 25) waitv<4>(); else waitv<0>();
          rd(i + 1, R[(i + 1) & 1]);
        }
      }
      SB0();
      const bf16x8* d = R[i & 1];
      acc[0][0] = MFMA(d[2], d[0], acc[0][0]); acc[0][1] = MFMA(d[2], d[1], acc[0][1]);
      acc[1][0] = MFMA(d[3], d[0], acc[1][0]); acc[1][1] = MFMA(d[3], d[1], acc[1][1]);
      SB0();
      if (i + P2 < 28) issue(i + P2);
      else if (i + P2 < 32) { if (i + P2 < nk) issue(i + P2); }
      SB0();
    }
  }
  waitv<0>();
  asm volatile("" :: "v"(dummy));
  __syncthreads();
  STAMP(1);
  float* mine = xch + wave * 64 * XROW;
#pragma unroll
  for (int n = 0; n < 2; ++n)
#pragma unroll
    for (int m = 0; m < 2; ++m)
#pragma unroll
      for (int a = 0; a < 4; ++a) {
        f32x4 t = {acc[n][m][4 * a], acc[n][m][4 * a + 1], acc[n][m][4 * a + 2], acc[n][m][4 * a + 3]};
        *(f32x4*)(mine + (32 * m + r32) * XROW + 32 * n + 8 * a + 4 * h) = t;
      }
  __syncthreads();
  STAMP(2);
  const float* rp = xch + (tid >> 2) * XROW + (tid & 3) * 16;
  u32x4 res[2];
#pragma unroll
  for (int q = 0; q < 4; ++q) {
    const f32x4 s0 = *(const f32x4*)(rp + 4 * q), o0 = *(const f32x4*)(rp + 64 * XROW + 4 * q);
    const f32x4 o1 = *(const f32x4*)(rp + 128 * XROW + 4 * q), o2 = *(const f32x4*)(rp + 192 * XROW + 4 * q);
#pragma unroll
    for (int e = 0; e < 4; e += 2) {
      const unsigned su = cvtpk(s0[e], s0[e + 1]);
      const unsigned ou = cvtpk((o0[e] + o1[e]) + o2[e], (o0[e + 1] + o1[e + 1]) + o2[e + 1]);
      const unsigned gu = cvtpk(silu_f(bflo(ou)), silu_f(bfhi(ou)));
      const unsigned tu = cvtpk(bflo(su) * bflo(gu), bfhi(su) * bfhi(gu));
      const unsigned xw = (q < 2 ? xa : xb)[(q & 1) * 2 + (e >> 1)];
      res[q >> 1][(q & 1) * 2 + (e >> 1)] = cvtpk(bflo(xw) + bflo(tu), bfhi(xw) + bfhi(tu));
    }
  }
  *(u32x4*)(out + (size_t)erow * D_ + ecol) = res[0];
  *(u32x4*)(out + (size_t)erow * D_ + ecol + 8) = res[1];
  STAMP(3); STAMPR(31);
  asm volatile(".Lk2_end:");
}

extern "C" void kernel_launch(void* const* d_in, const int* in_sizes, int n_in, void* d_out, int out_size, void* d_ws, size_t ws_size,
                              hipStream_t stream) {
  const u16* x = (const u16*)d_in[0];
  const u16* wqkv = (const u16*)d_in[1];
  const u16* wphi = (const u16*)d_in[2];
  const u16* wo = (const u16*)d_in[3];
  const u16* w1 = (const u16*)d_in[5];
  const u16* w3 = (const u16*)d_in[6];
  const u16* w2 = (const u16*)d_in[7];
  char* ws = (char*)d_ws;
  k1_kernel<<<256, 512, 0, stream>>>(x, wqkv, wphi, wo, w1, w3, w2, ws);
  k2_kernel<<<256, 256, 0, stream>>>(x, wo, w2, ws, (u16*)d_out);
}
```

```cpp
#include <hip/hip_runtime.h>
#include <hip/hip_bf16.h>
#include <stdint.h>

typedef __attribute__((ext_vector_type(8))) short bf16x8;
typedef __attribute__((ext_vector_type(4))) short s16x4;
typedef __attribute__((ext_vector_type(16))) float f32x16;
typedef __attribute__((ext_vector_type(4))) float f32x4;
typedef __attribute__((ext_vector_type(4))) unsigned u32x4;
typedef __attribute__((ext_vector_type(2))) unsigned u32x2;
typedef unsigned short u16;
#define LAS __attribute__((address_space(3)))
#define DI __device__ __forceinline__
#define MFMA(a, b, c) __builtin_amdgcn_mfma_f32_32x32x16_bf16((a), (b), (c), 0, 0, 0)
#define MFMA16(a, b, c) __builtin_amdgcn_mfma_f32_16x16x32_bf16(__builtin_bit_cast(bf16x8, (a)), __builtin_bit_cast(bf16x8, (b)), (c), 0, 0, 0)

constexpr int S_ = 2048, D_ = 512, NH = 32, F_ = 1376;
constexpr int KS_F = F_ / 16;
constexpr int KS_W = 32 + KS_F;
constexpr size_t WS_AB = 0;
constexpr size_t WS_WT = WS_AB + (size_t)64 * KS_W * 1024;
constexpr size_t WS_P1 = WS_WT + (size_t)16 * KS_W * 1024;
constexpr size_t WS_P2 = WS_P1 + (size_t)16 * 16 * 16 * 1024;
constexpr size_t WS_EP = WS_P2 + (size_t)16 * 16 * 512;
constexpr size_t WS_DUMP = WS_EP + 16 * 128;
constexpr size_t WS_END = WS_DUMP + 2048;

typedef __attribute__((ext_vector_type(2))) __bf16 bf16x2_t;
typedef __attribute__((ext_vector_type(2))) float f32x2;
DI unsigned cvtpk(float lo, float hi) { f32x2 v = {lo, hi}; return __builtin_bit_cast(unsigned, __builtin_convertvector(v, bf16x2_t)); }
DI float bflo(unsigned u) { return __uint_as_float(u << 16); }
DI float bfhi(unsigned u) { return __uint_as_float(u & 0xffff0000u); }
DI float bf16r(float x) { return bflo(cvtpk(x, x)); }
DI void glds16(const void* g, unsigned lds_base) {
  unsigned sv;
  asm volatile("s_mov_b32 %0, m0\n\ts_mov_b32 m0, %2\n\ts_nop 0\n\tglobal_load_lds_dwordx4 %1, off\n\ts_mov_b32 m0, %0"
               : "=&s"(sv) : "v"(g), "s"(lds_base) : "memory");
}
typedef short v4i16_t __attribute__((ext_vector_type(4)));
DI s16x4 trrd(unsigned a) { return __builtin_bit_cast(s16x4, __builtin_amdgcn_ds_read_tr16_b64_v4i16((LAS v4i16_t*)(uintptr_t)a)); }
DI bf16x8 trfrag(unsigned lo, unsigned hi) { s16x4 a = trrd(lo), b = trrd(hi); return __builtin_shufflevector(a, b, 0, 1, 2, 3, 4, 5, 6, 7); }
DI bf16x8 pack8(const float* v) {
  u32x4 p; p[0] = cvtpk(v[0], v[1]); p[1] = cvtpk(v[2], v[3]); p[2] = cvtpk(v[4], v[5]); p[3] = cvtpk(v[6], v[7]);
  return __builtin_bit_cast(bf16x8, p);
}
DI float xhalf(float v) {
  auto rr = __builtin_amdgcn_permlane32_swap(__float_as_uint(v), __float_as_uint(v), false, false);
  unsigned a = rr[0], b = rr[1];
  return __uint_as_float((threadIdx.x & 32) ? a : b);
}
template <int C> DI float dppror(float v) { return __uint_as_float(__builtin_amdgcn_update_dpp(0u, __float_as_uint(v), 0x120 + C, 0xf, 0xf, false)); }
DI float row16sum(float v) { v += dppror<8>(v); v += dppror<4>(v); v += dppror<2>(v); v += dppror<1>(v); return v; }
DI float silu_f(float x) { return x * __builtin_amdgcn_rcpf(1.0f + __builtin_amdgcn_exp2f(-1.4426950408889634f * x)); }
#define STAMP(i)
#define STAMPR(i)
#define STAMP_FLUSH(base)
#define BAR() asm volatile("s_waitcnt lgkmcnt(0)\n\ts_barrier" ::: "memory")
#define WAITV0() asm volatile("s_waitcnt vmcnt(0)" ::: "memory")

enum { T_NONE = 0, T_Q = 1, T_K = 2, T_V = 3, T_G = 4, T_U = 5 };
#define TD(ty, ix) ((unsigned long long)(((ty) << 6) | (ix)))
#define SB0() __builtin_amdgcn_sched_barrier(0)
#define GAS __attribute__((address_space(1)))
typedef unsigned long long u64;
DI float dot2(unsigned a, unsigned b, float c) {
  return __builtin_amdgcn_fdot2_f32_bf16(__builtin_bit_cast(bf16x2_t, a), __builtin_bit_cast(bf16x2_t, b), c, false);
}
DI void st_gran(void* p, unsigned val, unsigned tag) { __hip_atomic_store((GAS u64*)p, ((u64)tag << 32) | val, __ATOMIC_RELAXED, __HIP_MEMORY_SCOPE_AGENT); }
DI u64 ld_gran(const void* p) { return __hip_atomic_load((GAS u64*)p, __ATOMIC_RELAXED, __HIP_MEMORY_SCOPE_AGENT); }
constexpr unsigned SPIN_MAX = 1u << 16;

constexpr int SLOT = 32768;
constexpr int LDS_SCR = 4 * SLOT;
constexpr int LDS_DBG = LDS_SCR + 28672;
constexpr int K1_LDS = 5 * SLOT;

DI void glds16s(unsigned voff, const void* sbase, unsigned lds_base) {
  unsigned sv;
  asm volatile("s_mov_b32 %0, m0\n\ts_mov_b32 m0, %3\n\ts_nop 0\n\tglobal_load_lds_dwordx4 %1, %2\n\ts_mov_b32 m0, %0"
               : "=&s"(sv) : "v"(voff), "s"(sbase), "s"(lds_base) : "memory");
}
DI void center4(u32x4& a, float c) {
  unsigned t0, t1, t2, t3, t4, t5, t6, t7;
  asm("v_dot2_f32_bf16 %0, %8, %12, %14\n\tv_dot2_f32_bf16 %1, %8, %13, %14\n\tv_dot2_f32_bf16 %2, %9, %12, %14\n\tv_dot2_f32_bf16 %3, %9, %13, %14\n\t"
      "v_dot2_f32_bf16 %4, %10, %12, %14\n\tv_dot2_f32_bf16 %5, %10, %13, %14\n\tv_dot2_f32_bf16 %6, %11, %12, %14\n\tv_dot2_f32_bf16 %7, %11, %13, %14\n\t"
      "v_cvt_pk_bf16_f32 %0, %0, %1\n\tv_cvt_pk_bf16_f32 %2, %2, %3\n\tv_cvt_pk_bf16_f32 %4, %4, %5\n\tv_cvt_pk_bf16_f32 %6, %6, %7"
      : "=&v"(t0), "=&v"(t1), "=&v"(t2), "=&v"(t3), "=&v"(t4), "=&v"(t5), "=&v"(t6), "=&v"(t7)
      : "v"(a[0]), "v"(a[1]), "v"(a[2]), "v"(a[3]), "s"(0x00003f80u), "s"(0x3f800000u), "v"(c));
  a[0] = t0; a[1] = t2; a[2] = t4; a[3] = t6;
}
DI float rsum32(float v) { auto rr = __builtin_amdgcn_permlane32_swap(__float_as_uint(v), __float_as_uint(v), false, false); return __uint_as_float(rr[0]) + __uint_as_float(rr[1]); }
DI float rsum16(float v) { auto rr = __builtin_amdgcn_permlane16_swap(__float_as_uint(v), __float_as_uint(v), false, false); return __uint_as_float(rr[0]) + __uint_as_float(rr[1]); }
DI float rmax32(float v) { auto rr = __builtin_amdgcn_permlane32_swap(__float_as_uint(v), __float_as_uint(v), false, false); return fmaxf(__uint_as_float(rr[0]), __uint_as_float(rr[1])); }
DI float rmin32(float v) { auto rr = __builtin_amdgcn_permlane32_swap(__float_as_uint(v), __float_as_uint(v), false, false); return fminf(__uint_as_float(rr[0]), __uint_as_float(rr[1])); }
DI float nv(float x) { asm("" : "+v"(x)); return x; }
#define WAITV(n) asm volatile("s_waitcnt vmcnt(" #n ")" ::: "memory")

#define TOUCHW 1
#define PLATE 0
#define XEARLY 1
#define XB 8
#define PRIO_M 2
#define PFD 4
#define LDSP 4
#define PRIO_E 0
template <int RB, int L0 = 0, int LS = LDSP, class F>
DI void mfma_tile(f32x16& acc, const bf16x8 (&xf)[32], unsigned sb, bool ld, const char* g, unsigned gstep, unsigned voff, unsigned dst, F filler) {
  __builtin_amdgcn_s_setprio(PRIO_M);
  bf16x8 wq[PFD];
#pragma unroll
  for (int ks = 0; ks < PFD; ++ks) { const int o = ((ks >> 2) * 64 + (ks & 3) * 8) * RB; wq[ks] = trfrag(sb + o, sb + o + 4 * RB); }
#pragma unroll
  for (int i = 0; i < 16; ++i) acc[i] = 0.f;
  SB0();
#pragma unroll
  for (int ks = 0; ks < 32; ++ks) {
    acc = MFMA(wq[ks % PFD], xf[ks], acc);
    if (ks + PFD < 32) { const int o = (((ks + PFD) >> 2) * 64 + ((ks + PFD) & 3) * 8) * RB; wq[ks % PFD] = trfrag(sb + o, sb + o + 4 * RB); }
    if (ks >= L0 && ((ks - L0) % LS) == 0 && ks < L0 + 8 * LS) { if (ld) glds16s(voff, g + (size_t)((ks - L0) / LS) * gstep, dst + ((ks - L0) / LS) * 1024); }
    filler(ks);
    SB0();
  }
  __builtin_amdgcn_s_setprio(PRIO_E);
  asm volatile("" : "+v"(acc));
}
DI void feat_proj(const f32x16& acc, const u32x4& wf, int r32, unsigned (&pp)[8]) {
  float av[16];
#pragma unroll
  for (int i = 0; i < 16; ++i) av[i] = acc[i];
  const bf16x8 qb0 = pack8(av), qb1 = pack8(av + 8);
  const u32x4 z4 = {0u, 0u, 0u, 0u};
  const bf16x8 a0 = __builtin_bit_cast(bf16x8, (r32 < 16) ? wf : z4), a1 = __builtin_bit_cast(bf16x8, (r32 < 16) ? z4 : wf);
  f32x16 pq;
#pragma unroll
  for (int i = 0; i < 16; ++i) pq[i] = 0.f;
  SB0();
  __builtin_amdgcn_s_setprio(3);
  pq = MFMA(a0, qb0, pq);
  pq = MFMA(a1, qb1, pq);
  SB0();
  __builtin_amdgcn_s_setprio(PRIO_E);
#pragma unroll
  for (int j = 0; j < 8; ++j) pp[j] = cvtpk(pq[2 * j], pq[2 * j + 1]);
  asm volatile("" : "+v"(pp[0]), "+v"(pp[1]), "+v"(pp[2]), "+v"(pp[3]), "+v"(pp[4]), "+v"(pp[5]), "+v"(pp[6]), "+v"(pp[7]));
}
DI void feat_soft(const unsigned* pp, bf16x8& fp, bf16x8& fm) {
  float pv[8];
#pragma unroll
  for (int j = 0; j < 4; ++j) { pv[2 * j] = bflo(pp[j]); pv[2 * j + 1] = bfhi(pp[j]); }
  float mx = fmaxf(fmaxf(fmaxf(pv[0], pv[1]), pv[2]), fmaxf(fmaxf(pv[3], pv[4]), pv[5])), mn = fminf(fminf(fminf(pv[0], pv[1]), pv[2]), fminf(fminf(pv[3], pv[4]), pv[5]));
  mx = fmaxf(fmaxf(mx, pv[6]), pv[7]); mn = fminf(fminf(mn, pv[6]), pv[7]);
  mx = rmax32(mx); mn = rmin32(mn);
  const float L2E = 1.4426950408889634f, mxl = nv(-mx * L2E), mnl = nv(mn * L2E);
  float ep[8], em[8], sp = 0.f, sm = 0.f;
#pragma unroll
  for (int j = 0; j < 8; ++j) {
    ep[j] = __builtin_amdgcn_exp2f(nv(fmaf(pv[j], L2E, mxl))); sp = nv(sp + ep[j]);
    em[j] = __builtin_amdgcn_exp2f(nv(fmaf(pv[j], -L2E, mnl))); sm = nv(sm + em[j]);
  }
  sp = nv(rsum32(sp)); sm = nv(rsum32(sm));
  const float rp = __builtin_amdgcn_rcpf(sp), rm = __builtin_amdgcn_rcpf(sm);
#pragma unroll
  for (int j = 0; j < 8; ++j) { ep[j] = nv(ep[j] * rp); em[j] = nv(em[j] * rm); }
  fp = pack8(ep); fm = pack8(em);
  asm volatile("" : "+v"(fp), "+v"(fm));
}
#define WT_STORE 1
DI void st16(void* p, u32x4 v) {
  asm volatile("global_store_dwordx4 %0, %1, off sc1\n\ts_nop 1" :: "v"(p), "v"(v) : "memory");
}
DI void epi_g(const f32x16& acc, unsigned (&sg)[8]) {
#pragma unroll
  for (int i = 0; i < 16; i += 2) {
    const unsigned u = cvtpk(acc[i], acc[i + 1]);
    sg[i >> 1] = cvtpk(nv(silu_f(bflo(u))), nv(silu_f(bfhi(u))));
  }
  asm volatile("" : "+v"(sg[0]), "+v"(sg[1]), "+v"(sg[2]), "+v"(sg[3]), "+v"(sg[4]), "+v"(sg[5]), "+v"(sg[6]), "+v"(sg[7]));
}
DI void epi_u(const f32x16& acc, const unsigned (&sg)[8], char* dst) {
  float hv[16];
#pragma unroll
  for (int i = 0; i < 16; i += 2) { const unsigned u = cvtpk(acc[i], acc[i + 1]); hv[i] = nv(bflo(sg[i >> 1]) * bflo(u)); hv[i + 1] = nv(bfhi(sg[i >> 1]) * bfhi(u)); }
  st16(dst, __builtin_bit_cast(u32x4, pack8(hv))); st16(dst + 1024, __builtin_bit_cast(u32x4, pack8(hv + 8)));
}
DI void epi_u_calc(const f32x16& acc, const unsigned (&sg)[8], u32x4& h0, u32x4& h1) {
  float hv[16];
#pragma unroll
  for (int i = 0; i < 16; i += 2) { const unsigned u = cvtpk(acc[i], acc[i + 1]); hv[i] = nv(bflo(sg[i >> 1]) * bflo(u)); hv[i + 1] = nv(bfhi(sg[i >> 1]) * bfhi(u)); }
  h0 = __builtin_bit_cast(u32x4, pack8(hv)); h1 = __builtin_bit_cast(u32x4, pack8(hv + 8));
  asm volatile("" : "+v"(h0), "+v"(h1));
}
DI void o_head(u64 (&g2)[8], const char* p2r, unsigned tag, const bf16x8& fp, const bf16x8& fm, char* dst) {
  for (unsigned spin = 0; spin < SPIN_MAX; ++spin) {
    bool ok = true;
#pragma unroll
    for (int j = 0; j < 8; ++j) ok &= (unsigned)(g2[j] >> 32) == tag;
    if (__all(ok)) break;
    __builtin_amdgcn_s_sleep(4);
#pragma unroll
    for (int j = 0; j < 8; ++j) g2[j] = ld_gran(p2r + j * 512);
  }
  __builtin_amdgcn_s_setprio(3);
  const u32x4 f0 = {(unsigned)g2[0], (unsigned)g2[1], (unsigned)g2[2], (unsigned)g2[3]};
  const u32x4 f1 = {(unsigned)g2[4], (unsigned)g2[5], (unsigned)g2[6], (unsigned)g2[7]};
  f32x16 o;
#pragma unroll
  for (int q = 0; q < 16; ++q) o[q] = 0.f;
  o = MFMA(__builtin_bit_cast(bf16x8, f0), fp, o);
  o = MFMA(__builtin_bit_cast(bf16x8, f1), fm, o);
  const float rd = __builtin_amdgcn_rcpf(bf16r(bf16r(o[8]) + 1e-6f));
  float ov[8];
#pragma unroll
  for (int j = 0; j < 8; j += 2) { const unsigned u = cvtpk(o[j], o[j + 1]); ov[j] = nv(bflo(u) * rd); ov[j + 1] = nv(bfhi(u) * rd); }
  __builtin_amdgcn_s_setprio(PRIO_E);
  st16(dst, __builtin_bit_cast(u32x4, pack8(ov)));
}
#define PHASE_END(p) do { STAMP(2 + 2 * (p)); SB0(); BAR(); SB0(); STAMP(3 + 2 * (p)); } while (0)

__global__ __launch_bounds__(512, 2) void k1_kernel(const u16* __restrict__ x, const u16* __restrict__ wqkv, const u16* __restrict__ wphi,
                                                    const u16* __restrict__ wo, const u16* __restrict__ w1, const u16* __restrict__ w3,
                                                    const u16* __restrict__ w2, char* __restrict__ ws) {
  __shared__ __attribute__((aligned(16))) char smem[K1_LDS];
  const int tid = threadIdx.x, lane = tid & 63;
  const int wave = __builtin_amdgcn_readfirstlane(tid >> 6), grp = wave >> 2, wr = wave & 3;
  const int b = blockIdx.x, xcd = b & 7, idx = b >> 3;
  const int mt = (xcd & 1) * 8 + (idx & 7), ng = (xcd >> 1) * 4 + (idx >> 3);
  const int r32 = lane & 31, h = lane >> 5;
  const unsigned lds0 = (unsigned)(uintptr_t)smem;
  unsigned dummy0 = 0;
  if (wave == 0) {
    unsigned long long pc; unsigned len;
    asm volatile("s_getpc_b64 %0\n.Lk1_pc:\n\ts_mov_b32 %1, .Lk2_end-.Lk1_pc" : "=s"(pc), "=s"(len));
    const unsigned off = (lane * 8 + (idx & 7)) * 128;
    if (off < len) asm volatile("global_load_dword %0, %1, %2" : "+v"(dummy0) : "v"(off), "s"(pc) : "memory");
  }
  unsigned dummy1 = 0;
#define XTOUCH 0
  if (XTOUCH && grp == 0) asm volatile("global_load_dword %0, %1, %2" : "+v"(dummy1) : "v"(lane * 128), "s"((const char*)(x + (size_t)(mt * 128 + (idx >> 3) * 32 + wr * 8) * D_)) : "memory");
  const unsigned reg = lds0 + SLOT + (wr * 2 + grp) * 16384;
  {
    const char* xb = (const char*)(x + (size_t)(mt * 128 + wr * 32 + grp * 16) * D_);
    const int rr = lane >> 5, qd = (lane >> 3) & 3, pos = lane & 7;
#pragma unroll
    for (int cc = 0; cc < 2; ++cc)
#pragma unroll
      for (int i = 0; i < 8; ++i)
        glds16s(rr * 1024 + qd * 256 + (pos << 4), xb + i * 2048 + cc * 128, reg + cc * 8192 + i * 1024);
  }
  const int g0 = ng < 11 ? 3 * ng : 33 + 2 * (ng - 11);
  const bool full = ng < 11;
  const char* wK = (const char*)(wqkv + 512 + 32 * ng);
  const char* wG = (const char*)(w1 + 32 * g0);
  const char* wU = (const char*)(w3 + 32 * g0);
  const unsigned voffA = (lane >> 2) * 3072 + (lane & 3) * 16, voffF = (lane >> 2) * 2752 + (lane & 3) * 16;
  unsigned dummy = 0, dummy2 = 0, tagv;
  u32x4 wf, wfb;
  {
    tagv = __hip_atomic_load((GAS unsigned*)(ws + WS_EP + ng * 128), __ATOMIC_RELAXED, __HIP_MEMORY_SCOPE_AGENT);
    const u16* wp = wphi + (size_t)(2 * ng + (r32 >> 4)) * 256 + (4 * h) * 16 + (lane & 15);
#pragma unroll
    for (int j = 0; j < 4; ++j) {
      const int k0 = 8 * (j >> 1) + 2 * (j & 1);
      asm volatile("global_load_ushort %0, %2, off offset:%3\n\tglobal_load_ushort %1, %2, off offset:%4" : "=&v"(wf[j]), "=&v"(wfb[j]) : "v"(wp), "n"(k0 * 32), "n"(k0 * 32 + 32) : "memory");
    }
    if (grp) {
      const char* g = wK + (size_t)wr * 128 * 3072;
#pragma unroll
      for (int i = 0; i < 8; ++i) glds16s(voffA, g + i * 16 * 3072, lds0 + wr * 8192 + i * 1024);
      const int trow = (idx & 7) * 64 + lane;
      const int e = (wr == 3 && !full) ? 0 : wr - 1;
      const char* p = wr == 0 ? wK + 1024 + (size_t)trow * 3072 : wG + 64 * e + (size_t)trow * 2752;
      const char* p2 = wr == 0 ? wK - 1024 + (size_t)trow * 3072 : wU + 64 * e + (size_t)trow * 2752;
      asm volatile("global_load_dword %0, %1, off" : "+v"(dummy) : "v"(p) : "memory");
      asm volatile("global_load_dword %0, %1, off" : "+v"(dummy2) : "v"(p2) : "memory");
    }
  }
  bf16x8 xf[32];
  unsigned tag;
  {
    const int r16 = lane & 15, qd = lane >> 4;
    const unsigned rb = reg + (r16 >> 1) * 1024 + (r16 & 1) * 512 + qd * 128 + ((r16 & 7) << 4);
    u32x4 xr[16];
    float q0 = 0.f, q1 = 0.f;
    f32x4 sum4 = {0.f, 0.f, 0.f, 0.f};
    const u32x4 ones = {0x3f803f80u, 0x3f803f80u, 0x3f803f80u, 0x3f803f80u};
    if (grp) WAITV(27); else WAITV(17);
#pragma unroll
    for (int q = 0; q < 8; ++q) xr[q] = *(const LAS u32x4*)(uintptr_t)(rb ^ (q << 4));
#pragma unroll
    for (int q = 0; q < 8; ++q)
#pragma unroll
      for (int e = 0; e < 4; e += 2) {
        if (e == 0) sum4 = MFMA16(ones, xr[q], sum4);
        q0 = dot2(xr[q][e], xr[q][e], q0); q1 = dot2(xr[q][e + 1], xr[q][e + 1], q1);
      }
    asm volatile("" : "+v"(sum4), "+v"(q0), "+v"(q1));
    if (grp) WAITV(19); else WAITV(9);
#pragma unroll
    for (int q = 0; q < 8; ++q) xr[8 + q] = *(const LAS u32x4*)(uintptr_t)((rb ^ (q << 4)) + 8192);
#pragma unroll
    for (int q = 8; q < 16; ++q)
#pragma unroll
      for (int e = 0; e < 4; e += 2) {
        if (e == 0) sum4 = MFMA16(ones, xr[q], sum4);
        q0 = dot2(xr[q][e], xr[q][e], q0); q1 = dot2(xr[q][e + 1], xr[q][e + 1], q1);
      }
    const float mean = sum4[0] * (1.0f / 512.0f);
    const float var = fmaxf(rsum32(rsum16(q0 + q1)) * (1.0f / 512.0f) - mean * mean, 0.f);
    const float nmb = -bf16r(mean);
    const float rs = 1.0f / bf16r(sqrtf(bf16r(bf16r(var) + 1e-5f)));
    const unsigned wb = reg + qd * 4096 + r16 * 16 + (r16 & 3) * 512 + ((r16 >> 2) & 1) * 256;
#pragma unroll
    for (int q = 0; q < 16; ++q) {
      u32x4 o;
      center4(xr[q], nmb);
#pragma unroll
      for (int e = 0; e < 4; ++e) o[e] = cvtpk(bflo(xr[q][e]) * rs, bfhi(xr[q][e]) * rs);
      *(LAS u32x4*)(uintptr_t)((wb ^ ((q & 3) * 512 + ((q >> 2) & 1) * 256)) + (q >> 3) * 2048) = o;
    }
    const unsigned xb = lds0 + SLOT * (1 + wr) + (r32 >> 4) * 16384 + (h * 16 + (r32 & 15)) * 16;
    if (XEARLY && grp == 0 && r32 < 16) {
#pragma unroll
      for (int ks = 0; ks < 32; ++ks) xf[ks] = *(const LAS bf16x8*)(uintptr_t)(xb + ks * 512);
    }
    SB0();
    WAITV(0);
    asm volatile("" : "+v"(dummy), "+v"(dummy0), "+v"(dummy1), "+v"(dummy2), "+v"(wf), "+v"(wfb));
#pragma unroll
    for (int j = 0; j < 4; ++j) wf[j] |= wfb[j] << 16;
    tag = __builtin_amdgcn_readfirstlane(tagv) + 1u;
    asm volatile("" : "+s"(tag));
    BAR();
    if (!XEARLY || grp || r32 >= 16) {
#pragma unroll
      for (int ks = 0; ks < 32; ++ks) xf[ks] = *(const LAS bf16x8*)(uintptr_t)(xb + ks * 512);
    }
    if (!XEARLY || grp) BAR();
  }
  STAMP(1);
  const unsigned voffP = (lane >> 3) * 2752 + (((lane & 7) ^ ((lane >> 2) & 4)) << 4);
  const size_t rowP = (size_t)wave * 64 * 2752;
  const int i16 = lane & 15, blk = (lane >> 4) & 1;
  const unsigned tr0 = lds0 + (32 * h + (i16 >> 2)) * 64 + 8 * (4 * blk + (i16 & 3));
  const unsigned trP = lds0 + (32 * h + (i16 >> 2)) * 128 + (((grp ^ (i16 >> 3)) & 1) << 6) + 8 * (4 * blk + (i16 & 3));
  const int mblk = mt * 4 + wr;
  const unsigned ldd = lds0 + wr * 8192;
  const size_t rowF = (size_t)wr * 128 * 2752, rowA = (size_t)wr * 128 * 3072;
  char* const hdst = ws + WS_AB + ((size_t)mblk * KS_W + 32 + 2 * g0) * 1024 + lane * 16;
  unsigned sg[8], pp[8];
  f32x16 acc, acc2;
  const int row = wr * 32 + r32, sw = (row >> 1) & 7;
  auto nofill = [](int) {};
  const unsigned ppx = lds0 + LDS_SCR + 24576 + wr * 1024 + lane * 16;
  const char* p2r = ws + WS_P2 + (size_t)(ng * 16 + (grp ? 0 : 8)) * 512 + lane * 8;
  char* const odst = ws + WS_AB + ((size_t)mblk * KS_W + 2 * ng + (grp ? 0 : 1)) * 1024 + lane * 16;
  u64 g2[8];
  bf16x8 fq, fn;
  if (grp == 0) {
#define QL0 16
    mfma_tile<64, QL0, 2>(acc, xf, tr0, true, wK - 1024 + rowA, 16 * 3072, voffA, ldd + 2 * SLOT, [&](int ks) { if (XEARLY && ks == XB) BAR(); });
    PHASE_END(0);
    feat_proj(acc, wf, r32, pp);
#pragma unroll
    for (int s = 0; s < 2; ++s) {
      if (s) {
#pragma unroll
        for (int i = 0; i < 4; ++i) glds16s(voffP, wG + rowP + i * 8 * 2752, lds0 + wave * 8192 + i * 1024);
      }
      bf16x8 fp, fm;
      feat_soft(pp + 4 * s, fp, fm);
      if (s) {
#pragma unroll
        for (int i = 4; i < 8; ++i) glds16s(voffP, wG + rowP + i * 8 * 2752, lds0 + wave * 8192 + i * 1024);
      }
      const unsigned pk = lds0 + LDS_SCR + s * 8192 + row * 64;
      const u32x4 up = __builtin_bit_cast(u32x4, fp), um = __builtin_bit_cast(u32x4, fm);
      u32x2 t;
      t[0] = up[0]; t[1] = up[1]; *(LAS u32x2*)(uintptr_t)(pk + 8 * ((h) ^ sw)) = t;
      t[0] = up[2]; t[1] = up[3]; *(LAS u32x2*)(uintptr_t)(pk + 8 * ((2 + h) ^ sw)) = t;
      t[0] = um[0]; t[1] = um[1]; *(LAS u32x2*)(uintptr_t)(pk + 8 * ((4 + h) ^ sw)) = t;
      t[0] = um[2]; t[1] = um[3]; *(LAS u32x2*)(uintptr_t)(pk + 8 * ((6 + h) ^ sw)) = t;
      if (s == 0) { SB0(); WAITV(0); }
      PHASE_END(1 + s);
    }
    unsigned d0 = 0, d1 = 0;
    const int tr = idx * 59 + (lane < 59 ? lane : 58);
    const char* tp = (tr < 512 ? (const char*)wo + (size_t)tr * 1024 : (const char*)w2 + (size_t)(tr - 512) * 1024) + wr * 256;
    if (TOUCHW == 1) asm volatile("global_load_dword %0, %1, off" : "+v"(d0) : "v"(tp) : "memory");
    if (full) mfma_tile<64>(acc2, xf, tr0 + 3 * SLOT, false, nullptr, 0, 0, 0, nofill);
    if (TOUCHW == 1) WAITV(1); else WAITV(0);
    PHASE_END(3);
    if (TOUCHW == 3) asm volatile("global_load_dword %0, %1, off" : "+v"(d0) : "v"(tp) : "memory");
    mfma_tile<128>(acc, xf, trP, true, wU + rowP, 8 * 2752, voffP, lds0 + 2 * SLOT + wave * 8192, nofill);
    PHASE_END(4);
    if (TOUCHW == 2) asm volatile("global_load_dword %0, %1, off" : "+v"(d0) : "v"(tp) : "memory");
    if (TOUCHW) asm volatile("global_load_dword %0, %1, off offset:128" : "+v"(d1) : "v"(tp) : "memory");
    epi_g(acc, sg);
    { const u32x4 t = *(const LAS u32x4*)(uintptr_t)ppx; pp[0] = t[0]; pp[1] = t[1]; pp[2] = t[2]; pp[3] = t[3]; }
    feat_soft(pp, fq, fn);
    if (TOUCHW == 2) WAITV(2); else if (TOUCHW) WAITV(1); else WAITV(0);
    PHASE_END(5);
    mfma_tile<128>(acc, xf, trP + 2 * SLOT, full, wU + 128 + rowF, 16 * 2752, voffF, ldd, nofill);
    PHASE_END(6);
    epi_u(acc, sg, hdst);
    if (full) epi_g(acc2, sg);
    WAITV(2);
    asm volatile("" :: "v"(d0), "v"(d1));
    PHASE_END(7);
#define G2L 12
    u32x4 h0, h1;
    if (full) {
      mfma_tile<64>(acc, xf, tr0, false, nullptr, 0, 0, 0, [&](int ks) { if (ks >= G2L && ks < G2L + 8) g2[ks - G2L] = ld_gran(p2r + (ks - G2L) * 512); });
      epi_u_calc(acc, sg, h0, h1);
    } else {
#pragma unroll
      for (int j = 0; j < 8; ++j) g2[j] = ld_gran(p2r + j * 512);
    }
    STAMP(18);
    o_head(g2, p2r, tag, fq, fn, odst);
    STAMP(19);
    if (full) { st16(hdst + 4096, h0); st16(hdst + 5120, h1); }
    STAMP(20);
  } else {
#pragma unroll
    for (int i = 0; i < 8; ++i) glds16s(voffA, wK + 1024 + rowA + i * 16 * 3072, ldd + SLOT + i * 1024);
    WAITV(0);
    PHASE_END(0);
    mfma_tile<64>(acc2, xf, tr0 + SLOT, full, wG + 128 + rowF, 16 * 2752, voffF, ldd + 3 * SLOT, nofill);
    PHASE_END(1);
    {
      const unsigned vi = lds0 + LDS_SCR + 16384 + row * 64;
      mfma_tile<64>(acc, xf, tr0 + 2 * SLOT, !PLATE, wG + rowP, 8 * 2752, voffP, lds0 + wave * 8192, [&](int ks) {
        if (ks >= 1 && ks <= 4) {
          const int a = ks - 1;
          u32x2 t; t[0] = cvtpk(acc2[4 * a], acc2[4 * a + 1]); t[1] = cvtpk(acc2[4 * a + 2], acc2[4 * a + 3]);
          *(LAS u32x2*)(uintptr_t)(vi + 8 * ((2 * a + h) ^ sw)) = t;
        }
      });
    }
    if (PLATE) WAITV(0); else WAITV(8);
    PHASE_END(2);
    if (PLATE) {
#pragma unroll
      for (int i = 0; i < 8; ++i) glds16s(voffP, wG + rowP + i * 8 * 2752, lds0 + wave * 8192 + i * 1024);
    }
    if (wr < 2) {
      f32x16 a2;
#pragma unroll
      for (int i = 0; i < 16; ++i) a2[i] = 0.f;
      const int rowq = 8 * h + (i16 >> 2);
      const int ca = 4 * blk + (lane & 3), cb = 4 * (blk ^ wr) + (lane & 3);
      const int sw0 = (rowq >> 1) & 7, sw1 = ((rowq + 4) >> 1) & 7;
      const unsigned pk = lds0 + LDS_SCR + wr * 8192 + rowq * 64, vi = lds0 + LDS_SCR + 16384 + rowq * 64;
      const unsigned pa0 = pk + 8 * (ca ^ sw0), pa1 = pk + 256 + 8 * (ca ^ sw1), pb0 = vi + 8 * (cb ^ sw0), pb1 = vi + 256 + 8 * (cb ^ sw1);
      __builtin_amdgcn_s_setprio(3);
#pragma unroll
      for (int ks = 0; ks < 8; ++ks) {
        bf16x8 af = trfrag(pa0 + ks * 1024, pa1 + ks * 1024);
        bf16x8 bfr = trfrag(pb0 + ks * 1024, pb1 + ks * 1024);
        if (blk) {
#pragma unroll
          for (int j = 0; j < 8; ++j) bfr[j] = (short)0x3f80;
        }
        a2 = MFMA(af, bfr, a2);
      }
      asm volatile("" : "+v"(a2));
      __builtin_amdgcn_s_setprio(PRIO_E);
      char* p1w = ws + WS_P1 + (((size_t)(ng * 16 + wr * 8) * 16 + mt) * 1024) + lane * 8;
#pragma unroll
      for (int i = 0; i < 16; ++i) st_gran(p1w + (size_t)(i >> 1) * 16384 + (i & 1) * 512, __float_as_uint(a2[i]), tag);
    }
    feat_proj(acc, wf, r32, pp);
    { const u32x4 t = {pp[4], pp[5], pp[6], pp[7]}; *(LAS u32x4*)(uintptr_t)ppx = t; }
    if (wr >= 2) feat_soft(pp, fq, fn);
    if (wr < 2) WAITV(16); else WAITV(0);
    PHASE_END(3);
#pragma unroll
    for (int i = 0; i < 4; ++i) glds16s(voffP, wU + rowP + i * 8 * 2752, lds0 + 2 * SLOT + wave * 8192 + i * 1024);
    if (wr < 2) feat_soft(pp, fq, fn);
#pragma unroll
    for (int i = 4; i < 8; ++i) glds16s(voffP, wU + rowP + i * 8 * 2752, lds0 + 2 * SLOT + wave * 8192 + i * 1024);
    PHASE_END(4);
    u64 g1[8];
    const char* p1r = ws + WS_P1 + ((size_t)(ng * 16 + mt) * 16 + h * 8) * 1024 + blk * 512 + (wr * 16 + i16) * 8;
    mfma_tile<128>(acc, xf, trP, false, nullptr, 0, 0, 0, [&](int ks) { if (ks >= 16 && ks < 24) g1[ks - 16] = ld_gran(p1r + (ks - 16) * 1024); });
    WAITV(8);
    PHASE_END(5);
    {
      for (unsigned spin = 0; spin < SPIN_MAX; ++spin) {
        bool ok = true;
#pragma unroll
        for (int j = 0; j < 8; ++j) ok &= (unsigned)(g1[j] >> 32) == tag;
        if (__all(ok)) break;
        __builtin_amdgcn_s_sleep(4);
#pragma unroll
        for (int j = 0; j < 8; ++j) g1[j] = ld_gran(p1r + j * 1024);
      }
      float a = 0.f;
#pragma unroll
      for (int j = 0; j < 8; ++j) a = nv(a + __uint_as_float((unsigned)g1[j]));
      a = rsum32(a);
      auto rr = __builtin_amdgcn_permlane16_swap(__float_as_uint(a), __float_as_uint(a), false, false);
      if (lane < 16) st_gran(ws + WS_P2 + (size_t)(ng * 16 + mt) * 512 + (wr * 16 + lane) * 8, cvtpk(__uint_as_float(rr[0]), __uint_as_float(rr[1])), tag);
      if (mt == 0 && wr == 0 && lane == 0) __hip_atomic_store((GAS unsigned*)(ws + WS_EP + ng * 128), tag, __ATOMIC_RELAXED, __HIP_MEMORY_SCOPE_AGENT);
      SB0();
      epi_g(acc, sg);
    }
    PHASE_END(6);
    mfma_tile<128>(acc, xf, trP + 2 * SLOT, false, nullptr, 0, 0, 0, nofill);
    PHASE_END(7);
#pragma unroll
    for (int j = 0; j < 8; ++j) g2[j] = ld_gran(p2r + j * 512);
    SB0();
    u32x4 h0, h1;
    epi_u_calc(acc, sg, h0, h1);
    STAMP(18);
    o_head(g2, p2r, tag, fq, fn, odst);
    STAMP(19);
    st16(hdst + 2048, h0); st16(hdst + 3072, h1);
    STAMP(20);
  }
  STAMPR(31);
  STAMP_FLUSH(0);
  asm volatile(".Lk1_end:");
}

constexpr int XROW = 68;
constexpr int P2 = 10;
template <int N> DI void waitv() { asm volatile("s_waitcnt vmcnt(%0)" :: "n"(N) : "memory"); }
__global__ __launch_bounds__(256, 1) void k2_kernel(const u16* __restrict__ x, const u16* __restrict__ wo, const u16* __restrict__ w2, const char* __restrict__ ws, u16* __restrict__ out) {
  __shared__ __attribute__((aligned(16))) char smem[4 * P2 * 4096];
  float* xch = (float*)smem;
  const int tid = threadIdx.x, lane = tid & 63;
  const int wave = __builtin_amdgcn_readfirstlane(tid >> 6);
  const int b = blockIdx.x, xcd = b & 7, idx = b >> 3;
  const int mt = (xcd & 1) * 16 + (xcd >> 1) * 4 + (idx & 3), nt = idx >> 2;
  const int r32 = lane & 31, h = lane >> 5;
  const int erow = mt * 64 + (tid >> 2), ecol = nt * 64 + (tid & 3) * 16;
  const u32x4 xa = *(const u32x4*)(x + (size_t)erow * D_ + ecol), xb = *(const u32x4*)(x + (size_t)erow * D_ + ecol + 8);
  f32x16 acc[2][2];
#pragma unroll
  for (int n = 0; n < 2; ++n)
#pragma unroll
    for (int m = 0; m < 2; ++m)
#pragma unroll
      for (int i = 0; i < 16; ++i) acc[n][m][i] = 0.f;
  const int k0 = wave == 0 ? 0 : 32 + (wave - 1) * 29;
  const int nk = wave == 0 ? 32 : (wave == 3 ? 28 : 29);
  const char* wsrc = (wave == 0 ? (const char*)wo : (const char*)w2 + (size_t)(k0 - 32) * 16384) + nt * 128;
  const char* ha = ws + WS_AB + ((size_t)(2 * mt) * KS_W + k0) * 1024;
  const char* hb = ha + (size_t)KS_W * 1024;
  constexpr int RS = P2 * 4096;
  const unsigned ring = (unsigned)(uintptr_t)smem + wave * RS;
  const unsigned lo = lane * 16, voffW = (lane >> 3) * 1024 + (((lane & 7) ^ ((lane >> 2) & 4)) << 4);
  const int i16 = lane & 15, blk = (lane >> 4) & 1;
  const unsigned tb0 = ring + 2048 + (4 * h + (i16 >> 2)) * 128 + (((i16 >> 3) & 1) << 6) + 8 * (4 * blk + (i16 & 3)), tb1 = tb0 ^ 64;
  auto issue = [&](int i) {
    const unsigned d = ring + (i % P2) * 4096;
    glds16s(lo, ha + i * 1024, d); glds16s(lo, hb + i * 1024, d + 1024);
    glds16s(voffW, wsrc + i * 16384, d + 2048); glds16s(voffW, wsrc + i * 16384 + 8192, d + 3072);
  };
  bf16x8 R[2][4];
  auto rd = [&](int i, bf16x8* d) {
    const int o = (i % P2) * 4096;
    d[0] = *(const LAS bf16x8*)(uintptr_t)(ring + lo + o); d[1] = *(const LAS bf16x8*)(uintptr_t)(ring + lo + o + 1024);
    d[2] = trfrag(tb0 + o, tb0 + o + 1024); d[3] = trfrag(tb1 + o, tb1 + o + 1024);
  };
#pragma unroll
  for (int i = 0; i < P2; ++i) issue(i);
  waitv<4 * (P2 - 1)>();
  rd(0, R[0]);
#pragma unroll
  for (int i = 0; i < 32; ++i) {
    if (i < 28 || i < nk) {
      if (i + 1 < 28 || i + 1 < nk) {
        if (i + 1 < 32) {
          constexpr int full_w = 4 * (P2 - 2);
          if (4 * (26 - i) >= full_w) waitv<full_w>();
          else if (i == 19) waitv<28>(); else if (i == 20) waitv<24>(); else if (i == 21) waitv<20>(); else if (i == 22) waitv<16>();
          else if (i == 23) waitv<12>(); else if (i == 24) waitv<8>(); else if (i == 25) waitv<4>(); else waitv<0>();
          rd(i + 1, R[(i + 1) & 1]);
        }
      }
      SB0();
      const bf16x8* d = R[i & 1];
      acc[0][0] = MFMA(d[2], d[0], acc[0][0]); acc[0][1] = MFMA(d[2], d[1], acc[0][1]);
      acc[1][0] = MFMA(d[3], d[0], acc[1][0]); acc[1][1] = MFMA(d[3], d[1], acc[1][1]);
      SB0();
      if (i + P2 < 28) issue(i + P2);
      else if (i + P2 < 32) { if (i + P2 < nk) issue(i + P2); }
      SB0();
    }
  }
  __syncthreads();
  STAMP(1);
  float* mine = xch + wave * 64 * XROW;
#pragma unroll
  for (int n = 0; n < 2; ++n)
#pragma unroll
    for (int m = 0; m < 2; ++m)
#pragma unroll
      for (int a = 0; a < 4; ++a) {
        f32x4 t = {acc[n][m][4 * a], acc[n][m][4 * a + 1], acc[n][m][4 * a + 2], acc[n][m][4 * a + 3]};
        *(f32x4*)(mine + (32 * m + r32) * XROW + 32 * n + 8 * a + 4 * h) = t;
      }
  __syncthreads();
  STAMP(2);
  const float* rp = xch + (tid >> 2) * XROW + (tid & 3) * 16;
  u32x4 res[2];
#pragma unroll
  for (int q = 0; q < 4; ++q) {
    const f32x4 s0 = *(const f32x4*)(rp + 4 * q), o0 = *(const f32x4*)(rp + 64 * XROW + 4 * q);
    const f32x4 o1 = *(const f32x4*)(rp + 128 * XROW + 4 * q), o2 = *(const f32x4*)(rp + 192 * XROW + 4 * q);
#pragma unroll
    for (int e = 0; e < 4; e += 2) {
      const unsigned su = cvtpk(s0[e], s0[e + 1]);
      const unsigned ou = cvtpk((o0[e] + o1[e]) + o2[e], (o0[e + 1] + o1[e + 1]) + o2[e + 1]);
      const unsigned gu = cvtpk(silu_f(bflo(ou)), silu_f(bfhi(ou)));
      const unsigned tu = cvtpk(bflo(su) * bflo(gu), bfhi(su) * bfhi(gu));
      const unsigned xw = (q < 2 ? xa : xb)[(q & 1) * 2 + (e >> 1)];
      res[q >> 1][(q & 1) * 2 + (e >> 1)] = cvtpk(bflo(xw) + bflo(tu), bfhi(xw) + bfhi(tu));
    }
  }
  *(u32x4*)(out + (size_t)erow * D_ + ecol) = res[0];
  *(u32x4*)(out + (size_t)erow * D_ + ecol + 8) = res[1];
  STAMP(3); STAMPR(31);
  asm volatile(".Lk2_end:");
}

extern "C" void kernel_launch(void* const* d_in, const int* in_sizes, int n_in, void* d_out, int out_size, void* d_ws, size_t ws_size,
                              hipStream_t stream) {
  const u16* x = (const u16*)d_in[0];
  const u16* wqkv = (const u16*)d_in[1];
  const u16* wphi = (const u16*)d_in[2];
  const u16* wo = (const u16*)d_in[3];
  const u16* w1 = (const u16*)d_in[5];
  const u16* w3 = (const u16*)d_in[6];
  const u16* w2 = (const u16*)d_in[7];
  char* ws = (char*)d_ws;
  k1_kernel<<<256, 512, 0, stream>>>(x, wqkv, wphi, wo, w1, w3, w2, ws);
  k2_kernel<<<256, 256, 0, stream>>>(x, wo, w2, ws, (u16*)d_out);
}
```

```cpp
#include <hip/hip_runtime.h>
#include <hip/hip_bf16.h>
#include <stdint.h>

typedef __attribute__((ext_vector_type(8))) short bf16x8;
typedef __attribute__((ext_vector_type(4))) short s16x4;
typedef __attribute__((ext_vector_type(16))) float f32x16;
typedef __attribute__((ext_vector_type(4))) float f32x4;
typedef __attribute__((ext_vector_type(4))) unsigned u32x4;
typedef __attribute__((ext_vector_type(2))) unsigned u32x2;
typedef unsigned short u16;
#define LAS __attribute__((address_space(3)))
#define DI __device__ __forceinline__
#define MFMA(a, b, c) __builtin_amdgcn_mfma_f32_32x32x16_bf16((a), (b), (c), 0, 0, 0)
#define MFMA16(a, b, c) __builtin_amdgcn_mfma_f32_16x16x32_bf16(__builtin_bit_cast(bf16x8, (a)), __builtin_bit_cast(bf16x8, (b)), (c), 0, 0, 0)

constexpr int S_ = 2048, D_ = 512, NH = 32, F_ = 1376;
constexpr int KS_F = F_ / 16;
constexpr int KS_W = 32 + KS_F;
constexpr size_t WS_AB = 0;
constexpr size_t WS_WT = WS_AB + (size_t)64 * KS_W * 1024;
constexpr size_t WS_P1 = WS_WT + (size_t)16 * KS_W * 1024;
constexpr size_t WS_P2 = WS_P1 + (size_t)16 * 16 * 16 * 1024;
constexpr size_t WS_EP = WS_P2 + (size_t)16 * 16 * 512;
constexpr size_t WS_DUMP = WS_EP + 16 * 128;
constexpr size_t WS_END = WS_DUMP + 2048;

typedef __attribute__((ext_vector_type(2))) __bf16 bf16x2_t;
typedef __attribute__((ext_vector_type(2))) float f32x2;
DI unsigned cvtpk(float lo, float hi) { f32x2 v = {lo, hi}; return __builtin_bit_cast(unsigned, __builtin_convertvector(v, bf16x2_t)); }
DI float bflo(unsigned u) { return __uint_as_float(u << 16); }
DI float bfhi(unsigned u) { return __uint_as_float(u & 0xffff0000u); }
DI float bf16r(float x) { return bflo(cvtpk(x, x)); }
DI void glds16(const void* g, unsigned lds_base) {
  unsigned sv;
  asm volatile("s_mov_b32 %0, m0\n\ts_mov_b32 m0, %2\n\ts_nop 0\n\tglobal_load_lds_dwordx4 %1, off\n\ts_mov_b32 m0, %0"
               : "=&s"(sv) : "v"(g), "s"(lds_base) : "memory");
}
typedef short v4i16_t __attribute__((ext_vector_type(4)));
DI s16x4 trrd(unsigned a) { return __builtin_bit_cast(s16x4, __builtin_amdgcn_ds_read_tr16_b64_v4i16((LAS v4i16_t*)(uintptr_t)a)); }
DI bf16x8 trfrag(unsigned lo, unsigned hi) { s16x4 a = trrd(lo), b = trrd(hi); return __builtin_shufflevector(a, b, 0, 1, 2, 3, 4, 5, 6, 7); }
DI bf16x8 pack8(const float* v) {
  u32x4 p; p[0] = cvtpk(v[0], v[1]); p[1] = cvtpk(v[2], v[3]); p[2] = cvtpk(v[4], v[5]); p[3] = cvtpk(v[6], v[7]);
  return __builtin_bit_cast(bf16x8, p);
}
DI float xhalf(float v) {
  auto rr = __builtin_amdgcn_permlane32_swap(__float_as_uint(v), __float_as_uint(v), false, false);
  unsigned a = rr[0], b = rr[1];
  return __uint_as_float((threadIdx.x & 32) ? a : b);
}
template <int C> DI float dppror(float v) { return __uint_as_float(__builtin_amdgcn_update_dpp(0u, __float_as_uint(v), 0x120 + C, 0xf, 0xf, false)); }
DI float row16sum(float v) { v += dppror<8>(v); v += dppror<4>(v); v += dppror<2>(v); v += dppror<1>(v); return v; }
DI float silu_f(float x) { return x * __builtin_amdgcn_rcpf(1.0f + __builtin_amdgcn_exp2f(-1.4426950408889634f * x)); }
#define STAMP(i)
#define STAMPR(i)
#define STAMP_FLUSH(base)
#define BAR() asm volatile("s_waitcnt lgkmcnt(0)\n\ts_barrier" ::: "memory")
#define WAITV0() asm volatile("s_waitcnt vmcnt(0)" ::: "memory")

enum { T_NONE = 0, T_Q = 1, T_K = 2, T_V = 3, T_G = 4, T_U = 5 };
#define TD(ty, ix) ((unsigned long long)(((ty) << 6) | (ix)))
#define SB0() __builtin_amdgcn_sched_barrier(0)
#define GAS __attribute__((address_space(1)))
typedef unsigned long long u64;
DI float dot2(unsigned a, unsigned b, float c) {
  return __builtin_amdgcn_fdot2_f32_bf16(__builtin_bit_cast(bf16x2_t, a), __builtin_bit_cast(bf16x2_t, b), c, false);
}
DI void st_gran(void* p, unsigned val, unsigned tag) { __hip_atomic_store((GAS u64*)p, ((u64)tag << 32) | val, __ATOMIC_RELAXED, __HIP_MEMORY_SCOPE_AGENT); }
DI u64 ld_gran(const void* p) { return __hip_atomic_load((GAS u64*)p, __ATOMIC_RELAXED, __HIP_MEMORY_SCOPE_AGENT); }
constexpr unsigned SPIN_MAX = 1u << 16;

constexpr int SLOT = 32768;
constexpr int LDS_SCR = 4 * SLOT;
constexpr int LDS_DBG = LDS_SCR + 28672;
constexpr int K1_LDS = 5 * SLOT;

DI void glds16s(unsigned voff, const void* sbase, unsigned lds_base) {
  unsigned sv;
  asm volatile("s_mov_b32 %0, m0\n\ts_mov_b32 m0, %3\n\ts_nop 0\n\tglobal_load_lds_dwordx4 %1, %2\n\ts_mov_b32 m0, %0"
               : "=&s"(sv) : "v"(voff), "s"(sbase), "s"(lds_base) : "memory");
}
DI void center4(u32x4& a, float c) {
  unsigned t0, t1, t2, t3, t4, t5, t6, t7;
  asm("v_dot2_f32_bf16 %0, %8, %12, %14\n\tv_dot2_f32_bf16 %1, %8, %13, %14\n\tv_dot2_f32_bf16 %2, %9, %12, %14\n\tv_dot2_f32_bf16 %3, %9, %13, %14\n\t"
      "v_dot2_f32_bf16 %4, %10, %12, %14\n\tv_dot2_f32_bf16 %5, %10, %13, %14\n\tv_dot2_f32_bf16 %6, %11, %12, %14\n\tv_dot2_f32_bf16 %7, %11, %13, %14\n\t"
      "v_cvt_pk_bf16_f32 %0, %0, %1\n\tv_cvt_pk_bf16_f32 %2, %2, %3\n\tv_cvt_pk_bf16_f32 %4, %4, %5\n\tv_cvt_pk_bf16_f32 %6, %6, %7"
      : "=&v"(t0), "=&v"(t1), "=&v"(t2), "=&v"(t3), "=&v"(t4), "=&v"(t5), "=&v"(t6), "=&v"(t7)
      : "v"(a[0]), "v"(a[1]), "v"(a[2]), "v"(a[3]), "s"(0x00003f80u), "s"(0x3f800000u), "v"(c));
  a[0] = t0; a[1] = t2; a[2] = t4; a[3] = t6;
}
DI float rsum32(float v) { auto rr = __builtin_amdgcn_permlane32_swap(__float_as_uint(v), __float_as_uint(v), false, false); return __uint_as_float(rr[0]) + __uint_as_float(rr[1]); }
DI float rsum16(float v) { auto rr = __builtin_amdgcn_permlane16_swap(__float_as_uint(v), __float_as_uint(v), false, false); return __uint_as_float(rr[0]) + __uint_as_float(rr[1]); }
DI float rmax32(float v) { auto rr = __builtin_amdgcn_permlane32_swap(__float_as_uint(v), __float_as_uint(v), false, false); return fmaxf(__uint_as_float(rr[0]), __uint_as_float(rr[1])); }
DI float rmin32(float v) { auto rr = __builtin_amdgcn_permlane32_swap(__float_as_uint(v), __float_as_uint(v), false, false); return fminf(__uint_as_float(rr[0]), __uint_as_float(rr[1])); }
DI float nv(float x) { asm("" : "+v"(x)); return x; }
#define WAITV(n) asm volatile("s_waitcnt vmcnt(" #n ")" ::: "memory")

#define TOUCHW 1
#define PLATE 0
#define PRIO_M 2
#define PFD 4
#define LDSP 4
#define PRIO_E 0
template <int RB, int L0 = 0, int LS = LDSP, class F>
DI void mfma_tile(f32x16& acc, const bf16x8 (&xf)[32], unsigned sb, bool ld, const char* g, unsigned gstep, unsigned voff, unsigned dst, F filler) {
  __builtin_amdgcn_s_setprio(PRIO_M);
  bf16x8 wq[PFD];
#pragma unroll
  for (int ks = 0; ks < PFD; ++ks) { const int o = ((ks >> 2) * 64 + (ks & 3) * 8) * RB; wq[ks] = trfrag(sb + o, sb + o + 4 * RB); }
#pragma unroll
  for (int i = 0; i < 16; ++i) acc[i] = 0.f;
  SB0();
#pragma unroll
  for (int ks = 0; ks < 32; ++ks) {
    acc = MFMA(wq[ks % PFD], xf[ks], acc);
    if (ks + PFD < 32) { const int o = (((ks + PFD) >> 2) * 64 + ((ks + PFD) & 3) * 8) * RB; wq[ks % PFD] = trfrag(sb + o, sb + o + 4 * RB); }
    if (ks >= L0 && ((ks - L0) % LS) == 0 && ks < L0 + 8 * LS) { if (ld) glds16s(voff, g + (size_t)((ks - L0) / LS) * gstep, dst + ((ks - L0) / LS) * 1024); }
    filler(ks);
    SB0();
  }
  __builtin_amdgcn_s_setprio(PRIO_E);
  asm volatile("" : "+v"(acc));
}
DI void feat_proj(const f32x16& acc, const u32x4& wf, int r32, unsigned (&pp)[8]) {
  float av[16];
#pragma unroll
  for (int i = 0; i < 16; ++i) av[i] = acc[i];
  const bf16x8 qb0 = pack8(av), qb1 = pack8(av + 8);
  const u32x4 z4 = {0u, 0u, 0u, 0u};
  const bf16x8 a0 = __builtin_bit_cast(bf16x8, (r32 < 16) ? wf : z4), a1 = __builtin_bit_cast(bf16x8, (r32 < 16) ? z4 : wf);
  f32x16 pq;
#pragma unroll
  for (int i = 0; i < 16; ++i) pq[i] = 0.f;
  SB0();
  __builtin_amdgcn_s_setprio(3);
  pq = MFMA(a0, qb0, pq);
  pq = MFMA(a1, qb1, pq);
  SB0();
  __builtin_amdgcn_s_setprio(PRIO_E);
#pragma unroll
  for (int j = 0; j < 8; ++j) pp[j] = cvtpk(pq[2 * j], pq[2 * j + 1]);
  asm volatile("" : "+v"(pp[0]), "+v"(pp[1]), "+v"(pp[2]), "+v"(pp[3]), "+v"(pp[4]), "+v"(pp[5]), "+v"(pp[6]), "+v"(pp[7]));
}
DI void feat_soft(const unsigned* pp, bf16x8& fp, bf16x8& fm) {
  float pv[8];
#pragma unroll
  for (int j = 0; j < 4; ++j) { pv[2 * j] = bflo(pp[j]); pv[2 * j + 1] = bfhi(pp[j]); }
  float mx = fmaxf(fmaxf(fmaxf(pv[0], pv[1]), pv[2]), fmaxf(fmaxf(pv[3], pv[4]), pv[5])), mn = fminf(fminf(fminf(pv[0], pv[1]), pv[2]), fminf(fminf(pv[3], pv[4]), pv[5]));
  mx = fmaxf(fmaxf(mx, pv[6]), pv[7]); mn = fminf(fminf(mn, pv[6]), pv[7]);
  mx = rmax32(mx); mn = rmin32(mn);
  const float L2E = 1.4426950408889634f, mxl = nv(-mx * L2E), mnl = nv(mn * L2E);
  float ep[8], em[8], sp = 0.f, sm = 0.f;
#pragma unroll
  for (int j = 0; j < 8; ++j) {
    ep[j] = __builtin_amdgcn_exp2f(nv(fmaf(pv[j], L2E, mxl))); sp = nv(sp + ep[j]);
    em[j] = __builtin_amdgcn_exp2f(nv(fmaf(pv[j], -L2E, mnl))); sm = nv(sm + em[j]);
  }
  sp = nv(rsum32(sp)); sm = nv(rsum32(sm));
  const float rp = __builtin_amdgcn_rcpf(sp), rm = __builtin_amdgcn_rcpf(sm);
#pragma unroll
  for (int j = 0; j < 8; ++j) { ep[j] = nv(ep[j] * rp); em[j] = nv(em[j] * rm); }
  fp = pack8(ep); fm = pack8(em);
  asm volatile("" : "+v"(fp), "+v"(fm));
}
#define WT_STORE 1
DI void st16(void* p, u32x4 v) {
  asm volatile("global_store_dwordx4 %0, %1, off sc1\n\ts_nop 1" :: "v"(p), "v"(v) : "memory");
}
DI void epi_g(const f32x16& acc, unsigned (&sg)[8]) {
#pragma unroll
  for (int i = 0; i < 16; i += 2) {
    const unsigned u = cvtpk(acc[i], acc[i + 1]);
    sg[i >> 1] = cvtpk(nv(silu_f(bflo(u))), nv(silu_f(bfhi(u))));
  }
  asm volatile("" : "+v"(sg[0]), "+v"(sg[1]), "+v"(sg[2]), "+v"(sg[3]), "+v"(sg[4]), "+v"(sg[5]), "+v"(sg[6]), "+v"(sg[7]));
}
DI void epi_u(const f32x16& acc, const unsigned (&sg)[8], char* dst) {
  float hv[16];
#pragma unroll
  for (int i = 0; i < 16; i += 2) { const unsigned u = cvtpk(acc[i], acc[i + 1]); hv[i] = nv(bflo(sg[i >> 1]) * bflo(u)); hv[i + 1] = nv(bfhi(sg[i >> 1]) * bfhi(u)); }
  st16(dst, __builtin_bit_cast(u32x4, pack8(hv))); st16(dst + 1024, __builtin_bit_cast(u32x4, pack8(hv + 8)));
}
DI void epi_u_calc(const f32x16& acc, const unsigned (&sg)[8], u32x4& h0, u32x4& h1) {
  float hv[16];
#pragma unroll
  for (int i = 0; i < 16; i += 2) { const unsigned u = cvtpk(acc[i], acc[i + 1]); hv[i] = nv(bflo(sg[i >> 1]) * bflo(u)); hv[i + 1] = nv(bfhi(sg[i >> 1]) * bfhi(u)); }
  h0 = __builtin_bit_cast(u32x4, pack8(hv)); h1 = __builtin_bit_cast(u32x4, pack8(hv + 8));
  asm volatile("" : "+v"(h0), "+v"(h1));
}
DI void o_head(u64 (&g2)[8], const char* p2r, unsigned tag, const bf16x8& fp, const bf16x8& fm, char* dst) {
  for (unsigned spin = 0; spin < SPIN_MAX; ++spin) {
    bool ok = true;
#pragma unroll
    for (int j = 0; j < 8; ++j) ok &= (unsigned)(g2[j] >> 32) == tag;
    if (__all(ok)) break;
    __builtin_amdgcn_s_sleep(4);
#pragma unroll
    for (int j = 0; j < 8; ++j) g2[j] = ld_gran(p2r + j * 512);
  }
  __builtin_amdgcn_s_setprio(3);
  const u32x4 f0 = {(unsigned)g2[0], (unsigned)g2[1], (unsigned)g2[2], (unsigned)g2[3]};
  const u32x4 f1 = {(unsigned)g2[4], (unsigned)g2[5], (unsigned)g2[6], (unsigned)g2[7]};
  f32x16 o;
#pragma unroll
  for (int q = 0; q < 16; ++q) o[q] = 0.f;
  o = MFMA(__builtin_bit_cast(bf16x8, f0), fp, o);
  o = MFMA(__builtin_bit_cast(bf16x8, f1), fm, o);
  const float rd = __builtin_amdgcn_rcpf(bf16r(bf16r(o[8]) + 1e-6f));
  float ov[8];
#pragma unroll
  for (int j = 0; j < 8; j += 2) { const unsigned u = cvtpk(o[j], o[j + 1]); ov[j] = nv(bflo(u) * rd); ov[j + 1] = nv(bfhi(u) * rd); }
  __builtin_amdgcn_s_setprio(PRIO_E);
  st16(dst, __builtin_bit_cast(u32x4, pack8(ov)));
}
#define PHASE_END(p) do { STAMP(2 + 2 * (p)); SB0(); BAR(); SB0(); STAMP(3 + 2 * (p)); } while (0)

__global__ __launch_bounds__(512, 2) void k1_kernel(const u16* __restrict__ x, const u16* __restrict__ wqkv, const u16* __restrict__ wphi,
                                                    const u16* __restrict__ wo, const u16* __restrict__ w1, const u16* __restrict__ w3,
                                                    const u16* __restrict__ w2, char* __restrict__ ws) {
  __shared__ __attribute__((aligned(16))) char smem[K1_LDS];
  const int tid = threadIdx.x, lane = tid & 63;
  const int wave = __builtin_amdgcn_readfirstlane(tid >> 6), grp = wave >> 2, wr = wave & 3;
  const int b = blockIdx.x, xcd = b & 7, idx = b >> 3;
  const int mt = (xcd & 1) * 8 + (idx & 7), ng = (xcd >> 1) * 4 + (idx >> 3);
  const int r32 = lane & 31, h = lane >> 5;
  const unsigned lds0 = (unsigned)(uintptr_t)smem;
  unsigned dummy0 = 0;
  if (wave == 0) {
    unsigned long long pc; unsigned len;
    asm volatile("s_getpc_b64 %0\n.Lk1_pc:\n\ts_mov_b32 %1, .Lk2_end-.Lk1_pc" : "=s"(pc), "=s"(len));
    const unsigned off = (lane * 8 + (idx & 7)) * 128;
    if (off < len) asm volatile("global_load_dword %0, %1, %2" : "+v"(dummy0) : "v"(off), "s"(pc) : "memory");
  }
  unsigned dummy1 = 0;
#define XTOUCH 0
  if (XTOUCH && grp == 0) asm volatile("global_load_dword %0, %1, %2" : "+v"(dummy1) : "v"(lane * 128), "s"((const char*)(x + (size_t)(mt * 128 + (idx >> 3) * 32 + wr * 8) * D_)) : "memory");
  const unsigned reg = lds0 + SLOT + (wr * 2 + grp) * 16384;
  {
    const char* xb = (const char*)(x + (size_t)(mt * 128 + wr * 32 + grp * 16) * D_);
    const int rr = lane >> 5, qd = (lane >> 3) & 3, pos = lane & 7;
#pragma unroll
    for (int cc = 0; cc < 2; ++cc)
#pragma unroll
      for (int i = 0; i < 8; ++i)
        glds16s(rr * 1024 + qd * 256 + (pos << 4), xb + i * 2048 + cc * 128, reg + cc * 8192 + i * 1024);
  }
  const int g0 = ng < 11 ? 3 * ng : 33 + 2 * (ng - 11);
  const bool full = ng < 11;
  const char* wK = (const char*)(wqkv + 512 + 32 * ng);
  const char* wG = (const char*)(w1 + 32 * g0);
  const char* wU = (const char*)(w3 + 32 * g0);
  const unsigned voffA = (lane >> 2) * 3072 + (lane & 3) * 16, voffF = (lane >> 2) * 2752 + (lane & 3) * 16;
  unsigned dummy = 0, dummy2 = 0, tagv;
  u32x4 wf, wfb;
  {
    tagv = __hip_atomic_load((GAS unsigned*)(ws + WS_EP + ng * 128), __ATOMIC_RELAXED, __HIP_MEMORY_SCOPE_AGENT);
    const u16* wp = wphi + (size_t)(2 * ng + (r32 >> 4)) * 256 + (4 * h) * 16 + (lane & 15);
#pragma unroll
    for (int j = 0; j < 4; ++j) {
      const int k0 = 8 * (j >> 1) + 2 * (j & 1);
      asm volatile("global_load_ushort %0, %2, off offset:%3\n\tglobal_load_ushort %1, %2, off offset:%4" : "=&v"(wf[j]), "=&v"(wfb[j]) : "v"(wp), "n"(k0 * 32), "n"(k0 * 32 + 32) : "memory");
    }
    if (grp) {
      const char* g = wK + (size_t)wr * 128 * 3072;
#pragma unroll
      for (int i = 0; i < 8; ++i) glds16s(voffA, g + i * 16 * 3072, lds0 + wr * 8192 + i * 1024);
      const int trow = (idx & 7) * 64 + lane;
      const int e = (wr == 3 && !full) ? 0 : wr - 1;
      const char* p = wr == 0 ? wK + 1024 + (size_t)trow * 3072 : wG + 64 * e + (size_t)trow * 2752;
      const char* p2 = wr == 0 ? wK - 1024 + (size_t)trow * 3072 : wU + 64 * e + (size_t)trow * 2752;
      asm volatile("global_load_dword %0, %1, off" : "+v"(dummy) : "v"(p) : "memory");
      asm volatile("global_load_dword %0, %1, off" : "+v"(dummy2) : "v"(p2) : "memory");
    }
  }
  bf16x8 xf[32];
  unsigned tag;
  {
    const int r16 = lane & 15, qd = lane >> 4;
    const unsigned rb = reg + (r16 >> 1) * 1024 + (r16 & 1) * 512 + qd * 128 + ((r16 & 7) << 4);
    u32x4 xr[16];
    float q0 = 0.f, q1 = 0.f;
    f32x4 sum4 = {0.f, 0.f, 0.f, 0.f};
    const u32x4 ones = {0x3f803f80u, 0x3f803f80u, 0x3f803f80u, 0x3f803f80u};
    if (grp) WAITV(27); else WAITV(17);
#pragma unroll
    for (int q = 0; q < 8; ++q) xr[q] = *(const LAS u32x4*)(uintptr_t)(rb ^ (q << 4));
#pragma unroll
    for (int q = 0; q < 8; ++q)
#pragma unroll
      for (int e = 0; e < 4; e += 2) {
        if (e == 0) sum4 = MFMA16(ones, xr[q], sum4);
        q0 = dot2(xr[q][e], xr[q][e], q0); q1 = dot2(xr[q][e + 1], xr[q][e + 1], q1);
      }
    asm volatile("" : "+v"(sum4), "+v"(q0), "+v"(q1));
    if (grp) WAITV(19); else WAITV(9);
#pragma unroll
    for (int q = 0; q < 8; ++q) xr[8 + q] = *(const LAS u32x4*)(uintptr_t)((rb ^ (q << 4)) + 8192);
#pragma unroll
    for (int q = 8; q < 16; ++q)
#pragma unroll
      for (int e = 0; e < 4; e += 2) {
        if (e == 0) sum4 = MFMA16(ones, xr[q], sum4);
        q0 = dot2(xr[q][e], xr[q][e], q0); q1 = dot2(xr[q][e + 1], xr[q][e + 1], q1);
      }
    const float mean = sum4[0] * (1.0f / 512.0f);
    const float var = fmaxf(rsum32(rsum16(q0 + q1)) * (1.0f / 512.0f) - mean * mean, 0.f);
    const float nmb = -bf16r(mean);
    const float rs = 1.0f / bf16r(sqrtf(bf16r(bf16r(var) + 1e-5f)));
    const unsigned wb = reg + qd * 4096 + r16 * 16 + (r16 & 3) * 512 + ((r16 >> 2) & 1) * 256;
#pragma unroll
    for (int q = 0; q < 16; ++q) {
      u32x4 o;
      center4(xr[q], nmb);
#pragma unroll
      for (int e = 0; e < 4; ++e) o[e] = cvtpk(bflo(xr[q][e]) * rs, bfhi(xr[q][e]) * rs);
      *(LAS u32x4*)(uintptr_t)((wb ^ ((q & 3) * 512 + ((q >> 2) & 1) * 256)) + (q >> 3) * 2048) = o;
    }
    SB0();
    WAITV(0);
    asm volatile("" : "+v"(dummy), "+v"(dummy0), "+v"(dummy1), "+v"(dummy2), "+v"(wf), "+v"(wfb));
#pragma unroll
    for (int j = 0; j < 4; ++j) wf[j] |= wfb[j] << 16;
    tag = __builtin_amdgcn_readfirstlane(tagv) + 1u;
    asm volatile("" : "+s"(tag));
    BAR();
    const unsigned xb = lds0 + SLOT * (1 + wr) + (r32 >> 4) * 16384 + (h * 16 + (r32 & 15)) * 16;
#pragma unroll
    for (int ks = 0; ks < 32; ++ks) xf[ks] = *(const LAS bf16x8*)(uintptr_t)(xb + ks * 512);
    BAR();
  }
  STAMP(1);
  const unsigned voffP = (lane >> 3) * 2752 + (((lane & 7) ^ ((lane >> 2) & 4)) << 4);
  const size_t rowP = (size_t)wave * 64 * 2752;
  const int i16 = lane & 15, blk = (lane >> 4) & 1;
  const unsigned tr0 = lds0 + (32 * h + (i16 >> 2)) * 64 + 8 * (4 * blk + (i16 & 3));
  const unsigned trP = lds0 + (32 * h + (i16 >> 2)) * 128 + (((grp ^ (i16 >> 3)) & 1) << 6) + 8 * (4 * blk + (i16 & 3));
  const int mblk = mt * 4 + wr;
  const unsigned ldd = lds0 + wr * 8192;
  const size_t rowF = (size_t)wr * 128 * 2752, rowA = (size_t)wr * 128 * 3072;
  char* const hdst = ws + WS_AB + ((size_t)mblk * KS_W + 32 + 2 * g0) * 1024 + lane * 16;
  unsigned sg[8], pp[8];
  f32x16 acc, acc2;
  const int row = wr * 32 + r32, sw = (row >> 1) & 7;
  auto nofill = [](int) {};
  const unsigned ppx = lds0 + LDS_SCR + 24576 + wr * 1024 + lane * 16;
  const char* p2r = ws + WS_P2 + (size_t)(ng * 16 + (grp ? 0 : 8)) * 512 + lane * 8;
  char* const odst = ws + WS_AB + ((size_t)mblk * KS_W + 2 * ng + (grp ? 0 : 1)) * 1024 + lane * 16;
  u64 g2[8];
  bf16x8 fq, fn;
  if (grp == 0) {
#define QL0 16
    mfma_tile<64, QL0, 2>(acc, xf, tr0, true, wK - 1024 + rowA, 16 * 3072, voffA, ldd + 2 * SLOT, nofill);
    PHASE_END(0);
    feat_proj(acc, wf, r32, pp);
#pragma unroll
    for (int s = 0; s < 2; ++s) {
      if (s) {
#pragma unroll
        for (int i = 0; i < 4; ++i) glds16s(voffP, wG + rowP + i * 8 * 2752, lds0 + wave * 8192 + i * 1024);
      }
      bf16x8 fp, fm;
      feat_soft(pp + 4 * s, fp, fm);
      if (s) {
#pragma unroll
        for (int i = 4; i < 8; ++i) glds16s(voffP, wG + rowP + i * 8 * 2752, lds0 + wave * 8192 + i * 1024);
      }
      const unsigned pk = lds0 + LDS_SCR + s * 8192 + row * 64;
      const u32x4 up = __builtin_bit_cast(u32x4, fp), um = __builtin_bit_cast(u32x4, fm);
      u32x2 t;
      t[0] = up[0]; t[1] = up[1]; *(LAS u32x2*)(uintptr_t)(pk + 8 * ((h) ^ sw)) = t;
      t[0] = up[2]; t[1] = up[3]; *(LAS u32x2*)(uintptr_t)(pk + 8 * ((2 + h) ^ sw)) = t;
      t[0] = um[0]; t[1] = um[1]; *(LAS u32x2*)(uintptr_t)(pk + 8 * ((4 + h) ^ sw)) = t;
      t[0] = um[2]; t[1] = um[3]; *(LAS u32x2*)(uintptr_t)(pk + 8 * ((6 + h) ^ sw)) = t;
      if (s == 0) { SB0(); WAITV(0); }
      PHASE_END(1 + s);
    }
    unsigned d0 = 0, d1 = 0;
    const int tr = idx * 59 + (lane < 59 ? lane : 58);
    const char* tp = (tr < 512 ? (const char*)wo + (size_t)tr * 1024 : (const char*)w2 + (size_t)(tr - 512) * 1024) + wr * 256;
    if (TOUCHW == 1) asm volatile("global_load_dword %0, %1, off" : "+v"(d0) : "v"(tp) : "memory");
    if (full) mfma_tile<64>(acc2, xf, tr0 + 3 * SLOT, false, nullptr, 0, 0, 0, nofill);
    if (TOUCHW == 1) WAITV(1); else WAITV(0);
    PHASE_END(3);
    if (TOUCHW == 3) asm volatile("global_load_dword %0, %1, off" : "+v"(d0) : "v"(tp) : "memory");
    mfma_tile<128>(acc, xf, trP, true, wU + rowP, 8 * 2752, voffP, lds0 + 2 * SLOT + wave * 8192, nofill);
    PHASE_END(4);
    if (TOUCHW == 2) asm volatile("global_load_dword %0, %1, off" : "+v"(d0) : "v"(tp) : "memory");
    if (TOUCHW) asm volatile("global_load_dword %0, %1, off offset:128" : "+v"(d1) : "v"(tp) : "memory");
    epi_g(acc, sg);
    { const u32x4 t = *(const LAS u32x4*)(uintptr_t)ppx; pp[0] = t[0]; pp[1] = t[1]; pp[2] = t[2]; pp[3] = t[3]; }
    feat_soft(pp, fq, fn);
    if (TOUCHW == 2) WAITV(2); else if (TOUCHW) WAITV(1); else WAITV(0);
    PHASE_END(5);
    mfma_tile<128>(acc, xf, trP + 2 * SLOT, full, wU + 128 + rowF, 16 * 2752, voffF, ldd, nofill);
    PHASE_END(6);
    epi_u(acc, sg, hdst);
    if (full) epi_g(acc2, sg);
    WAITV(2);
    asm volatile("" :: "v"(d0), "v"(d1));
    PHASE_END(7);
#define G2L 12
    u32x4 h0, h1;
    if (full) {
      mfma_tile<64>(acc, xf, tr0, false, nullptr, 0, 0, 0, [&](int ks) { if (ks >= G2L && ks < G2L + 8) g2[ks - G2L] = ld_gran(p2r + (ks - G2L) * 512); });
      epi_u_calc(acc, sg, h0, h1);
    } else {
#pragma unroll
      for (int j = 0; j < 8; ++j) g2[j] = ld_gran(p2r + j * 512);
    }
    STAMP(18);
    o_head(g2, p2r, tag, fq, fn, odst);
    STAMP(19);
    if (full) { st16(hdst + 4096, h0); st16(hdst + 5120, h1); }
    STAMP(20);
  } else {
#pragma unroll
    for (int i = 0; i < 8; ++i) glds16s(voffA, wK + 1024 + rowA + i * 16 * 3072, ldd + SLOT + i * 1024);
    WAITV(0);
    PHASE_END(0);
    mfma_tile<64>(acc2, xf, tr0 + SLOT, full, wG + 128 + rowF, 16 * 2752, voffF, ldd + 3 * SLOT, nofill);
    PHASE_END(1);
    {
      const unsigned vi = lds0 + LDS_SCR + 16384 + row * 64;
      mfma_tile<64>(acc, xf, tr0 + 2 * SLOT, !PLATE, wG + rowP, 8 * 2752, voffP, lds0 + wave * 8192, [&](int ks) {
        if (ks >= 1 && ks <= 4) {
          const int a = ks - 1;
          u32x2 t; t[0] = cvtpk(acc2[4 * a], acc2[4 * a + 1]); t[1] = cvtpk(acc2[4 * a + 2], acc2[4 * a + 3]);
          *(LAS u32x2*)(uintptr_t)(vi + 8 * ((2 * a + h) ^ sw)) = t;
        }
      });
    }
    if (PLATE) WAITV(0); else WAITV(8);
    PHASE_END(2);
    if (PLATE) {
#pragma unroll
      for (int i = 0; i < 8; ++i) glds16s(voffP, wG + rowP + i * 8 * 2752, lds0 + wave * 8192 + i * 1024);
    }
    if (wr < 2) {
      f32x16 a2;
#pragma unroll
      for (int i = 0; i < 16; ++i) a2[i] = 0.f;
      const int rowq = 8 * h + (i16 >> 2);
      const int ca = 4 * blk + (lane & 3), cb = 4 * (blk ^ wr) + (lane & 3);
      const int sw0 = (rowq >> 1) & 7, sw1 = ((rowq + 4) >> 1) & 7;
      const unsigned pk = lds0 + LDS_SCR + wr * 8192 + rowq * 64, vi = lds0 + LDS_SCR + 16384 + rowq * 64;
      const unsigned pa0 = pk + 8 * (ca ^ sw0), pa1 = pk + 256 + 8 * (ca ^ sw1), pb0 = vi + 8 * (cb ^ sw0), pb1 = vi + 256 + 8 * (cb ^ sw1);
      __builtin_amdgcn_s_setprio(3);
#pragma unroll
      for (int ks = 0; ks < 8; ++ks) {
        bf16x8 af = trfrag(pa0 + ks * 1024, pa1 + ks * 1024);
        bf16x8 bfr = trfrag(pb0 + ks * 1024, pb1 + ks * 1024);
        if (blk) {
#pragma unroll
          for (int j = 0; j < 8; ++j) bfr[j] = (short)0x3f80;
        }
        a2 = MFMA(af, bfr, a2);
      }
      asm volatile("" : "+v"(a2));
      __builtin_amdgcn_s_setprio(PRIO_E);
      char* p1w = ws + WS_P1 + (((size_t)(ng * 16 + wr * 8) * 16 + mt) * 1024) + lane * 8;
#pragma unroll
      for (int i = 0; i < 16; ++i) st_gran(p1w + (size_t)(i >> 1) * 16384 + (i & 1) * 512, __float_as_uint(a2[i]), tag);
    }
    feat_proj(acc, wf, r32, pp);
    { const u32x4 t = {pp[4], pp[5], pp[6], pp[7]}; *(LAS u32x4*)(uintptr_t)ppx = t; }
    if (wr >= 2) feat_soft(pp, fq, fn);
    if (wr < 2) WAITV(16); else WAITV(0);
    PHASE_END(3);
#pragma unroll
    for (int i = 0; i < 4; ++i) glds16s(voffP, wU + rowP + i * 8 * 2752, lds0 + 2 * SLOT + wave * 8192 + i * 1024);
    if (wr < 2) feat_soft(pp, fq, fn);
#pragma unroll
    for (int i = 4; i < 8; ++i) glds16s(voffP, wU + rowP + i * 8 * 2752, lds0 + 2 * SLOT + wave * 8192 + i * 1024);
    PHASE_END(4);
    u64 g1[8];
    const char* p1r = ws + WS_P1 + ((size_t)(ng * 16 + mt) * 16 + h * 8) * 1024 + blk * 512 + (wr * 16 + i16) * 8;
    mfma_tile<128>(acc, xf, trP, false, nullptr, 0, 0, 0, [&](int ks) { if (ks >= 16 && ks < 24) g1[ks - 16] = ld_gran(p1r + (ks - 16) * 1024); });
    WAITV(8);
    PHASE_END(5);
    {
      for (unsigned spin = 0; spin < SPIN_MAX; ++spin) {
        bool ok = true;
#pragma unroll
        for (int j = 0; j < 8; ++j) ok &= (unsigned)(g1[j] >> 32) == tag;
        if (__all(ok)) break;
        __builtin_amdgcn_s_sleep(4);
#pragma unroll
        for (int j = 0; j < 8; ++j) g1[j] = ld_gran(p1r + j * 1024);
      }
      float a = 0.f;
#pragma unroll
      for (int j = 0; j < 8; ++j) a = nv(a + __uint_as_float((unsigned)g1[j]));
      a = rsum32(a);
      auto rr = __builtin_amdgcn_permlane16_swap(__float_as_uint(a), __float_as_uint(a), false, false);
      if (lane < 16) st_gran(ws + WS_P2 + (size_t)(ng * 16 + mt) * 512 + (wr * 16 + lane) * 8, cvtpk(__uint_as_float(rr[0]), __uint_as_float(rr[1])), tag);
      if (mt == 0 && wr == 0 && lane == 0) __hip_atomic_store((GAS unsigned*)(ws + WS_EP + ng * 128), tag, __ATOMIC_RELAXED, __HIP_MEMORY_SCOPE_AGENT);
      SB0();
      epi_g(acc, sg);
    }
    PHASE_END(6);
    mfma_tile<128>(acc, xf, trP + 2 * SLOT, false, nullptr, 0, 0, 0, nofill);
    PHASE_END(7);
#pragma unroll
    for (int j = 0; j < 8; ++j) g2[j] = ld_gran(p2r + j * 512);
    SB0();
    u32x4 h0, h1;
    epi_u_calc(acc, sg, h0, h1);
    STAMP(18);
    o_head(g2, p2r, tag, fq, fn, odst);
    STAMP(19);
    st16(hdst + 2048, h0); st16(hdst + 3072, h1);
    STAMP(20);
  }
  STAMPR(31);
  STAMP_FLUSH(0);
  asm volatile(".Lk1_end:");
}

constexpr int XROW = 68;
constexpr int P2 = 10;
template <int N> DI void waitv() { asm volatile("s_waitcnt vmcnt(%0)" :: "n"(N) : "memory"); }
__global__ __launch_bounds__(256, 1) void k2_kernel(const u16* __restrict__ x, const u16* __restrict__ wo, const u16* __restrict__ w2, const char* __restrict__ ws, u16* __restrict__ out) {
  __shared__ __attribute__((aligned(16))) char smem[4 * P2 * 4096];
  float* xch = (float*)smem;
  const int tid = threadIdx.x, lane = tid & 63;
  const int wave = __builtin_amdgcn_readfirstlane(tid >> 6);
  const int b = blockIdx.x, xcd = b & 7, idx = b >> 3;
  const int mt = (xcd & 1) * 16 + (xcd >> 1) * 4 + (idx & 3), nt = idx >> 2;
  const int r32 = lane & 31, h = lane >> 5;
  const int erow = mt * 64 + (tid >> 2), ecol = nt * 64 + (tid & 3) * 16;
  const u32x4 xa = *(const u32x4*)(x + (size_t)erow * D_ + ecol), xb = *(const u32x4*)(x + (size_t)erow * D_ + ecol + 8);
  f32x16 acc[2][2];
#pragma unroll
  for (int n = 0; n < 2; ++n)
#pragma unroll
    for (int m = 0; m < 2; ++m)
#pragma unroll
      for (int i = 0; i < 16; ++i) acc[n][m][i] = 0.f;
  const int k0 = wave == 0 ? 0 : 32 + (wave - 1) * 29;
  const int nk = wave == 0 ? 32 : (wave == 3 ? 28 : 29);
  const char* wsrc = (wave == 0 ? (const char*)wo : (const char*)w2 + (size_t)(k0 - 32) * 16384) + nt * 128;
  const char* ha = ws + WS_AB + ((size_t)(2 * mt) * KS_W + k0) * 1024;
  const char* hb = ha + (size_t)KS_W * 1024;
  constexpr int RS = P2 * 4096;
  const unsigned ring = (unsigned)(uintptr_t)smem + wave * RS;
  const unsigned lo = lane * 16, voffW = (lane >> 3) * 1024 + (((lane & 7) ^ ((lane >> 2) & 4)) << 4);
  const int i16 = lane & 15, blk = (lane >> 4) & 1;
  const unsigned tb0 = ring + 2048 + (4 * h + (i16 >> 2)) * 128 + (((i16 >> 3) & 1) << 6) + 8 * (4 * blk + (i16 & 3)), tb1 = tb0 ^ 64;
  auto issue = [&](int i) {
    const unsigned d = ring + (i % P2) * 4096;
    glds16s(lo, ha + i * 1024, d); glds16s(lo, hb + i * 1024, d + 1024);
    glds16s(voffW, wsrc + i * 16384, d + 2048); glds16s(voffW, wsrc + i * 16384 + 8192, d + 3072);
  };
  bf16x8 R[2][4];
  auto rd = [&](int i, bf16x8* d) {
    const int o = (i % P2) * 4096;
    d[0] = *(const LAS bf16x8*)(uintptr_t)(ring + lo + o); d[1] = *(const LAS bf16x8*)(uintptr_t)(ring + lo + o + 1024);
    d[2] = trfrag(tb0 + o, tb0 + o + 1024); d[3] = trfrag(tb1 + o, tb1 + o + 1024);
  };
#pragma unroll
  for (int i = 0; i < P2; ++i) issue(i);
  waitv<4 * (P2 - 1)>();
  rd(0, R[0]);
#pragma unroll
  for (int i = 0; i < 32; ++i) {
    if (i < 28 || i < nk) {
      if (i + 1 < 28 || i + 1 < nk) {
        if (i + 1 < 32) {
          constexpr int full_w = 4 * (P2 - 2);
          if (4 * (26 - i) >= full_w) waitv<full_w>();
          else if (i == 19) waitv<28>(); else if (i == 20) waitv<24>(); else if (i == 21) waitv<20>(); else if (i == 22) waitv<16>();
          else if (i == 23) waitv<12>(); else if (i == 24) waitv<8>(); else if (i == 25) waitv<4>(); else waitv<0>();
          rd(i + 1, R[(i + 1) & 1]);
        }
      }
      SB0();
      const bf16x8* d = R[i & 1];
      acc[0][0] = MFMA(d[2], d[0], acc[0][0]); acc[0][1] = MFMA(d[2], d[1], acc[0][1]);
      acc[1][0] = MFMA(d[3], d[0], acc[1][0]); acc[1][1] = MFMA(d[3], d[1], acc[1][1]);
      SB0();
      if (i + P2 < 28) issue(i + P2);
      else if (i + P2 < 32) { if (i + P2 < nk) issue(i + P2); }
      SB0();
    }
  }
  __syncthreads();
  STAMP(1);
  float* mine = xch + wave * 64 * XROW;
#pragma unroll
  for (int n = 0; n < 2; ++n)
#pragma unroll
    for (int m = 0; m < 2; ++m)
#pragma unroll
      for (int a = 0; a < 4; ++a) {
        f32x4 t = {acc[n][m][4 * a], acc[n][m][4 * a + 1], acc[n][m][4 * a + 2], acc[n][m][4 * a + 3]};
        *(f32x4*)(mine + (32 * m + r32) * XROW + 32 * n + 8 * a + 4 * h) = t;
      }
  __syncthreads();
  STAMP(2);
  const float* rp = xch + (tid >> 2) * XROW + (tid & 3) * 16;
  u32x4 res[2];
#pragma unroll
  for (int q = 0; q < 4; ++q) {
    const f32x4 s0 = *(const f32x4*)(rp + 4 * q), o0 = *(const f32x4*)(rp + 64 * XROW + 4 * q);
    const f32x4 o1 = *(const f32x4*)(rp + 128 * XROW + 4 * q), o2 = *(const f32x4*)(rp + 192 * XROW + 4 * q);
#pragma unroll
    for (int e = 0; e < 4; e += 2) {
      const unsigned su = cvtpk(s0[e], s0[e + 1]);
      const unsigned ou = cvtpk((o0[e] + o1[e]) + o2[e], (o0[e + 1] + o1[e + 1]) + o2[e + 1]);
      const unsigned gu = cvtpk(silu_f(bflo(ou)), silu_f(bfhi(ou)));
      const unsigned tu = cvtpk(bflo(su) * bflo(gu), bfhi(su) * bfhi(gu));
      const unsigned xw = (q < 2 ? xa : xb)[(q & 1) * 2 + (e >> 1)];
      res[q >> 1][(q & 1) * 2 + (e >> 1)] = cvtpk(bflo(xw) + bflo(tu), bfhi(xw) + bfhi(tu));
    }
  }
  *(u32x4*)(out + (size_t)erow * D_ + ecol) = res[0];
  *(u32x4*)(out + (size_t)erow * D_ + ecol + 8) = res[1];
  STAMP(3); STAMPR(31);
  asm volatile(".Lk2_end:");
}

extern "C" void kernel_launch(void* const* d_in, const int* in_sizes, int n_in, void* d_out, int out_size, void* d_ws, size_t ws_size,
                              hipStream_t stream) {
  const u16* x = (const u16*)d_in[0];
  const u16* wqkv = (const u16*)d_in[1];
  const u16* wphi = (const u16*)d_in[2];
  const u16* wo = (const u16*)d_in[3];
  const u16* w1 = (const u16*)d_in[5];
  const u16* w3 = (const u16*)d_in[6];
  const u16* w2 = (const u16*)d_in[7];
  char* ws = (char*)d_ws;
  k1_kernel<<<256, 512, 0, stream>>>(x, wqkv, wphi, wo, w1, w3, w2, ws);
  k2_kernel<<<256, 256, 0, stream>>>(x, wo, w2, ws, (u16*)d_out);
}
```
